```python
import torch

def _softmax(x, axis=-1):
    return torch.softmax(x, dim=axis)
def _gelu(x):
    return torch.nn.functional.gelu(x, approximate="tanh")
def _silu(x):
    return torch.nn.functional.silu(x)
def _sigmoid(x):
    return torch.sigmoid(x)
def _layernorm(x, axis=-1, eps=1e-5):
    m = x.mean(dim=axis, keepdim=True)
    v = x.var(dim=axis, keepdim=True, unbiased=False)
    return (x - m) / torch.sqrt(v + eps)
def _scatter_add(tgt, idx, src, axis=0):
    return tgt.clone().index_add_(axis, idx, src)
def _segment_sum(src, seg, n):
    out = torch.zeros((n,) + src.shape[1:], dtype=src.dtype)
    return out.index_add_(0, seg, src)
def _pad(x, pw):
    flat = tuple(v for pair in reversed(pw) for v in pair)
    return torch.nn.functional.pad(x, flat)

kernel_name = 'standalone__gqa_block_cached__near_ridge'

def setup_inputs():

    _w = (lambda a: (lambda t: t.to(torch.bfloat16) if t.is_floating_point() else t)(torch.as_tensor(a)))
    B, Sq, D = 8, 112, 576
    Hq, Hkv, Sk, Dh = 32, 16, 512, 128
    assert Hq % Hkv == 0, (Hq, Hkv)
    return {
        "x": _w((lambda *s: torch.randn(*s, dtype=torch.bfloat16) * 0.8)(B, Sq, D)),
        "k_cache": _w((lambda *s: torch.randn(*s, dtype=torch.bfloat16) * 0.8)(B, Hkv, Sk, Dh)),
        "v_cache": _w((lambda *s: torch.randn(*s, dtype=torch.bfloat16) * 0.05)(B, Hkv, Sk, Dh)),
        "wq": _w((lambda *s: torch.randn(*s, dtype=torch.bfloat16) * 0.8)(D, Hq * Dh) * (D ** -0.5)),
        "wo": _w((lambda *s: torch.randn(*s, dtype=torch.bfloat16) * 0.05)(Hq * Dh, D) * ((Hq * Dh) ** -0.5)),
    }

def reference(x, k_cache, v_cache, wq, wo):
    B, Sq, D = x.shape
    Hq, Dh = 32, 128
    Hkv = k_cache.shape[1]
    rep = Hq // Hkv
    q = (lambda x, *d: x.permute(*d))((x @ wq).reshape(B, Sq, Hq, Dh), 0, 2, 1, 3)
    k = (lambda x, n, axis: torch.repeat_interleave(x, n, dim=axis))(k_cache, rep, axis=1)
    v = (lambda x, n, axis: torch.repeat_interleave(x, n, dim=axis))(v_cache, rep, axis=1)
    scores = torch.einsum("bhqd,bhkd->bhqk", q, k) * (Dh ** -0.5)
    p = _softmax(scores, axis=-1)
    o = torch.einsum("bhqk,bhkd->bhqd", p, v)
    return (lambda x, *d: x.permute(*d))(o, 0, 2, 1, 3).reshape(B, Sq, Hq * Dh) @ wo
```

```cpp
#include <hip/hip_runtime.h>
#include <hip/hip_bf16.h>
#include <stdint.h>

typedef __attribute__((ext_vector_type(8))) __bf16 bf16x8;
typedef __attribute__((ext_vector_type(4))) __bf16 bf16x4;
typedef __attribute__((ext_vector_type(2))) __bf16 bf16x2;
typedef __attribute__((ext_vector_type(16))) float f32x16;
typedef __attribute__((ext_vector_type(4))) float f32x4;
typedef __attribute__((ext_vector_type(2))) float f32x2;
typedef __attribute__((ext_vector_type(4))) unsigned u32x4;
#define LDSP __attribute__((address_space(3)))
#define DI __device__ __forceinline__

constexpr int NB = 8, SQ = 112, DM = 576, HQ = 32, HKV = 16, SK = 512, DH = 128;
constexpr int MROWS = NB * SQ;
constexpr int KO = HQ * DH;

DI unsigned cvtpk(float lo, float hi) {
  f32x2 v = {lo, hi};
  bf16x2 b = __builtin_convertvector(v, bf16x2);
  return __builtin_bit_cast(unsigned, b);
}
DI void glds16(const void* sbase, unsigned voff, unsigned lds_dst) {
  unsigned keep;
  asm volatile("s_mov_b32 %0, m0\n\ts_mov_b32 m0, %3\n\ts_nop 0\n\tglobal_load_lds_dwordx4 %1, %2\n\ts_mov_b32 m0, %0"
               : "=&s"(keep) : "v"(voff), "s"(sbase), "s"(lds_dst) : "memory");
}
template <int N> DI void wait_vm() { asm volatile("s_waitcnt vmcnt(%0)" ::"i"(N) : "memory"); }
DI void bar() { asm volatile("s_waitcnt lgkmcnt(0)\n\ts_barrier" ::: "memory"); }
DI bf16x8 lds_b128(unsigned a) { return *(const LDSP bf16x8*)(uintptr_t)a; }
DI bf16x8 lds_tr2(unsigned a0, unsigned a1) {
  bf16x4 lo = __builtin_amdgcn_ds_read_tr16_b64_v4bf16((LDSP bf16x4*)(uintptr_t)a0);
  bf16x4 hi = __builtin_amdgcn_ds_read_tr16_b64_v4bf16((LDSP bf16x4*)(uintptr_t)a1);
  return __builtin_shufflevector(lo, hi, 0, 1, 2, 3, 4, 5, 6, 7);
}
DI bf16x8 pack8(const f32x16& x, int s) {
  u32x4 w = {cvtpk(x[8 * s], x[8 * s + 1]), cvtpk(x[8 * s + 2], x[8 * s + 3]), cvtpk(x[8 * s + 4], x[8 * s + 5]),
             cvtpk(x[8 * s + 6], x[8 * s + 7])};
  return __builtin_bit_cast(bf16x8, w);
}
#define MFMA(a, b, c) __builtin_amdgcn_mfma_f32_32x32x16_bf16((a), (b), (c), 0, 0, 0)

constexpr unsigned STG = 32768u;
constexpr unsigned MISC = 4u * STG;
constexpr int K1_LDS = 160 * 1024;

__global__ __launch_bounds__(512, 2) void k1_qattn(const __hip_bfloat16* __restrict__ x, const __hip_bfloat16* __restrict__ kc,
                                                    const __hip_bfloat16* __restrict__ vc, const __hip_bfloat16* __restrict__ wq,
                                                    __hip_bfloat16* __restrict__ o_ws) {
  extern __shared__ __attribute__((aligned(16))) char lds[];
  const unsigned L0 = (unsigned)(uintptr_t)(LDSP char*)lds;
  const int tid = threadIdx.x, lane = tid & 63;
  const int wid = __builtin_amdgcn_readfirstlane(tid >> 6);
  const int rg = wid & 3, kh = wid >> 2;
  const int l31 = lane & 31, h = lane >> 5;
  const int bid = blockIdx.x;
  const int hq = 4 * (bid & 7) + ((bid >> 3) & 3), b = bid >> 5;
  const int hkv = hq >> 1;

  unsigned vx[2], vw[2], vkv[4];
#pragma unroll
  for (int e = 0; e < 2; ++e) {
    const int i = 2 * wid + e;
    int row = 8 * i + (lane >> 3);
    const int c = (lane & 7) ^ ((row >> 1) & 7);
    row = row < SQ ? row : SQ - 1;
    vx[e] = (unsigned)(row * (DM * 2) + c * 16);
    const int rw = 4 * i + (lane >> 4);
    const int cw = (lane & 15) ^ (((rw & 3) << 2) | ((rw >> 2) & 3));
    vw[e] = (unsigned)(rw * (KO * 2) + cw * 16);
  }
#pragma unroll
  for (int e = 0; e < 4; ++e) {
    const int r = 4 * (4 * (wid & 3) + e) + (lane >> 4);
    const int c = (lane & 15) ^ (((r & 3) << 2) | ((r >> 2) & 3));
    vkv[e] = (unsigned)(r * 256 + c * 16);
  }
  const char* xb = (const char*)x + (size_t)b * SQ * DM * 2;
  const char* wb = (const char*)wq + (size_t)hq * DH * 2;
  const size_t kvo = ((size_t)(b * HKV + hkv) * SK + 256 * kh) * DH * 2;
  const char* kb = (const char*)kc + kvo;
  const char* vb = (const char*)vc + kvo;

  auto issue = [&](int t) {
    const unsigned sb = L0 + (unsigned)(t & 3) * STG;
    if (t < 9) {
#pragma unroll
      for (int e = 0; e < 2; ++e) glds16(xb + t * 128, vx[e], sb + (2 * wid + e) * 1024);
#pragma unroll
      for (int e = 0; e < 2; ++e) glds16(wb + (size_t)t * 64 * KO * 2, vw[e], sb + 16384 + (2 * wid + e) * 1024);
    } else if (t < 13) {
#pragma unroll
      for (int e = 0; e < 4; ++e) glds16(kb + (t - 9) * 16384, vkv[e], sb + wid * 4096 + e * 1024);
    } else if (t < 17) {
#pragma unroll
      for (int e = 0; e < 4; ++e) glds16(vb + (t - 13) * 16384, vkv[e], sb + wid * 4096 + e * 1024);
    }
  };

  issue(0); issue(1); issue(2);

  const int q4 = (lane & 15) >> 2, p4 = lane & 3, g16 = (lane >> 4) & 1;
  unsigned ax[4];
#pragma unroll
  for (int ks = 0; ks < 4; ++ks) ax[ks] = (unsigned)((32 * rg + l31) * 128 + (((2 * ks + h) ^ ((l31 >> 1) & 7)) * 16));
  unsigned aw[2][2];
#pragma unroll
  for (int dl = 0; dl < 2; ++dl)
#pragma unroll
    for (int t = 0; t < 2; ++t) {
      const int dt = 2 * kh + dl;
      const int k = 8 * h + 4 * t + q4;
      const int c = (4 * dt + 2 * g16 + (p4 & 1)) ^ ((q4 << 2) | ((2 * h + t) & 3));
      aw[dl][t] = (unsigned)(16384 + k * 256 + c * 16 + (p4 >> 1) * 8);
    }

  f32x16 qa[2] = {};
#pragma unroll
  for (int t = 0; t < 9; ++t) {
    wait_vm<8>(); bar();
    issue(t + 3);
    const unsigned sb = L0 + (unsigned)(t & 3) * STG;
#pragma unroll
    for (int ks = 0; ks < 4; ++ks) {
      const bf16x8 bf = lds_b128(sb + ax[ks]);
      const bf16x8 a0 = lds_tr2(sb + aw[0][0] + ks * 4096, sb + aw[0][1] + ks * 4096);
      const bf16x8 a1 = lds_tr2(sb + aw[1][0] + ks * 4096, sb + aw[1][1] + ks * 4096);
      qa[0] = MFMA(a0, bf, qa[0]);
      qa[1] = MFMA(a1, bf, qa[1]);
    }
  }
  bf16x8 qf[8];
  {
    bf16x8 own[4] = {pack8(qa[0], 0), pack8(qa[0], 1), pack8(qa[1], 0), pack8(qa[1], 1)};
    const unsigned qw = L0 + MISC + wid * 4096 + lane * 16;
#pragma unroll
    for (int f = 0; f < 4; ++f) *(LDSP bf16x8*)(uintptr_t)(qw + f * 1024) = own[f];
    wait_vm<8>(); bar();
    const unsigned qr = L0 + MISC + (wid ^ 4) * 4096 + lane * 16;
    if (kh == 0) {
#pragma unroll
      for (int f = 0; f < 4; ++f) { qf[f] = own[f]; qf[4 + f] = lds_b128(qr + f * 1024); }
    } else {
#pragma unroll
      for (int f = 0; f < 4; ++f) { qf[4 + f] = own[f]; qf[f] = lds_b128(qr + f * 1024); }
    }
  }

  unsigned ak[8];
#pragma unroll
  for (int ks = 0; ks < 8; ++ks)
    ak[ks] = (unsigned)(kh * 16384 + l31 * 256 + (((2 * ks + h) ^ (((l31 & 3) << 2) | ((l31 >> 2) & 3))) * 16));
  f32x16 s[8];
#pragma unroll
  for (int i = 0; i < 4; ++i) {
    const int t = 9 + i;
    if (i > 0) { wait_vm<8>(); bar(); }
    issue(t + 3);
    const unsigned sb = L0 + (unsigned)(t & 3) * STG;
#pragma unroll
    for (int kt = 0; kt < 2; ++kt) {
      f32x16 acc = {};
#pragma unroll
      for (int ks = 0; ks < 8; ++ks) acc = MFMA(lds_b128(sb + ak[ks] + kt * 8192), qf[ks], acc);
      s[2 * i + kt] = acc;
    }
  }
  float mx = s[0][0];
#pragma unroll
  for (int i = 0; i < 8; ++i)
#pragma unroll
    for (int r = 0; r < 16; ++r) mx = fmaxf(mx, s[i][r]);
  {
    auto rr = __builtin_amdgcn_permlane32_swap(__float_as_uint(mx), __float_as_uint(mx), false, false);
    mx = fmaxf(__uint_as_float(rr[0]), __uint_as_float(rr[1]));
  }
  const float CS = 0.08838834764831845f * 1.4426950408889634f;
  const float mb = mx * CS;
  float ls = 0.f;

  unsigned av[4][2];
#pragma unroll
  for (int dt = 0; dt < 4; ++dt)
#pragma unroll
    for (int t = 0; t < 2; ++t) {
      const int key = 8 * t + 4 * h + q4;
      const int c = (4 * dt + 2 * (p4 & 1) + g16) ^ ((q4 << 2) | ((2 * t + h) & 3));
      av[dt][t] = (unsigned)(kh * 16384 + key * 256 + c * 16 + (p4 >> 1) * 8);
    }
  f32x16 oa[4] = {};
#pragma unroll
  for (int i = 0; i < 4; ++i) {
    const int t = 13 + i;
    if (i < 2) wait_vm<8>(); else if (i == 2) wait_vm<4>(); else wait_vm<0>();
    bar();
    issue(t + 3);
    const unsigned sb = L0 + (unsigned)(t & 3) * STG;
#pragma unroll
    for (int kt = 0; kt < 2; ++kt) {
      f32x16 p = s[2 * i + kt];
#pragma unroll
      for (int r = 0; r < 16; ++r) { p[r] = __builtin_amdgcn_exp2f(fmaf(p[r], CS, -mb)); ls += p[r]; }
#pragma unroll
      for (int s2 = 0; s2 < 2; ++s2) {
        const bf16x8 pf = pack8(p, s2);
#pragma unroll
        for (int dt = 0; dt < 4; ++dt) {
          const unsigned o = sb + kt * 8192 + s2 * 4096;
          oa[dt] = MFMA(lds_tr2(o + av[dt][0], o + av[dt][1]), pf, oa[dt]);
        }
      }
    }
  }
  {
    auto rr = __builtin_amdgcn_permlane32_swap(__float_as_uint(ls), __float_as_uint(ls), false, false);
    ls = __uint_as_float(rr[0]) + __uint_as_float(rr[1]);
  }
  bar();
  {
    const unsigned ow = L0 + wid * 8192 + lane * 16;
    f32x16 keep[2], send[2];
    if (kh == 0) { keep[0] = oa[0]; keep[1] = oa[1]; send[0] = oa[2]; send[1] = oa[3]; }
    else         { keep[0] = oa[2]; keep[1] = oa[3]; send[0] = oa[0]; send[1] = oa[1]; }
#pragma unroll
    for (int dl = 0; dl < 2; ++dl) {
      const f32x16 v = send[dl];
#pragma unroll
      for (int g = 0; g < 4; ++g) {
        f32x4 w = {v[4 * g], v[4 * g + 1], v[4 * g + 2], v[4 * g + 3]};
        *(LDSP f32x4*)(uintptr_t)(ow + (dl * 4 + g) * 1024) = w;
      }
    }
    f32x2 ml = {mx, ls};
    *(LDSP f32x2*)(uintptr_t)(L0 + MISC + wid * 512 + lane * 8) = ml;
    bar();
    const f32x2 pml = *(const LDSP f32x2*)(uintptr_t)(L0 + MISC + (wid ^ 4) * 512 + lane * 8);
    const float mt = fmaxf(mx, pml[0]);
    const float f0 = __builtin_amdgcn_exp2f((mx - mt) * CS), f1 = __builtin_amdgcn_exp2f((pml[0] - mt) * CS);
    const float inv = 1.f / (f0 * ls + f1 * pml[1]);
    const float g0 = f0 * inv, g1 = f1 * inv;
    const unsigned orr = L0 + (wid ^ 4) * 8192 + lane * 16;
    const int qrow = 32 * rg + l31;
    char* orow = (char*)o_ws + ((size_t)(b * SQ + qrow) * KO + hq * DH) * 2 + kh * 128 + h * 32;
#pragma unroll
    for (int dl = 0; dl < 2; ++dl) {
      const f32x16 v = keep[dl];
      float r[16];
#pragma unroll
      for (int g = 0; g < 4; ++g) {
        const f32x4 w = *(const LDSP f32x4*)(uintptr_t)(orr + (dl * 4 + g) * 1024);
#pragma unroll
        for (int e = 0; e < 4; ++e) r[4 * g + e] = v[4 * g + e] * g0 + w[e] * g1;
      }
      if (qrow < SQ) {
        u32x4 w0 = {cvtpk(r[0], r[1]), cvtpk(r[2], r[3]), cvtpk(r[4], r[5]), cvtpk(r[6], r[7])};
        u32x4 w1 = {cvtpk(r[8], r[9]), cvtpk(r[10], r[11]), cvtpk(r[12], r[13]), cvtpk(r[14], r[15])};
        *(u32x4*)(orow + dl * 64) = w0;
        *(u32x4*)(orow + dl * 64 + 16) = w1;
      }
    }
  }
}

constexpr unsigned STG2 = 24576u;
constexpr int NST2 = 6;
constexpr int K2_LDS = NST2 * STG2;

__global__ __launch_bounds__(512, 2) void k2_oproj(const __hip_bfloat16* __restrict__ o_ws, const __hip_bfloat16* __restrict__ wo,
                                                    float* __restrict__ part) {
  extern __shared__ __attribute__((aligned(16))) char lds[];
  const unsigned L0 = (unsigned)(uintptr_t)(LDSP char*)lds;
  const int tid = threadIdx.x, lane = tid & 63;
  const int wid = __builtin_amdgcn_readfirstlane(tid >> 6);
  const int mt = wid & 3, kp = wid >> 2;
  const int l31 = lane & 31, h = lane >> 5;
  const int bid = blockIdx.x;
  const int xcd = bid & 7, idx = bid >> 3;
  const int kc = xcd >> 1;
  const int tile = (xcd & 1) ? 32 + idx : idx;
  if (tile >= 63 || (!(xcd & 1) && idx >= 32)) return;
  const int tm = tile / 9, tn = tile % 9;

  unsigned vo[2], vw;
#pragma unroll
  for (int e = 0; e < 2; ++e) {
    const int row = 8 * (2 * wid + e) + (lane >> 3);
    const int c = (lane & 7) ^ ((row >> 1) & 7);
    vo[e] = (unsigned)(row * (KO * 2) + c * 16);
  }
  {
    const int row = 8 * wid + (lane >> 3);
    const int c = (lane & 7) ^ (((row >> 1) & 1) << 2);
    vw = (unsigned)(row * (DM * 2) + c * 16);
  }
  const char* ob = (const char*)o_ws + ((size_t)tm * 128 * KO + kc * 1024) * 2;
  const char* wb = (const char*)wo + ((size_t)kc * 1024 * DM + tn * 64) * 2;
  auto issue = [&](int t) {
    const unsigned sb = L0 + (unsigned)(t % NST2) * STG2;
    glds16(ob + t * 128, vo[0], sb + (2 * wid) * 1024);
    glds16(ob + t * 128, vo[1], sb + (2 * wid + 1) * 1024);
    glds16(wb + (size_t)t * 64 * DM * 2, vw, sb + 16384 + wid * 1024);
  };
#pragma unroll
  for (int t = 0; t < NST2 - 1; ++t) issue(t);

  const int q4 = (lane & 15) >> 2, p4 = lane & 3, g16 = (lane >> 4) & 1;
  unsigned ab[2], aw[2][2];
#pragma unroll
  for (int j = 0; j < 2; ++j) ab[j] = (unsigned)((32 * mt + l31) * 128 + (((2 * (2 * kp + j) + h) ^ ((l31 >> 1) & 7)) * 16));
#pragma unroll
  for (int nt = 0; nt < 2; ++nt)
#pragma unroll
    for (int t = 0; t < 2; ++t) {
      const int k = 32 * kp + 8 * h + 4 * t + q4;
      const int c = (4 * nt + 2 * (p4 & 1) + g16) ^ (((q4 >> 1) & 1) << 2);
      aw[nt][t] = (unsigned)(16384 + k * 128 + c * 16 + (p4 >> 1) * 8);
    }
  f32x16 acc[2] = {};
#pragma unroll
  for (int t = 0; t < 16; ++t) {
    if (t <= 11) wait_vm<12>(); else if (t == 12) wait_vm<9>(); else if (t == 13) wait_vm<6>(); else if (t == 14) wait_vm<3>(); else wait_vm<0>();
    bar();
    if (t + NST2 - 1 < 16) issue(t + NST2 - 1);
    const unsigned sb = L0 + (unsigned)(t % NST2) * STG2;
#pragma unroll
    for (int j = 0; j < 2; ++j) {
      const bf16x8 bf = lds_b128(sb + ab[j]);
      acc[0] = MFMA(lds_tr2(sb + aw[0][0] + j * 2048, sb + aw[0][1] + j * 2048), bf, acc[0]);
      acc[1] = MFMA(lds_tr2(sb + aw[1][0] + j * 2048, sb + aw[1][1] + j * 2048), bf, acc[1]);
    }
  }
  bar();
  {
    f32x16 v, a;
    if (kp == 0) { a = acc[0]; v = acc[1]; } else { a = acc[1]; v = acc[0]; }
    const unsigned ow = L0 + wid * 4096 + lane * 16;
#pragma unroll
    for (int g = 0; g < 4; ++g) {
      f32x4 w = {v[4 * g], v[4 * g + 1], v[4 * g + 2], v[4 * g + 3]};
      *(LDSP f32x4*)(uintptr_t)(ow + g * 1024) = w;
    }
    bar();
    const unsigned orr = L0 + (wid ^ 4) * 4096 + lane * 16;
    float* dst = part + ((size_t)kc * MROWS + tm * 128 + 32 * mt + l31) * DM + tn * 64 + kp * 32 + 16 * h;
#pragma unroll
    for (int g = 0; g < 4; ++g) {
      const f32x4 w = *(const LDSP f32x4*)(uintptr_t)(orr + g * 1024);
      f32x4 r = {a[4 * g] + w[0], a[4 * g + 1] + w[1], a[4 * g + 2] + w[2], a[4 * g + 3] + w[3]};
      *(f32x4*)(dst + 4 * g) = r;
    }
  }
}

__global__ __launch_bounds__(256) void k3_reduce(const float* __restrict__ part, __hip_bfloat16* __restrict__ out) {
  const int i = (blockIdx.x * 256 + threadIdx.x) * 8;
  if (i >= MROWS * DM) return;
  float r[8] = {};
#pragma unroll
  for (int kc = 0; kc < 4; ++kc) {
    const f32x4 a = *(const f32x4*)(part + (size_t)kc * MROWS * DM + i);
    const f32x4 c = *(const f32x4*)(part + (size_t)kc * MROWS * DM + i + 4);
#pragma unroll
    for (int e = 0; e < 4; ++e) { r[e] += a[e]; r[4 + e] += c[e]; }
  }
  u32x4 w = {cvtpk(r[0], r[1]), cvtpk(r[2], r[3]), cvtpk(r[4], r[5]), cvtpk(r[6], r[7])};
  *(u32x4*)(out + i) = w;
}

extern "C" void kernel_launch(void* const* d_in, const int* in_sizes, int n_in, void* d_out, int out_size, void* d_ws,
                              size_t ws_size, hipStream_t stream) {
  const __hip_bfloat16* x = (const __hip_bfloat16*)d_in[0];
  const __hip_bfloat16* kc = (const __hip_bfloat16*)d_in[1];
  const __hip_bfloat16* vc = (const __hip_bfloat16*)d_in[2];
  const __hip_bfloat16* wq = (const __hip_bfloat16*)d_in[3];
  const __hip_bfloat16* wo = (const __hip_bfloat16*)d_in[4];
  __hip_bfloat16* o_ws = (__hip_bfloat16*)d_ws;
  float* part = (float*)((char*)d_ws + (size_t)8 * 1024 * 1024);
  hipFuncSetAttribute((const void*)k1_qattn, hipFuncAttributeMaxDynamicSharedMemorySize, K1_LDS);
  hipFuncSetAttribute((const void*)k2_oproj, hipFuncAttributeMaxDynamicSharedMemorySize, K2_LDS);
  hipLaunchKernelGGL(k1_qattn, dim3(256), dim3(512), K1_LDS, stream, x, kc, vc, wq, o_ws);
  hipLaunchKernelGGL(k2_oproj, dim3(256), dim3(512), K2_LDS, stream, o_ws, wo, part);
  hipLaunchKernelGGL(k3_reduce, dim3((MROWS * DM / 8 + 255) / 256), dim3(256), 0, stream, part, (__hip_bfloat16*)d_out);
}
```

```cpp
#include <hip/hip_runtime.h>
#include <hip/hip_bf16.h>
#include <stdint.h>

typedef __attribute__((ext_vector_type(8))) __bf16 bf16x8;
typedef __attribute__((ext_vector_type(4))) __bf16 bf16x4;
typedef __attribute__((ext_vector_type(2))) __bf16 bf16x2;
typedef __attribute__((ext_vector_type(16))) float f32x16;
typedef __attribute__((ext_vector_type(4))) float f32x4;
typedef __attribute__((ext_vector_type(2))) float f32x2;
typedef __attribute__((ext_vector_type(4))) unsigned u32x4;
#define LDSP __attribute__((address_space(3)))
#define DI __device__ __forceinline__

constexpr int NB = 8, SQ = 112, DM = 576, HQ = 32, HKV = 16, SK = 512, DH = 128;
constexpr int MROWS = NB * SQ;
constexpr int KO = HQ * DH;

DI unsigned cvtpk(float lo, float hi) {
  f32x2 v = {lo, hi};
  bf16x2 b = __builtin_convertvector(v, bf16x2);
  return __builtin_bit_cast(unsigned, b);
}
DI void glds16(const void* sbase, unsigned voff, unsigned lds_dst) {
  unsigned keep;
  asm volatile("s_mov_b32 %0, m0\n\ts_mov_b32 m0, %3\n\ts_nop 0\n\tglobal_load_lds_dwordx4 %1, %2\n\ts_mov_b32 m0, %0"
               : "=&s"(keep) : "v"(voff), "s"(sbase), "s"(lds_dst) : "memory");
}
template <int N> DI void wait_vm() { asm volatile("s_waitcnt vmcnt(%0)" ::"i"(N) : "memory"); }
DI void bar() { asm volatile("s_waitcnt lgkmcnt(0)\n\ts_barrier" ::: "memory"); }
DI bf16x8 lds_b128(unsigned a) { return *(const LDSP bf16x8*)(uintptr_t)a; }
DI bf16x8 lds_tr2(unsigned a0, unsigned a1) {
  bf16x4 lo = __builtin_amdgcn_ds_read_tr16_b64_v4bf16((LDSP bf16x4*)(uintptr_t)a0);
  bf16x4 hi = __builtin_amdgcn_ds_read_tr16_b64_v4bf16((LDSP bf16x4*)(uintptr_t)a1);
  return __builtin_shufflevector(lo, hi, 0, 1, 2, 3, 4, 5, 6, 7);
}
DI bf16x8 pack8(const f32x16& x, int s) {
  u32x4 w = {cvtpk(x[8 * s], x[8 * s + 1]), cvtpk(x[8 * s + 2], x[8 * s + 3]), cvtpk(x[8 * s + 4], x[8 * s + 5]),
             cvtpk(x[8 * s + 6], x[8 * s + 7])};
  return __builtin_bit_cast(bf16x8, w);
}
#define STAMP(i)
#define MFMA(a, b, c) __builtin_amdgcn_mfma_f32_32x32x16_bf16((a), (b), (c), 0, 0, 0)

constexpr unsigned STG = 32768u;
constexpr unsigned MISC = 4u * STG;
constexpr int K1_LDS = 160 * 1024;
#define SCHED() __builtin_amdgcn_sched_barrier(0)
DI void rawbar() { asm volatile("s_barrier" ::: "memory"); }

__global__ __launch_bounds__(512, 2) void k1_qattn(const __hip_bfloat16* __restrict__ x, const __hip_bfloat16* __restrict__ kc,
                                                    const __hip_bfloat16* __restrict__ vc, const __hip_bfloat16* __restrict__ wq,
                                                    __hip_bfloat16* __restrict__ o_ws) {
  extern __shared__ __attribute__((aligned(16))) char lds[];
  const int tid = threadIdx.x, lane = tid & 63;
  const int wid = __builtin_amdgcn_readfirstlane(tid >> 6);
  const int rg = wid & 3, kh = wid >> 2;
  const int l31 = lane & 31, h = lane >> 5;
  const int bid = blockIdx.x;
  const int hq = 4 * (bid & 7) + ((bid >> 3) & 3), b = bid >> 5;
  const int hkv = hq >> 1;

  unsigned vx[2], vw[2], vkv[4];
#pragma unroll
  for (int e = 0; e < 2; ++e) {
    const int i = 2 * wid + e;
    int row = 8 * i + (lane >> 3);
    const int c = (lane & 7) ^ ((row >> 1) & 7);
    row = row < SQ ? row : SQ - 1;
    vx[e] = (unsigned)(row * (DM * 2) + c * 16);
    const int rw = 4 * i + (lane >> 4);
    const int cw = (lane & 15) ^ (((rw & 3) << 2) | ((rw >> 2) & 3));
    vw[e] = (unsigned)(rw * (KO * 2) + cw * 16);
  }
#pragma unroll
  for (int e = 0; e < 4; ++e) {
    const int r = 4 * (4 * (wid & 3) + e) + (lane >> 4);
    const int c = (lane & 15) ^ (((r & 3) << 2) | ((r >> 2) & 3));
    vkv[e] = (unsigned)(r * 256 + c * 16);
  }
  const char* xb = (const char*)x + (size_t)b * SQ * DM * 2;
  const char* wb = (const char*)wq + (size_t)hq * DH * 2;
  const size_t kvo = ((size_t)(b * HKV + hkv) * SK + 256 * kh) * DH * 2;
  const char* kb = (const char*)kc + kvo;
  const char* vb = (const char*)vc + kvo;

  auto issue1 = [&](int t, int e) {
    const unsigned sb = (unsigned)(t & 3) * STG;
    if (t < 9) {
      if (e < 2) glds16(xb + t * 128, vx[e], sb + (2 * wid + e) * 1024);
      else       glds16(wb + (size_t)t * 64 * KO * 2, vw[e - 2], sb + 16384 + (2 * wid + e - 2) * 1024);
    } else if (t < 13) glds16(kb + (t - 9) * 16384, vkv[e], sb + wid * 4096 + e * 1024);
    else if (t < 17)   glds16(vb + (t - 13) * 16384, vkv[e], sb + wid * 4096 + e * 1024);
  };
  STAMP(0);
#pragma unroll
  for (int t = 0; t < 3; ++t)
#pragma unroll
    for (int e = 0; e < 4; ++e) issue1(t, e);

  const int q4 = (lane & 15) >> 2, p4 = lane & 3, g16 = (lane >> 4) & 1;
  unsigned ax[4];
#pragma unroll
  for (int ks = 0; ks < 4; ++ks) ax[ks] = (unsigned)((32 * rg + l31) * 128 + (((2 * ks + h) ^ ((l31 >> 1) & 7)) * 16));
  unsigned aw[2][2];
#pragma unroll
  for (int dl = 0; dl < 2; ++dl)
#pragma unroll
    for (int t = 0; t < 2; ++t) {
      const int dt = 2 * kh + dl;
      const int k = 8 * h + 4 * t + q4;
      const int c = (4 * dt + 2 * g16 + (p4 & 1)) ^ ((q4 << 2) | ((2 * h + t) & 3));
      aw[dl][t] = (unsigned)(16384 + k * 256 + c * 16 + (p4 >> 1) * 8);
    }
  unsigned ak[8];
#pragma unroll
  for (int ks = 0; ks < 8; ++ks)
    ak[ks] = (unsigned)(kh * 16384 + l31 * 256 + (((2 * ks + h) ^ (((l31 & 3) << 2) | ((l31 >> 2) & 3))) * 16));
  unsigned av[4][2];
#pragma unroll
  for (int dt = 0; dt < 4; ++dt)
#pragma unroll
    for (int t = 0; t < 2; ++t) {
      const int key = 8 * t + 4 * h + q4;
      const int c = (4 * dt + 2 * (p4 & 1) + g16) ^ ((q4 << 2) | ((2 * t + h) & 3));
      av[dt][t] = (unsigned)(kh * 16384 + key * 256 + c * 16 + (p4 >> 1) * 8);
    }

  bf16x8 f1[3][3];
  auto ld1 = [&](int G) {
    const unsigned sb = (unsigned)((G >> 2) & 3) * STG; const int ks = G & 3, r = G % 3;
    f1[r][0] = lds_b128(sb + ax[ks]);
    f1[r][1] = lds_tr2(sb + aw[0][0] + ks * 4096, sb + aw[0][1] + ks * 4096);
    f1[r][2] = lds_tr2(sb + aw[1][0] + ks * 4096, sb + aw[1][1] + ks * 4096);
  };
  bf16x8 f2[3][4];
  auto ld2 = [&](int G) {
    const unsigned sb = (unsigned)((9 + (G >> 2)) & 3) * STG + ((G >> 1) & 1) * 8192; const int r = G % 3;
#pragma unroll
    for (int j = 0; j < 4; ++j) f2[r][j] = lds_b128(sb + ak[4 * (G & 1) + j]);
  };
  auto ld3 = [&](int G) {
    const unsigned sb = (unsigned)((13 + (G >> 2)) & 3) * STG + ((G >> 1) & 1) * 8192 + (G & 1) * 4096; const int r = (G + 1) % 3;
#pragma unroll
    for (int dt = 0; dt < 4; ++dt) f2[r][dt] = lds_tr2(sb + av[dt][0], sb + av[dt][1]);
  };

  wait_vm<8>(); rawbar();
  ld1(0); ld1(1);
  SCHED();

  f32x16 qa[2] = {};
#pragma unroll
  for (int t = 0; t < 9; ++t) {
    wait_vm<4>(); rawbar();
    STAMP(1 + t);
#pragma unroll
    for (int ks = 0; ks < 4; ++ks) {
      const int G = 4 * t + ks;
      issue1(t + 3, ks);
      if (G + 2 < 36) ld1(G + 2); else ld2(G + 2 - 36);
      qa[0] = MFMA(f1[G % 3][1], f1[G % 3][0], qa[0]);
      qa[1] = MFMA(f1[G % 3][2], f1[G % 3][0], qa[1]);
      SCHED();
    }
  }
  bf16x8 qf[8];
  {
    bf16x8 own[4] = {pack8(qa[0], 0), pack8(qa[0], 1), pack8(qa[1], 0), pack8(qa[1], 1)};
    const unsigned qw = MISC + wid * 4096 + lane * 16;
#pragma unroll
    for (int f = 0; f < 4; ++f) *(LDSP bf16x8*)(uintptr_t)(qw + f * 1024) = own[f];
    wait_vm<4>(); bar();
    const unsigned qr = MISC + (wid ^ 4) * 4096 + lane * 16;
    if (kh == 0) {
#pragma unroll
      for (int f = 0; f < 4; ++f) { qf[f] = own[f]; qf[4 + f] = lds_b128(qr + f * 1024); }
    } else {
#pragma unroll
      for (int f = 0; f < 4; ++f) { qf[4 + f] = own[f]; qf[f] = lds_b128(qr + f * 1024); }
    }
  }
  SCHED();

  const float CS = 0.08838834764831845f * 1.4426950408889634f;
  f32x16 s[8];
  bf16x8 pf[16];
  float m1 = 0.f, mb = 0.f, ls0 = 0.f, ls1 = 0.f;
  auto tilemax = [&](int t0) {
    float a = fmaxf(s[t0][0], s[t0][1]), c = fmaxf(s[t0 + 1][0], s[t0 + 1][1]);
    float d = fmaxf(s[t0 + 2][0], s[t0 + 2][1]), e = fmaxf(s[t0 + 3][0], s[t0 + 3][1]);
#pragma unroll
    for (int r = 2; r < 16; r += 2) {
      a = fmaxf(fmaxf(a, s[t0][r]), s[t0][r + 1]); c = fmaxf(fmaxf(c, s[t0 + 1][r]), s[t0 + 1][r + 1]);
      d = fmaxf(fmaxf(d, s[t0 + 2][r]), s[t0 + 2][r + 1]); e = fmaxf(fmaxf(e, s[t0 + 3][r]), s[t0 + 3][r + 1]);
    }
    float m = fmaxf(fmaxf(a, c), fmaxf(d, e));
    auto rr = __builtin_amdgcn_permlane32_swap(__float_as_uint(m), __float_as_uint(m), false, false);
    return fmaxf(__uint_as_float(rr[0]), __uint_as_float(rr[1]));
  };
  auto softfrag = [&](int f) {
    float p[8];
#pragma unroll
    for (int j = 0; j < 8; ++j) p[j] = __builtin_amdgcn_exp2f(fmaf(s[f >> 1][8 * (f & 1) + j], CS, -mb));
    ls0 += (p[0] + p[1]) + (p[2] + p[3]); ls1 += (p[4] + p[5]) + (p[6] + p[7]);
    u32x4 w = {cvtpk(p[0], p[1]), cvtpk(p[2], p[3]), cvtpk(p[4], p[5]), cvtpk(p[6], p[7])};
    pf[f] = __builtin_bit_cast(bf16x8, w);
  };
#pragma unroll
  for (int G = 0; G < 16; ++G) {
    const int i = G >> 2, t = 9 + i, kt = (G >> 1) & 1;
    if ((G & 3) == 0) {
      if (i > 0) { wait_vm<4>(); rawbar(); }
      STAMP(1 + t);
    }
    issue1(t + 3, G & 3);
    if (G + 2 < 16) ld2(G + 2); else ld3(G + 2 - 16);
    f32x16 acc;
    if (G & 1) acc = s[2 * i + kt]; else acc = f32x16{};
#pragma unroll
    for (int j = 0; j < 4; ++j) acc = MFMA(f2[G % 3][j], qf[4 * (G & 1) + j], acc);
    s[2 * i + kt] = acc;
    if (G == 8) { m1 = tilemax(0); mb = m1 * CS; }
    if (G >= 8) softfrag(G - 8);
    SCHED();
  }

  f32x16 oa[4] = {};
  float mx = 0.f;
#pragma unroll
  for (int G = 0; G < 16; ++G) {
    const int i = G >> 2, t = 13 + i;
    if ((G & 3) == 0) {
      if (i < 2) wait_vm<4>(); else if (i == 2) wait_vm<0>();
      if (i < 3) rawbar();
      STAMP(1 + t);
    }
    issue1(t + 3, G & 3);
    if (G + 2 < 16) ld3(G + 2);
    if (G == 8) {
      const float al = __builtin_amdgcn_exp2f((m1 - mx) * CS);
#pragma unroll
      for (int dt = 0; dt < 4; ++dt)
#pragma unroll
        for (int r = 0; r < 16; ++r) oa[dt][r] *= al;
    }
#pragma unroll
    for (int dt = 0; dt < 4; ++dt) oa[dt] = MFMA(f2[(G + 1) % 3][dt], pf[G], oa[dt]);
    if (G == 0) {
      mx = fmaxf(m1, tilemax(4)); mb = mx * CS;
      const float al = __builtin_amdgcn_exp2f((m1 - mx) * CS);
      ls0 *= al; ls1 *= al;
    }
    if (G < 8) softfrag(8 + G);
    SCHED();
  }
  float ls = ls0 + ls1;
  {
    auto rr = __builtin_amdgcn_permlane32_swap(__float_as_uint(ls), __float_as_uint(ls), false, false);
    ls = __uint_as_float(rr[0]) + __uint_as_float(rr[1]);
  }
  bar();
  STAMP(18);
  {
    const unsigned ow = wid * 8192 + lane * 16;
    f32x16 keep[2], send[2];
    if (kh == 0) { keep[0] = oa[0]; keep[1] = oa[1]; send[0] = oa[2]; send[1] = oa[3]; }
    else         { keep[0] = oa[2]; keep[1] = oa[3]; send[0] = oa[0]; send[1] = oa[1]; }
#pragma unroll
    for (int dl = 0; dl < 2; ++dl) {
      const f32x16 v = send[dl];
#pragma unroll
      for (int g = 0; g < 4; ++g) {
        f32x4 w = {v[4 * g], v[4 * g + 1], v[4 * g + 2], v[4 * g + 3]};
        *(LDSP f32x4*)(uintptr_t)(ow + (dl * 4 + g) * 1024) = w;
      }
    }
    f32x2 ml = {mx, ls};
    *(LDSP f32x2*)(uintptr_t)(MISC + wid * 512 + lane * 8) = ml;
    bar();
    const f32x2 pml = *(const LDSP f32x2*)(uintptr_t)(MISC + (wid ^ 4) * 512 + lane * 8);
    const float mt = fmaxf(mx, pml[0]);
    const float f0 = __builtin_amdgcn_exp2f((mx - mt) * CS), f1_ = __builtin_amdgcn_exp2f((pml[0] - mt) * CS);
    const float inv = 1.f / (f0 * ls + f1_ * pml[1]);
    const float g0 = f0 * inv, g1 = f1_ * inv;
    const unsigned orr = (wid ^ 4) * 8192 + lane * 16;
    const int qrow = 32 * rg + l31;
    char* orow = (char*)o_ws + ((size_t)(b * SQ + qrow) * KO + hq * DH) * 2 + kh * 128 + h * 32;
#pragma unroll
    for (int dl = 0; dl < 2; ++dl) {
      const f32x16 v = keep[dl];
      float r[16];
#pragma unroll
      for (int g = 0; g < 4; ++g) {
        const f32x4 w = *(const LDSP f32x4*)(uintptr_t)(orr + (dl * 4 + g) * 1024);
#pragma unroll
        for (int e = 0; e < 4; ++e) r[4 * g + e] = v[4 * g + e] * g0 + w[e] * g1;
      }
      if (qrow < SQ) {
        u32x4 w0 = {cvtpk(r[0], r[1]), cvtpk(r[2], r[3]), cvtpk(r[4], r[5]), cvtpk(r[6], r[7])};
        u32x4 w1 = {cvtpk(r[8], r[9]), cvtpk(r[10], r[11]), cvtpk(r[12], r[13]), cvtpk(r[14], r[15])};
        *(u32x4*)(orow + dl * 64) = w0;
        *(u32x4*)(orow + dl * 64 + 16) = w1;
      }
    }
  }
  STAMP(19);
}

constexpr unsigned STG2 = 24576u;
constexpr int NST2 = 6;
constexpr int K2_LDS = NST2 * STG2;

__global__ __launch_bounds__(512, 2) void k2_oproj(const __hip_bfloat16* __restrict__ o_ws, const __hip_bfloat16* __restrict__ wo,
                                                    float* __restrict__ part) {
  extern __shared__ __attribute__((aligned(16))) char lds[];
  const unsigned L0 = (unsigned)(uintptr_t)(LDSP char*)lds;
  const int tid = threadIdx.x, lane = tid & 63;
  const int wid = __builtin_amdgcn_readfirstlane(tid >> 6);
  const int mt = wid & 3, kp = wid >> 2;
  const int l31 = lane & 31, h = lane >> 5;
  const int bid = blockIdx.x;
  const int xcd = bid & 7, idx = bid >> 3;
  const int kc = xcd >> 1;
  const int tile = (xcd & 1) ? 32 + idx : idx;
  if (tile >= 63 || (!(xcd & 1) && idx >= 32)) return;
  const int tm = tile / 9, tn = tile % 9;

  unsigned vo[2], vw;
#pragma unroll
  for (int e = 0; e < 2; ++e) {
    const int row = 8 * (2 * wid + e) + (lane >> 3);
    const int c = (lane & 7) ^ ((row >> 1) & 7);
    vo[e] = (unsigned)(row * (KO * 2) + c * 16);
  }
  {
    const int row = 8 * wid + (lane >> 3);
    const int c = (lane & 7) ^ (((row >> 1) & 1) << 2);
    vw = (unsigned)(row * (DM * 2) + c * 16);
  }
  const char* ob = (const char*)o_ws + ((size_t)tm * 128 * KO + kc * 1024) * 2;
  const char* wb = (const char*)wo + ((size_t)kc * 1024 * DM + tn * 64) * 2;
  auto issue = [&](int t) {
    const unsigned sb = L0 + (unsigned)(t % NST2) * STG2;
    glds16(ob + t * 128, vo[0], sb + (2 * wid) * 1024);
    glds16(ob + t * 128, vo[1], sb + (2 * wid + 1) * 1024);
    glds16(wb + (size_t)t * 64 * DM * 2, vw, sb + 16384 + wid * 1024);
  };
#pragma unroll
  for (int t = 0; t < NST2 - 1; ++t) issue(t);

  const int q4 = (lane & 15) >> 2, p4 = lane & 3, g16 = (lane >> 4) & 1;
  unsigned ab[2], aw[2][2];
#pragma unroll
  for (int j = 0; j < 2; ++j) ab[j] = (unsigned)((32 * mt + l31) * 128 + (((2 * (2 * kp + j) + h) ^ ((l31 >> 1) & 7)) * 16));
#pragma unroll
  for (int nt = 0; nt < 2; ++nt)
#pragma unroll
    for (int t = 0; t < 2; ++t) {
      const int k = 32 * kp + 8 * h + 4 * t + q4;
      const int c = (4 * nt + 2 * (p4 & 1) + g16) ^ (((q4 >> 1) & 1) << 2);
      aw[nt][t] = (unsigned)(16384 + k * 128 + c * 16 + (p4 >> 1) * 8);
    }
  f32x16 acc[2] = {};
#pragma unroll
  for (int t = 0; t < 16; ++t) {
    if (t <= 11) wait_vm<12>(); else if (t == 12) wait_vm<9>(); else if (t == 13) wait_vm<6>(); else if (t == 14) wait_vm<3>(); else wait_vm<0>();
    bar();
    if (t + NST2 - 1 < 16) issue(t + NST2 - 1);
    const unsigned sb = L0 + (unsigned)(t % NST2) * STG2;
#pragma unroll
    for (int j = 0; j < 2; ++j) {
      const bf16x8 bf = lds_b128(sb + ab[j]);
      acc[0] = MFMA(lds_tr2(sb + aw[0][0] + j * 2048, sb + aw[0][1] + j * 2048), bf, acc[0]);
      acc[1] = MFMA(lds_tr2(sb + aw[1][0] + j * 2048, sb + aw[1][1] + j * 2048), bf, acc[1]);
    }
  }
  bar();
  {
    f32x16 v, a;
    if (kp == 0) { a = acc[0]; v = acc[1]; } else { a = acc[1]; v = acc[0]; }
    const unsigned ow = L0 + wid * 4096 + lane * 16;
#pragma unroll
    for (int g = 0; g < 4; ++g) {
      f32x4 w = {v[4 * g], v[4 * g + 1], v[4 * g + 2], v[4 * g + 3]};
      *(LDSP f32x4*)(uintptr_t)(ow + g * 1024) = w;
    }
    bar();
    const unsigned orr = L0 + (wid ^ 4) * 4096 + lane * 16;
    float* dst = part + ((size_t)kc * MROWS + tm * 128 + 32 * mt + l31) * DM + tn * 64 + kp * 32 + 16 * h;
#pragma unroll
    for (int g = 0; g < 4; ++g) {
      const f32x4 w = *(const LDSP f32x4*)(uintptr_t)(orr + g * 1024);
      f32x4 r = {a[4 * g] + w[0], a[4 * g + 1] + w[1], a[4 * g + 2] + w[2], a[4 * g + 3] + w[3]};
      *(f32x4*)(dst + 4 * g) = r;
    }
  }
}

__global__ __launch_bounds__(256) void k3_reduce(const float* __restrict__ part, __hip_bfloat16* __restrict__ out) {
  const int i = (blockIdx.x * 256 + threadIdx.x) * 8;
  if (i >= MROWS * DM) return;
  float r[8] = {};
#pragma unroll
  for (int kc = 0; kc < 4; ++kc) {
    const f32x4 a = *(const f32x4*)(part + (size_t)kc * MROWS * DM + i);
    const f32x4 c = *(const f32x4*)(part + (size_t)kc * MROWS * DM + i + 4);
#pragma unroll
    for (int e = 0; e < 4; ++e) { r[e] += a[e]; r[4 + e] += c[e]; }
  }
  u32x4 w = {cvtpk(r[0], r[1]), cvtpk(r[2], r[3]), cvtpk(r[4], r[5]), cvtpk(r[6], r[7])};
  *(u32x4*)(out + i) = w;
}

extern "C" void kernel_launch(void* const* d_in, const int* in_sizes, int n_in, void* d_out, int out_size, void* d_ws,
                              size_t ws_size, hipStream_t stream) {
  const __hip_bfloat16* x = (const __hip_bfloat16*)d_in[0];
  const __hip_bfloat16* kc = (const __hip_bfloat16*)d_in[1];
  const __hip_bfloat16* vc = (const __hip_bfloat16*)d_in[2];
  const __hip_bfloat16* wq = (const __hip_bfloat16*)d_in[3];
  const __hip_bfloat16* wo = (const __hip_bfloat16*)d_in[4];
  __hip_bfloat16* o_ws = (__hip_bfloat16*)d_ws;
  float* part = (float*)((char*)d_ws + (size_t)8 * 1024 * 1024);
  hipFuncSetAttribute((const void*)k1_qattn, hipFuncAttributeMaxDynamicSharedMemorySize, K1_LDS);
  hipFuncSetAttribute((const void*)k2_oproj, hipFuncAttributeMaxDynamicSharedMemorySize, K2_LDS);
  hipLaunchKernelGGL(k1_qattn, dim3(256), dim3(512), K1_LDS, stream, x, kc, vc, wq, o_ws);
  hipLaunchKernelGGL(k2_oproj, dim3(256), dim3(512), K2_LDS, stream, o_ws, wo, part);
  hipLaunchKernelGGL(k3_reduce, dim3((MROWS * DM / 8 + 255) / 256), dim3(256), 0, stream, part, (__hip_bfloat16*)d_out);
}
```

```cpp
#include <hip/hip_runtime.h>
#include <hip/hip_bf16.h>
#include <stdint.h>

typedef __attribute__((ext_vector_type(8))) __bf16 bf16x8;
typedef __attribute__((ext_vector_type(4))) __bf16 bf16x4;
typedef __attribute__((ext_vector_type(2))) __bf16 bf16x2;
typedef __attribute__((ext_vector_type(16))) float f32x16;
typedef __attribute__((ext_vector_type(4))) float f32x4;
typedef __attribute__((ext_vector_type(2))) float f32x2;
typedef __attribute__((ext_vector_type(4))) unsigned u32x4;
#define LDSP __attribute__((address_space(3)))
#define DI __device__ __forceinline__

constexpr int NB = 8, SQ = 112, DM = 576, HQ = 32, HKV = 16, SK = 512, DH = 128;
constexpr int MROWS = NB * SQ;
constexpr int KO = HQ * DH;

DI unsigned cvtpk(float lo, float hi) {
  f32x2 v = {lo, hi};
  bf16x2 b = __builtin_convertvector(v, bf16x2);
  return __builtin_bit_cast(unsigned, b);
}
DI void glds16(const void* sbase, unsigned voff, unsigned lds_dst) {
  unsigned keep;
  asm volatile("s_mov_b32 %0, m0\n\ts_mov_b32 m0, %3\n\ts_nop 0\n\tglobal_load_lds_dwordx4 %1, %2\n\ts_mov_b32 m0, %0"
               : "=&s"(keep) : "v"(voff), "s"(sbase), "s"(lds_dst) : "memory");
}
DI void touch(const void* sbase, unsigned voff, unsigned lds_dst) {
  unsigned keep;
  asm volatile("s_mov_b32 %0, m0\n\ts_mov_b32 m0, %3\n\ts_nop 0\n\tglobal_load_lds_dword %1, %2\n\ts_mov_b32 m0, %0"
               : "=&s"(keep) : "v"(voff), "s"(sbase), "s"(lds_dst) : "memory");
}
DI void glds16m(const void* sbase, unsigned voff, unsigned lds_dst, unsigned long long mask) {
  unsigned keep;
  asm volatile("s_mov_b32 %0, m0\n\ts_mov_b32 m0, %3\n\ts_mov_b64 exec, %4\n\tglobal_load_lds_dwordx4 %1, %2\n\ts_mov_b64 exec, -1\n\ts_mov_b32 m0, %0"
               : "=&s"(keep) : "v"(voff), "s"(sbase), "s"(lds_dst), "s"(mask) : "memory");
}
DI void touchm(const void* sbase, unsigned voff, unsigned lds_dst, unsigned long long mask) {
  unsigned keep;
  asm volatile("s_mov_b32 %0, m0\n\ts_mov_b32 m0, %3\n\ts_mov_b64 exec, %4\n\tglobal_load_lds_dword %1, %2\n\ts_mov_b64 exec, -1\n\ts_mov_b32 m0, %0"
               : "=&s"(keep) : "v"(voff), "s"(sbase), "s"(lds_dst), "s"(mask) : "memory");
}
template <int N> DI void wait_vm_if(int role) {
  asm volatile("s_cmp_eq_u32 %0, 0\n\ts_cbranch_scc1 1f\n\ts_waitcnt vmcnt(%1)\n1:" ::"s"(role), "i"(N) : "memory", "scc");
}
template <int N> DI void wait_vm() { asm volatile("s_waitcnt vmcnt(%0)" ::"i"(N) : "memory"); }
DI void bar() { asm volatile("s_waitcnt lgkmcnt(0)\n\ts_barrier" ::: "memory"); }
DI bf16x8 lds_b128(unsigned a) { return *(const LDSP bf16x8*)(uintptr_t)a; }
DI bf16x8 lds_tr2(unsigned a0, unsigned a1) {
  bf16x4 lo = __builtin_amdgcn_ds_read_tr16_b64_v4bf16((LDSP bf16x4*)(uintptr_t)a0);
  bf16x4 hi = __builtin_amdgcn_ds_read_tr16_b64_v4bf16((LDSP bf16x4*)(uintptr_t)a1);
  return __builtin_shufflevector(lo, hi, 0, 1, 2, 3, 4, 5, 6, 7);
}
DI bf16x8 pack8(const f32x16& x, int s) {
  u32x4 w = {cvtpk(x[8 * s], x[8 * s + 1]), cvtpk(x[8 * s + 2], x[8 * s + 3]), cvtpk(x[8 * s + 4], x[8 * s + 5]),
             cvtpk(x[8 * s + 6], x[8 * s + 7])};
  return __builtin_bit_cast(bf16x8, w);
}
#define STAMP(i)
#define MFMA(a, b, c) __builtin_amdgcn_mfma_f32_32x32x16_bf16((a), (b), (c), 0, 0, 0)

constexpr unsigned STG = 32768u;
constexpr unsigned MISC = 4u * STG;
constexpr int K1_LDS = 160 * 1024;
#define VPF_STEP 3
#define SCHED() __builtin_amdgcn_sched_barrier(0)
DI void rawbar() { asm volatile("s_barrier" ::: "memory"); }

__global__ __launch_bounds__(512, 2) void k1_qattn(const __hip_bfloat16* __restrict__ x, const __hip_bfloat16* __restrict__ kc,
                                                    const __hip_bfloat16* __restrict__ vc, const __hip_bfloat16* __restrict__ wq,
                                                    __hip_bfloat16* __restrict__ o_ws) {
  extern __shared__ __attribute__((aligned(16))) char lds[];
  const int tid = threadIdx.x, lane = tid & 63;
  const int wid = __builtin_amdgcn_readfirstlane(tid >> 6);
  const int rg = wid & 3, kh = wid >> 2;
  const int l31 = lane & 31, h = lane >> 5;
  const int bid = blockIdx.x;
  const int hq = 4 * (bid & 7) + ((bid >> 3) & 3), b = bid >> 5;
  const int hkv = hq >> 1;

  unsigned vx[2], vw[4], vkv[4];
#pragma unroll
  for (int e = 0; e < 2; ++e) {
    const int row = 8 * e + (lane >> 3);
    const int c = (lane & 7) ^ ((row >> 1) & 7);
    vx[e] = (unsigned)((lane >> 3) * (DM * 2) + c * 16);
  }
#pragma unroll
  for (int e = 0; e < 4; ++e) {
    const int cw = (lane & 15) ^ (((lane >> 4) << 2) | e);
    vw[e] = (unsigned)((lane >> 4) * (KO * 2) + cw * 16);
    vkv[e] = (unsigned)((lane >> 4) * 256 + cw * 16);
  }
  const char* xb = (const char*)x + (size_t)b * SQ * DM * 2;
  const char* wb = (const char*)wq + (size_t)hq * DH * 2;
  const size_t kvo = (size_t)(b * HKV + hkv) * SK * DH * 2;
  const char* kb = (const char*)kc + kvo;
  const char* vb = (const char*)vc + kvo;
  const int w3 = wid & 3;
  const int ldr = 1 - kh;
  const unsigned ldm32 = __builtin_amdgcn_readfirstlane(kh - 1), pfm32 = __builtin_amdgcn_readfirstlane(-kh);
  const unsigned long long ldm = ((unsigned long long)ldm32 << 32) | ldm32, pfm = ((unsigned long long)pfm32 << 32) | pfm32;

  auto issue1 = [&](int t, int e) {
    const unsigned sb = (unsigned)(t & 3) * STG;
    if (t < 9) {
      if (e < 4) {
        const int i = 4 * w3 + e, is = i >= 14 ? i - 2 : i;
        glds16m(xb + t * 128 + is * (8 * DM * 2), vx[e & 1], sb + i * 1024, ldm);
      } else {
        const int i = 4 * w3 + e - 4;
        glds16m(wb + (size_t)(t * 64 + 4 * i) * KO * 2, vw[e - 4], sb + 16384 + i * 1024, ldm);
      }
    } else if (t < 17) {
      const int j = 8 * w3 + e;
      const char* base = (t < 13 ? kb + (t - 9) * 16384 : vb + (t - 13) * 16384) + (j >> 4) * 65536 + (j & 15) * 1024;
      glds16m(base, vkv[e & 3], sb + j * 1024, ldm);
    }
  };
  constexpr unsigned DUMMY = MISC + 3 * 4096 + 256;
  const unsigned uk = (unsigned)((hq & 1) * 65536 + (w3 * 64 + lane) * 64);
  STAMP(0);
  if (kh == 0) {
#pragma unroll
    for (int t = 0; t < 3; ++t)
#pragma unroll
      for (int e = 0; e < 8; ++e) issue1(t, e);
  } else {
    const unsigned u = (unsigned)(((bid >> 3) * 4 + w3) * 64 + lane);
#pragma unroll
    for (int r = 0; r < 2; ++r) {
      const unsigned ux = u + r * 8192u;
      touch(x, (ux < 16128u ? ux : u) * 64u, DUMMY);
    }
#pragma unroll
    for (int r = 0; r < 2; ++r) {
      unsigned uw = u + r * 8192u; uw = uw < 9216u ? uw : u;
      touch((const char*)wq + (bid & 7) * 1024, (uw >> 4) * (KO * 2) + (uw & 15) * 64u, DUMMY);
    }
#pragma unroll
    for (int r = 0; r < 4; ++r) touch(kb + r * 16384, uk, DUMMY);
  }

  const int q4 = (lane & 15) >> 2, p4 = lane & 3, g16 = (lane >> 4) & 1;
  unsigned ax[4];
#pragma unroll
  for (int ks = 0; ks < 4; ++ks) ax[ks] = (unsigned)((32 * rg + l31) * 128 + (((2 * ks + h) ^ ((l31 >> 1) & 7)) * 16));
  unsigned aw[2][2];
#pragma unroll
  for (int dl = 0; dl < 2; ++dl)
#pragma unroll
    for (int t = 0; t < 2; ++t) {
      const int dt = 2 * kh + dl;
      const int k = 8 * h + 4 * t + q4;
      const int c = (4 * dt + 2 * g16 + (p4 & 1)) ^ ((q4 << 2) | ((2 * h + t) & 3));
      aw[dl][t] = (unsigned)(16384 + k * 256 + c * 16 + (p4 >> 1) * 8);
    }
  unsigned ak[8];
#pragma unroll
  for (int ks = 0; ks < 8; ++ks)
    ak[ks] = (unsigned)(kh * 16384 + l31 * 256 + (((2 * ks + h) ^ (((l31 & 3) << 2) | ((l31 >> 2) & 3))) * 16));
  unsigned av[4][2];
#pragma unroll
  for (int dt = 0; dt < 4; ++dt)
#pragma unroll
    for (int t = 0; t < 2; ++t) {
      const int key = 8 * t + 4 * h + q4;
      const int c = (4 * dt + 2 * (p4 & 1) + g16) ^ ((q4 << 2) | ((2 * t + h) & 3));
      av[dt][t] = (unsigned)(kh * 16384 + key * 256 + c * 16 + (p4 >> 1) * 8);
    }

  bf16x8 f1[3][3];
  auto ld1 = [&](int G) {
    const unsigned sb = (unsigned)((G >> 2) & 3) * STG; const int ks = G & 3, r = G % 3;
    f1[r][0] = lds_b128(sb + ax[ks]);
    f1[r][1] = lds_tr2(sb + aw[0][0] + ks * 4096, sb + aw[0][1] + ks * 4096);
    f1[r][2] = lds_tr2(sb + aw[1][0] + ks * 4096, sb + aw[1][1] + ks * 4096);
  };
  bf16x8 f2[3][4];
  auto ld2 = [&](int G) {
    const unsigned sb = (unsigned)((9 + (G >> 2)) & 3) * STG + ((G >> 1) & 1) * 8192; const int r = G % 3;
#pragma unroll
    for (int j = 0; j < 4; ++j) f2[r][j] = lds_b128(sb + ak[4 * (G & 1) + j]);
  };
  auto ld3 = [&](int G) {
    const unsigned sb = (unsigned)((13 + (G >> 2)) & 3) * STG + ((G >> 1) & 1) * 8192 + (G & 1) * 4096; const int r = (G + 1) % 3;
#pragma unroll
    for (int dt = 0; dt < 4; ++dt) f2[r][dt] = lds_tr2(sb + av[dt][0], sb + av[dt][1]);
  };

  wait_vm_if<16>(ldr);
  rawbar();
  ld1(0); ld1(1);
  SCHED();

  f32x16 qa[2] = {};
#pragma unroll
  for (int t = 0; t < 9; ++t) {
    wait_vm_if<8>(ldr);
    rawbar();
    STAMP(1 + t);
#pragma unroll
    for (int ks = 0; ks < 4; ++ks) {
      const int G = 4 * t + ks;
      issue1(t + 3, 2 * ks); issue1(t + 3, 2 * ks + 1);
      if (t == VPF_STEP) touchm(vb + ks * 16384, uk, DUMMY, pfm);
      if (G + 2 < 36) ld1(G + 2); else ld2(G + 2 - 36);
      qa[0] = MFMA(f1[G % 3][1], f1[G % 3][0], qa[0]);
      qa[1] = MFMA(f1[G % 3][2], f1[G % 3][0], qa[1]);
      SCHED();
    }
  }
  bf16x8 qf[8];
  {
    bf16x8 own[4] = {pack8(qa[0], 0), pack8(qa[0], 1), pack8(qa[1], 0), pack8(qa[1], 1)};
    const unsigned qw = MISC + wid * 4096 + lane * 16;
#pragma unroll
    for (int f = 0; f < 4; ++f) *(LDSP bf16x8*)(uintptr_t)(qw + f * 1024) = own[f];
    wait_vm_if<8>(ldr);
    bar();
    const unsigned qr = MISC + (wid ^ 4) * 4096 + lane * 16;
    if (kh == 0) {
#pragma unroll
      for (int f = 0; f < 4; ++f) { qf[f] = own[f]; qf[4 + f] = lds_b128(qr + f * 1024); }
    } else {
#pragma unroll
      for (int f = 0; f < 4; ++f) { qf[4 + f] = own[f]; qf[f] = lds_b128(qr + f * 1024); }
    }
  }
  SCHED();

  const float CS = 0.08838834764831845f * 1.4426950408889634f;
  f32x16 s[8];
  bf16x8 pf[16];
  float m1 = 0.f, mb = 0.f, ls0 = 0.f, ls1 = 0.f;
  auto tilemax = [&](int t0) {
    float a = fmaxf(s[t0][0], s[t0][1]), c = fmaxf(s[t0 + 1][0], s[t0 + 1][1]);
    float d = fmaxf(s[t0 + 2][0], s[t0 + 2][1]), e = fmaxf(s[t0 + 3][0], s[t0 + 3][1]);
#pragma unroll
    for (int r = 2; r < 16; r += 2) {
      a = fmaxf(fmaxf(a, s[t0][r]), s[t0][r + 1]); c = fmaxf(fmaxf(c, s[t0 + 1][r]), s[t0 + 1][r + 1]);
      d = fmaxf(fmaxf(d, s[t0 + 2][r]), s[t0 + 2][r + 1]); e = fmaxf(fmaxf(e, s[t0 + 3][r]), s[t0 + 3][r + 1]);
    }
    float m = fmaxf(fmaxf(a, c), fmaxf(d, e));
    auto rr = __builtin_amdgcn_permlane32_swap(__float_as_uint(m), __float_as_uint(m), false, false);
    return fmaxf(__uint_as_float(rr[0]), __uint_as_float(rr[1]));
  };
  auto softfrag = [&](int f) {
    float p[8];
#pragma unroll
    for (int j = 0; j < 8; ++j) p[j] = __builtin_amdgcn_exp2f(fmaf(s[f >> 1][8 * (f & 1) + j], CS, -mb));
    ls0 += (p[0] + p[1]) + (p[2] + p[3]); ls1 += (p[4] + p[5]) + (p[6] + p[7]);
    u32x4 w = {cvtpk(p[0], p[1]), cvtpk(p[2], p[3]), cvtpk(p[4], p[5]), cvtpk(p[6], p[7])};
    pf[f] = __builtin_bit_cast(bf16x8, w);
  };
#pragma unroll
  for (int G = 0; G < 16; ++G) {
    const int i = G >> 2, t = 9 + i, kt = (G >> 1) & 1;
    if ((G & 3) == 0) {
      if (i > 0) { wait_vm_if<8>(ldr); rawbar(); }
      STAMP(1 + t);
    }
    issue1(t + 3, 2 * (G & 3)); issue1(t + 3, 2 * (G & 3) + 1);
    if (G + 2 < 16) ld2(G + 2); else ld3(G + 2 - 16);
    f32x16 acc;
    if (G & 1) acc = s[2 * i + kt]; else acc = f32x16{};
#pragma unroll
    for (int j = 0; j < 4; ++j) acc = MFMA(f2[G % 3][j], qf[4 * (G & 1) + j], acc);
    s[2 * i + kt] = acc;
    if (G == 8) { m1 = tilemax(0); mb = m1 * CS; }
    if (G >= 8) softfrag(G - 8);
    SCHED();
  }

  f32x16 oa[4] = {};
  float mx = 0.f;
#pragma unroll
  for (int G = 0; G < 16; ++G) {
    const int i = G >> 2, t = 13 + i;
    if ((G & 3) == 0) {
      if (i < 2) wait_vm_if<8>(ldr); else if (i == 2) wait_vm_if<0>(ldr);
      if (i < 3) rawbar();
      STAMP(1 + t);
    }
    issue1(t + 3, 2 * (G & 3)); issue1(t + 3, 2 * (G & 3) + 1);
    if (G + 2 < 16) ld3(G + 2);
    if (G == 8) {
      const float al = __builtin_amdgcn_exp2f((m1 - mx) * CS);
#pragma unroll
      for (int dt = 0; dt < 4; ++dt)
#pragma unroll
        for (int r = 0; r < 16; ++r) oa[dt][r] *= al;
    }
#pragma unroll
    for (int dt = 0; dt < 4; ++dt) oa[dt] = MFMA(f2[(G + 1) % 3][dt], pf[G], oa[dt]);
    if (G == 0) {
      mx = fmaxf(m1, tilemax(4)); mb = mx * CS;
      const float al = __builtin_amdgcn_exp2f((m1 - mx) * CS);
      ls0 *= al; ls1 *= al;
    }
    if (G < 8) softfrag(8 + G);
    SCHED();
  }
  float ls = ls0 + ls1;
  {
    auto rr = __builtin_amdgcn_permlane32_swap(__float_as_uint(ls), __float_as_uint(ls), false, false);
    ls = __uint_as_float(rr[0]) + __uint_as_float(rr[1]);
  }
  bar();
  STAMP(18);
  {
    const unsigned ow = wid * 8192 + lane * 16;
    f32x16 keep[2], send[2];
    if (kh == 0) { keep[0] = oa[0]; keep[1] = oa[1]; send[0] = oa[2]; send[1] = oa[3]; }
    else         { keep[0] = oa[2]; keep[1] = oa[3]; send[0] = oa[0]; send[1] = oa[1]; }
#pragma unroll
    for (int dl = 0; dl < 2; ++dl) {
      const f32x16 v = send[dl];
#pragma unroll
      for (int g = 0; g < 4; ++g) {
        f32x4 w = {v[4 * g], v[4 * g + 1], v[4 * g + 2], v[4 * g + 3]};
        *(LDSP f32x4*)(uintptr_t)(ow + (dl * 4 + g) * 1024) = w;
      }
    }
    f32x2 ml = {mx, ls};
    *(LDSP f32x2*)(uintptr_t)(MISC + wid * 512 + lane * 8) = ml;
    bar();
    const f32x2 pml = *(const LDSP f32x2*)(uintptr_t)(MISC + (wid ^ 4) * 512 + lane * 8);
    const float mt = fmaxf(mx, pml[0]);
    const float f0 = __builtin_amdgcn_exp2f((mx - mt) * CS), f1_ = __builtin_amdgcn_exp2f((pml[0] - mt) * CS);
    const float inv = 1.f / (f0 * ls + f1_ * pml[1]);
    const float g0 = f0 * inv, g1 = f1_ * inv;
    const unsigned orr = (wid ^ 4) * 8192 + lane * 16;
    const int qrow = 32 * rg + l31;
    char* orow = (char*)o_ws + ((size_t)(b * SQ + qrow) * KO + hq * DH) * 2 + kh * 128 + h * 32;
#pragma unroll
    for (int dl = 0; dl < 2; ++dl) {
      const f32x16 v = keep[dl];
      float r[16];
#pragma unroll
      for (int g = 0; g < 4; ++g) {
        const f32x4 w = *(const LDSP f32x4*)(uintptr_t)(orr + (dl * 4 + g) * 1024);
#pragma unroll
        for (int e = 0; e < 4; ++e) r[4 * g + e] = v[4 * g + e] * g0 + w[e] * g1;
      }
      if (qrow < SQ) {
        u32x4 w0 = {cvtpk(r[0], r[1]), cvtpk(r[2], r[3]), cvtpk(r[4], r[5]), cvtpk(r[6], r[7])};
        u32x4 w1 = {cvtpk(r[8], r[9]), cvtpk(r[10], r[11]), cvtpk(r[12], r[13]), cvtpk(r[14], r[15])};
        *(u32x4*)(orow + dl * 64) = w0;
        *(u32x4*)(orow + dl * 64 + 16) = w1;
      }
    }
  }
  STAMP(19);
}

constexpr unsigned STG2 = 24576u;
constexpr int NST2 = 6;
constexpr int K2_LDS = NST2 * STG2;

__global__ __launch_bounds__(512, 2) void k2_oproj(const __hip_bfloat16* __restrict__ o_ws, const __hip_bfloat16* __restrict__ wo,
                                                    float* __restrict__ part) {
  extern __shared__ __attribute__((aligned(16))) char lds[];
  const unsigned L0 = (unsigned)(uintptr_t)(LDSP char*)lds;
  const int tid = threadIdx.x, lane = tid & 63;
  const int wid = __builtin_amdgcn_readfirstlane(tid >> 6);
  const int mt = wid & 3, kp = wid >> 2;
  const int l31 = lane & 31, h = lane >> 5;
  const int bid = blockIdx.x;
  const int xcd = bid & 7, idx = bid >> 3;
  const int kc = xcd >> 1;
  const int tile = (xcd & 1) ? 32 + idx : idx;
  if (tile >= 63 || (!(xcd & 1) && idx >= 32)) return;
  const int tm = tile / 9, tn = tile % 9;

  unsigned vo[2], vw;
#pragma unroll
  for (int e = 0; e < 2; ++e) {
    const int row = 8 * (2 * wid + e) + (lane >> 3);
    const int c = (lane & 7) ^ ((row >> 1) & 7);
    vo[e] = (unsigned)(row * (KO * 2) + c * 16);
  }
  {
    const int row = 8 * wid + (lane >> 3);
    const int c = (lane & 7) ^ (((row >> 1) & 1) << 2);
    vw = (unsigned)(row * (DM * 2) + c * 16);
  }
  const char* ob = (const char*)o_ws + ((size_t)tm * 128 * KO + kc * 1024) * 2;
  const char* wb = (const char*)wo + ((size_t)kc * 1024 * DM + tn * 64) * 2;
  auto issue = [&](int t) {
    const unsigned sb = L0 + (unsigned)(t % NST2) * STG2;
    glds16(ob + t * 128, vo[0], sb + (2 * wid) * 1024);
    glds16(ob + t * 128, vo[1], sb + (2 * wid + 1) * 1024);
    glds16(wb + (size_t)t * 64 * DM * 2, vw, sb + 16384 + wid * 1024);
  };
#pragma unroll
  for (int t = 0; t < NST2 - 1; ++t) issue(t);

  const int q4 = (lane & 15) >> 2, p4 = lane & 3, g16 = (lane >> 4) & 1;
  unsigned ab[2], aw[2][2];
#pragma unroll
  for (int j = 0; j < 2; ++j) ab[j] = (unsigned)((32 * mt + l31) * 128 + (((2 * (2 * kp + j) + h) ^ ((l31 >> 1) & 7)) * 16));
#pragma unroll
  for (int nt = 0; nt < 2; ++nt)
#pragma unroll
    for (int t = 0; t < 2; ++t) {
      const int k = 32 * kp + 8 * h + 4 * t + q4;
      const int c = (4 * nt + 2 * (p4 & 1) + g16) ^ (((q4 >> 1) & 1) << 2);
      aw[nt][t] = (unsigned)(16384 + k * 128 + c * 16 + (p4 >> 1) * 8);
    }
  f32x16 acc[2] = {};
#pragma unroll
  for (int t = 0; t < 16; ++t) {
    if (t <= 11) wait_vm<12>(); else if (t == 12) wait_vm<9>(); else if (t == 13) wait_vm<6>(); else if (t == 14) wait_vm<3>(); else wait_vm<0>();
    bar();
    if (t + NST2 - 1 < 16) issue(t + NST2 - 1);
    const unsigned sb = L0 + (unsigned)(t % NST2) * STG2;
#pragma unroll
    for (int j = 0; j < 2; ++j) {
      const bf16x8 bf = lds_b128(sb + ab[j]);
      acc[0] = MFMA(lds_tr2(sb + aw[0][0] + j * 2048, sb + aw[0][1] + j * 2048), bf, acc[0]);
      acc[1] = MFMA(lds_tr2(sb + aw[1][0] + j * 2048, sb + aw[1][1] + j * 2048), bf, acc[1]);
    }
  }
  bar();
  {
    f32x16 v, a;
    if (kp == 0) { a = acc[0]; v = acc[1]; } else { a = acc[1]; v = acc[0]; }
    const unsigned ow = L0 + wid * 4096 + lane * 16;
#pragma unroll
    for (int g = 0; g < 4; ++g) {
      f32x4 w = {v[4 * g], v[4 * g + 1], v[4 * g + 2], v[4 * g + 3]};
      *(LDSP f32x4*)(uintptr_t)(ow + g * 1024) = w;
    }
    bar();
    const unsigned orr = L0 + (wid ^ 4) * 4096 + lane * 16;
    float* dst = part + ((size_t)kc * MROWS + tm * 128 + 32 * mt + l31) * DM + tn * 64 + kp * 32 + 16 * h;
#pragma unroll
    for (int g = 0; g < 4; ++g) {
      const f32x4 w = *(const LDSP f32x4*)(uintptr_t)(orr + g * 1024);
      f32x4 r = {a[4 * g] + w[0], a[4 * g + 1] + w[1], a[4 * g + 2] + w[2], a[4 * g + 3] + w[3]};
      *(f32x4*)(dst + 4 * g) = r;
    }
  }
}

__global__ __launch_bounds__(256) void k3_reduce(const float* __restrict__ part, __hip_bfloat16* __restrict__ out) {
  const int i = (blockIdx.x * 256 + threadIdx.x) * 8;
  if (i >= MROWS * DM) return;
  float r[8] = {};
#pragma unroll
  for (int kc = 0; kc < 4; ++kc) {
    const f32x4 a = *(const f32x4*)(part + (size_t)kc * MROWS * DM + i);
    const f32x4 c = *(const f32x4*)(part + (size_t)kc * MROWS * DM + i + 4);
#pragma unroll
    for (int e = 0; e < 4; ++e) { r[e] += a[e]; r[4 + e] += c[e]; }
  }
  u32x4 w = {cvtpk(r[0], r[1]), cvtpk(r[2], r[3]), cvtpk(r[4], r[5]), cvtpk(r[6], r[7])};
  *(u32x4*)(out + i) = w;
}

extern "C" void kernel_launch(void* const* d_in, const int* in_sizes, int n_in, void* d_out, int out_size, void* d_ws,
                              size_t ws_size, hipStream_t stream) {
  const __hip_bfloat16* x = (const __hip_bfloat16*)d_in[0];
  const __hip_bfloat16* kc = (const __hip_bfloat16*)d_in[1];
  const __hip_bfloat16* vc = (const __hip_bfloat16*)d_in[2];
  const __hip_bfloat16* wq = (const __hip_bfloat16*)d_in[3];
  const __hip_bfloat16* wo = (const __hip_bfloat16*)d_in[4];
  __hip_bfloat16* o_ws = (__hip_bfloat16*)d_ws;
  float* part = (float*)((char*)d_ws + (size_t)8 * 1024 * 1024);
  hipFuncSetAttribute((const void*)k1_qattn, hipFuncAttributeMaxDynamicSharedMemorySize, K1_LDS);
  hipFuncSetAttribute((const void*)k2_oproj, hipFuncAttributeMaxDynamicSharedMemorySize, K2_LDS);
  hipLaunchKernelGGL(k1_qattn, dim3(256), dim3(512), K1_LDS, stream, x, kc, vc, wq, o_ws);
  hipLaunchKernelGGL(k2_oproj, dim3(256), dim3(512), K2_LDS, stream, o_ws, wo, part);
  hipLaunchKernelGGL(k3_reduce, dim3((MROWS * DM / 8 + 255) / 256), dim3(256), 0, stream, part, (__hip_bfloat16*)d_out);
}
```

```cpp
#include <hip/hip_runtime.h>
#include <hip/hip_bf16.h>
#include <stdint.h>

typedef __attribute__((ext_vector_type(8))) __bf16 bf16x8;
typedef __attribute__((ext_vector_type(4))) __bf16 bf16x4;
typedef __attribute__((ext_vector_type(2))) __bf16 bf16x2;
typedef __attribute__((ext_vector_type(16))) float f32x16;
typedef __attribute__((ext_vector_type(4))) float f32x4;
typedef __attribute__((ext_vector_type(2))) float f32x2;
typedef __attribute__((ext_vector_type(4))) unsigned u32x4;
#define LDSP __attribute__((address_space(3)))
#define DI __device__ __forceinline__

constexpr int NB = 8, SQ = 112, DM = 576, HQ = 32, HKV = 16, SK = 512, DH = 128;
constexpr int MROWS = NB * SQ;
constexpr int KO = HQ * DH;

DI unsigned cvtpk(float lo, float hi) {
  f32x2 v = {lo, hi};
  bf16x2 b = __builtin_convertvector(v, bf16x2);
  return __builtin_bit_cast(unsigned, b);
}
DI void glds16(const void* sbase, unsigned voff, unsigned lds_dst) {
  unsigned keep;
  asm volatile("s_mov_b32 %0, m0\n\ts_mov_b32 m0, %3\n\ts_nop 0\n\tglobal_load_lds_dwordx4 %1, %2\n\ts_mov_b32 m0, %0"
               : "=&s"(keep) : "v"(voff), "s"(sbase), "s"(lds_dst) : "memory");
}
DI void glds16nt(const void* sbase, unsigned voff, unsigned lds_dst) {
  unsigned keep;
  asm volatile("s_mov_b32 %0, m0\n\ts_mov_b32 m0, %3\n\ts_nop 0\n\tglobal_load_lds_dwordx4 %1, %2 nt\n\ts_mov_b32 m0, %0"
               : "=&s"(keep) : "v"(voff), "s"(sbase), "s"(lds_dst) : "memory");
}
#define GL_KV glds16nt
#define GL_XW glds16
#define GL_WO glds16
#define GL_O glds16
template <int N> DI void wait_vm() { asm volatile("s_waitcnt vmcnt(%0)" ::"i"(N) : "memory"); }
DI void bar() { asm volatile("s_waitcnt lgkmcnt(0)\n\ts_barrier" ::: "memory"); }
DI bf16x8 lds_b128(unsigned a) { return *(const LDSP bf16x8*)(uintptr_t)a; }
DI bf16x8 lds_tr2(unsigned a0, unsigned a1) {
  bf16x4 lo = __builtin_amdgcn_ds_read_tr16_b64_v4bf16((LDSP bf16x4*)(uintptr_t)a0);
  bf16x4 hi = __builtin_amdgcn_ds_read_tr16_b64_v4bf16((LDSP bf16x4*)(uintptr_t)a1);
  return __builtin_shufflevector(lo, hi, 0, 1, 2, 3, 4, 5, 6, 7);
}
DI bf16x8 pack8(const f32x16& x, int s) {
  u32x4 w = {cvtpk(x[8 * s], x[8 * s + 1]), cvtpk(x[8 * s + 2], x[8 * s + 3]), cvtpk(x[8 * s + 4], x[8 * s + 5]),
             cvtpk(x[8 * s + 6], x[8 * s + 7])};
  return __builtin_bit_cast(bf16x8, w);
}
#define STAMP(i)
#define CSTAMP(i)
#define STAMP_DECL
#define STAMP_FLUSH
#define STAMP2(i)
#define CSTAMP2(i)
#define STAMP_DECL2
#define STAMP_FLUSH2
#define MFMA(a, b, c) __builtin_amdgcn_mfma_f32_32x32x16_bf16((a), (b), (c), 0, 0, 0)

constexpr unsigned STG = 32768u;
constexpr unsigned MISC = 4u * STG;
constexpr int K1_LDS = 160 * 1024;
#define SCHED() __builtin_amdgcn_sched_barrier(0)
DI void rawbar() { asm volatile("s_barrier" ::: "memory"); }

__global__ __launch_bounds__(512, 2) void k1_qattn(const __hip_bfloat16* __restrict__ x, const __hip_bfloat16* __restrict__ kc,
                                                    const __hip_bfloat16* __restrict__ vc, const __hip_bfloat16* __restrict__ wq,
                                                    __hip_bfloat16* __restrict__ o_ws) {
  extern __shared__ __attribute__((aligned(16))) char lds[];
  const int tid = threadIdx.x, lane = tid & 63;
  const int wid = __builtin_amdgcn_readfirstlane(tid >> 6);
  const int rg = wid & 3, kh = wid >> 2;
  const int l31 = lane & 31, h = lane >> 5;
  const int bid = blockIdx.x;
  const int hq = 4 * (bid & 7) + ((bid >> 3) & 3), b = bid >> 5;
  const int hkv = hq >> 1;
  STAMP_DECL;

  unsigned vx[2], vw[2], vkv[4];
#pragma unroll
  for (int e = 0; e < 2; ++e) {
    const int i = 2 * wid + e;
    int row = 8 * i + (lane >> 3);
    const int c = (lane & 7) ^ ((row >> 1) & 7);
    row = row < SQ ? row : SQ - 1;
    vx[e] = (unsigned)(row * (DM * 2) + c * 16);
    const int rw = 4 * i + (lane >> 4);
    const int cw = (lane & 15) ^ (((rw & 3) << 2) | ((rw >> 2) & 3));
    vw[e] = (unsigned)(rw * (KO * 2) + cw * 16);
  }
#pragma unroll
  for (int e = 0; e < 4; ++e) {
    const int r = 4 * (4 * (wid & 3) + e) + (lane >> 4);
    const int c = (lane & 15) ^ (((r & 3) << 2) | ((r >> 2) & 3));
    vkv[e] = (unsigned)(r * 256 + c * 16);
  }
  const char* xb = (const char*)x + (size_t)b * SQ * DM * 2;
  const char* wb = (const char*)wq + (size_t)hq * DH * 2;
  const size_t kvo = ((size_t)(b * HKV + hkv) * SK + 256 * kh) * DH * 2;
  const char* kb = (const char*)kc + kvo;
  const char* vb = (const char*)vc + kvo;

  auto issue1 = [&](int t, int e) {
    const unsigned sb = (unsigned)(t & 3) * STG;
    if (t < 9) {
      if (e < 2) GL_XW(xb + t * 128, vx[e], sb + (2 * wid + e) * 1024);
      else       GL_XW(wb + (size_t)t * 64 * KO * 2, vw[e - 2], sb + 16384 + (2 * wid + e - 2) * 1024);
    } else if (t < 13) GL_KV(kb + (t - 9) * 16384, vkv[e], sb + wid * 4096 + e * 1024);
    else if (t < 17)   GL_KV(vb + (t - 13) * 16384, vkv[e], sb + wid * 4096 + e * 1024);
  };
  STAMP(0); CSTAMP(20);
#pragma unroll
  for (int t = 0; t < 3; ++t)
#pragma unroll
    for (int e = 0; e < 4; ++e) issue1(t, e);

  const int q4 = (lane & 15) >> 2, p4 = lane & 3, g16 = (lane >> 4) & 1;
  unsigned ax[4];
#pragma unroll
  for (int ks = 0; ks < 4; ++ks) ax[ks] = (unsigned)((32 * rg + l31) * 128 + (((2 * ks + h) ^ ((l31 >> 1) & 7)) * 16));
  unsigned aw[2][2];
#pragma unroll
  for (int dl = 0; dl < 2; ++dl)
#pragma unroll
    for (int t = 0; t < 2; ++t) {
      const int dt = 2 * kh + dl;
      const int k = 8 * h + 4 * t + q4;
      const int c = (4 * dt + 2 * g16 + (p4 & 1)) ^ ((q4 << 2) | ((2 * h + t) & 3));
      aw[dl][t] = (unsigned)(16384 + k * 256 + c * 16 + (p4 >> 1) * 8);
    }
  unsigned ak[8];
#pragma unroll
  for (int ks = 0; ks < 8; ++ks)
    ak[ks] = (unsigned)(kh * 16384 + l31 * 256 + (((2 * ks + h) ^ (((l31 & 3) << 2) | ((l31 >> 2) & 3))) * 16));
  unsigned av[4][2];
#pragma unroll
  for (int dt = 0; dt < 4; ++dt)
#pragma unroll
    for (int t = 0; t < 2; ++t) {
      const int key = 8 * t + 4 * h + q4;
      const int c = (4 * dt + 2 * (p4 & 1) + g16) ^ ((q4 << 2) | ((2 * t + h) & 3));
      av[dt][t] = (unsigned)(kh * 16384 + key * 256 + c * 16 + (p4 >> 1) * 8);
    }

  bf16x8 f1[3][3];
  auto ld1 = [&](int G) {
    const unsigned sb = (unsigned)((G >> 2) & 3) * STG; const int ks = G & 3, r = G % 3;
    f1[r][0] = lds_b128(sb + ax[ks]);
    f1[r][1] = lds_tr2(sb + aw[0][0] + ks * 4096, sb + aw[0][1] + ks * 4096);
    f1[r][2] = lds_tr2(sb + aw[1][0] + ks * 4096, sb + aw[1][1] + ks * 4096);
  };
  bf16x8 f2[3][4];
  auto ld2 = [&](int G) {
    const unsigned sb = (unsigned)((9 + (G >> 2)) & 3) * STG + ((G >> 1) & 1) * 8192; const int r = G % 3;
#pragma unroll
    for (int j = 0; j < 4; ++j) f2[r][j] = lds_b128(sb + ak[4 * (G & 1) + j]);
  };
  auto ld3 = [&](int G) {
    const unsigned sb = (unsigned)((13 + (G >> 2)) & 3) * STG + ((G >> 1) & 1) * 8192 + (G & 1) * 4096; const int r = (G + 1) % 3;
#pragma unroll
    for (int dt = 0; dt < 4; ++dt) f2[r][dt] = lds_tr2(sb + av[dt][0], sb + av[dt][1]);
  };

  wait_vm<8>(); rawbar();
  ld1(0); ld1(1);
  SCHED();

  f32x16 qa[2] = {};
#pragma unroll
  for (int t = 0; t < 9; ++t) {
    wait_vm<4>(); rawbar();
    STAMP(1 + t);
#pragma unroll
    for (int ks = 0; ks < 4; ++ks) {
      const int G = 4 * t + ks;
      issue1(t + 3, ks);
      if (G + 2 < 36) ld1(G + 2); else ld2(G + 2 - 36);
      qa[0] = MFMA(f1[G % 3][1], f1[G % 3][0], qa[0]);
      qa[1] = MFMA(f1[G % 3][2], f1[G % 3][0], qa[1]);
      SCHED();
    }
  }
  bf16x8 qf[8];
  {
    bf16x8 own[4] = {pack8(qa[0], 0), pack8(qa[0], 1), pack8(qa[1], 0), pack8(qa[1], 1)};
    const unsigned qw = MISC + wid * 4096 + lane * 16;
#pragma unroll
    for (int f = 0; f < 4; ++f) *(LDSP bf16x8*)(uintptr_t)(qw + f * 1024) = own[f];
    wait_vm<4>(); bar();
    const unsigned qr = MISC + (wid ^ 4) * 4096 + lane * 16;
    if (kh == 0) {
#pragma unroll
      for (int f = 0; f < 4; ++f) { qf[f] = own[f]; qf[4 + f] = lds_b128(qr + f * 1024); }
    } else {
#pragma unroll
      for (int f = 0; f < 4; ++f) { qf[4 + f] = own[f]; qf[f] = lds_b128(qr + f * 1024); }
    }
  }
  SCHED();
  CSTAMP(22);
  const float CS = 0.08838834764831845f * 1.4426950408889634f;
  f32x16 s[8];
  bf16x8 pf[16];
  float m1 = 0.f, mb = 0.f, ls0 = 0.f, ls1 = 0.f;
  auto tilemax = [&](int t0) {
    float a = fmaxf(s[t0][0], s[t0][1]), c = fmaxf(s[t0 + 1][0], s[t0 + 1][1]);
    float d = fmaxf(s[t0 + 2][0], s[t0 + 2][1]), e = fmaxf(s[t0 + 3][0], s[t0 + 3][1]);
#pragma unroll
    for (int r = 2; r < 16; r += 2) {
      a = fmaxf(fmaxf(a, s[t0][r]), s[t0][r + 1]); c = fmaxf(fmaxf(c, s[t0 + 1][r]), s[t0 + 1][r + 1]);
      d = fmaxf(fmaxf(d, s[t0 + 2][r]), s[t0 + 2][r + 1]); e = fmaxf(fmaxf(e, s[t0 + 3][r]), s[t0 + 3][r + 1]);
    }
    float m = fmaxf(fmaxf(a, c), fmaxf(d, e));
    auto rr = __builtin_amdgcn_permlane32_swap(__float_as_uint(m), __float_as_uint(m), false, false);
    return fmaxf(__uint_as_float(rr[0]), __uint_as_float(rr[1]));
  };
  auto softfrag = [&](int f) {
    float p[8];
#pragma unroll
    for (int j = 0; j < 8; ++j) p[j] = __builtin_amdgcn_exp2f(fmaf(s[f >> 1][8 * (f & 1) + j], CS, -mb));
    ls0 += (p[0] + p[1]) + (p[2] + p[3]); ls1 += (p[4] + p[5]) + (p[6] + p[7]);
    u32x4 w = {cvtpk(p[0], p[1]), cvtpk(p[2], p[3]), cvtpk(p[4], p[5]), cvtpk(p[6], p[7])};
    pf[f] = __builtin_bit_cast(bf16x8, w);
  };
#pragma unroll
  for (int G = 0; G < 16; ++G) {
    const int i = G >> 2, t = 9 + i, kt = (G >> 1) & 1;
    if ((G & 3) == 0) {
      if (i > 0) { wait_vm<4>(); rawbar(); }
      STAMP(1 + t);
    }
    issue1(t + 3, G & 3);
    if (G + 2 < 16) ld2(G + 2); else ld3(G + 2 - 16);
    f32x16 acc;
    if (G & 1) acc = s[2 * i + kt]; else acc = f32x16{};
#pragma unroll
    for (int j = 0; j < 4; ++j) acc = MFMA(f2[G % 3][j], qf[4 * (G & 1) + j], acc);
    s[2 * i + kt] = acc;
    if (G == 8) { m1 = tilemax(0); mb = m1 * CS; }
    if (G >= 8) softfrag(G - 8);
    SCHED();
  }

  f32x16 oa[4] = {};
  float mx = 0.f;
#pragma unroll
  for (int G = 0; G < 16; ++G) {
    const int i = G >> 2, t = 13 + i;
    if ((G & 3) == 0) {
      if (i < 2) wait_vm<4>(); else if (i == 2) wait_vm<0>();
      if (i < 3) rawbar();
      STAMP(1 + t);
    }
    issue1(t + 3, G & 3);
    if (G + 2 < 16) ld3(G + 2);
    if (G == 8) {
      const float al = __builtin_amdgcn_exp2f((m1 - mx) * CS);
#pragma unroll
      for (int dt = 0; dt < 4; ++dt)
#pragma unroll
        for (int r = 0; r < 16; ++r) oa[dt][r] *= al;
    }
#pragma unroll
    for (int dt = 0; dt < 4; ++dt) oa[dt] = MFMA(f2[(G + 1) % 3][dt], pf[G], oa[dt]);
    if (G == 0) {
      mx = fmaxf(m1, tilemax(4)); mb = mx * CS;
      const float al = __builtin_amdgcn_exp2f((m1 - mx) * CS);
      ls0 *= al; ls1 *= al;
    }
    if (G < 8) softfrag(8 + G);
    SCHED();
  }
  float ls = ls0 + ls1;
  {
    auto rr = __builtin_amdgcn_permlane32_swap(__float_as_uint(ls), __float_as_uint(ls), false, false);
    ls = __uint_as_float(rr[0]) + __uint_as_float(rr[1]);
  }
  bar();
  STAMP(18); CSTAMP(23);
  {
    const unsigned ow = wid * 8192 + lane * 16;
    f32x16 keep[2], send[2];
    if (kh == 0) { keep[0] = oa[0]; keep[1] = oa[1]; send[0] = oa[2]; send[1] = oa[3]; }
    else         { keep[0] = oa[2]; keep[1] = oa[3]; send[0] = oa[0]; send[1] = oa[1]; }
#pragma unroll
    for (int dl = 0; dl < 2; ++dl) {
      const f32x16 v = send[dl];
#pragma unroll
      for (int g = 0; g < 4; ++g) {
        f32x4 w = {v[4 * g], v[4 * g + 1], v[4 * g + 2], v[4 * g + 3]};
        *(LDSP f32x4*)(uintptr_t)(ow + (dl * 4 + g) * 1024) = w;
      }
    }
    f32x2 ml = {mx, ls};
    *(LDSP f32x2*)(uintptr_t)(MISC + wid * 512 + lane * 8) = ml;
    bar();
    const f32x2 pml = *(const LDSP f32x2*)(uintptr_t)(MISC + (wid ^ 4) * 512 + lane * 8);
    const float mt = fmaxf(mx, pml[0]);
    const float f0 = __builtin_amdgcn_exp2f((mx - mt) * CS), f1_ = __builtin_amdgcn_exp2f((pml[0] - mt) * CS);
    const float inv = 1.f / (f0 * ls + f1_ * pml[1]);
    const float g0 = f0 * inv, g1 = f1_ * inv;
    const unsigned orr = (wid ^ 4) * 8192 + lane * 16;
    const int qrow = 32 * rg + l31;
    char* orow = (char*)o_ws + ((size_t)hq * MROWS + b * SQ + qrow) * (DH * 2) + kh * 128 + h * 32;
#pragma unroll
    for (int dl = 0; dl < 2; ++dl) {
      const f32x16 v = keep[dl];
      float r[16];
#pragma unroll
      for (int g = 0; g < 4; ++g) {
        const f32x4 w = *(const LDSP f32x4*)(uintptr_t)(orr + (dl * 4 + g) * 1024);
#pragma unroll
        for (int e = 0; e < 4; ++e) r[4 * g + e] = v[4 * g + e] * g0 + w[e] * g1;
      }
      if (qrow < SQ) {
        u32x4 w0 = {cvtpk(r[0], r[1]), cvtpk(r[2], r[3]), cvtpk(r[4], r[5]), cvtpk(r[6], r[7])};
        u32x4 w1 = {cvtpk(r[8], r[9]), cvtpk(r[10], r[11]), cvtpk(r[12], r[13]), cvtpk(r[14], r[15])};
        *(u32x4*)(orow + dl * 64) = w0;
        *(u32x4*)(orow + dl * 64 + 16) = w1;
      }
    }
  }
  STAMP(19); CSTAMP(21);
  STAMP_FLUSH;
}

constexpr unsigned STG2 = 24576u;
constexpr int NST2 = 6;
constexpr int K2_LDS = NST2 * STG2;

__global__ __launch_bounds__(512, 2) void k2_oproj(const __hip_bfloat16* __restrict__ o_ws, const __hip_bfloat16* __restrict__ wo,
                                                    float* __restrict__ part) {
  extern __shared__ __attribute__((aligned(16))) char lds[];
  const unsigned L0 = (unsigned)(uintptr_t)(LDSP char*)lds;
  const int tid = threadIdx.x, lane = tid & 63;
  const int wid = __builtin_amdgcn_readfirstlane(tid >> 6);
  const int mt = wid & 3, kp = wid >> 2;
  const int l31 = lane & 31, h = lane >> 5;
  const int bid = blockIdx.x;
  const int xcd = bid & 7, idx = bid >> 3;
  const int kc = xcd >> 1;
  const int tile = (xcd & 1) ? 32 + idx : idx;
  if (tile >= 63 || (!(xcd & 1) && idx >= 32)) return;
  const int tm = tile / 9, tn = tile % 9;
  STAMP_DECL2;
  STAMP2(0); CSTAMP2(20);

  unsigned vo[2], vw;
#pragma unroll
  for (int e = 0; e < 2; ++e) {
    const int row = 8 * (2 * wid + e) + (lane >> 3);
    const int c = (lane & 7) ^ ((row >> 1) & 7);
    vo[e] = (unsigned)(row * (DH * 2) + c * 16);
  }
  {
    const int row = 8 * wid + (lane >> 3);
    const int c = (lane & 7) ^ (((row >> 1) & 1) << 2);
    vw = (unsigned)(row * (DM * 2) + c * 16);
  }
  const char* ob = (const char*)o_ws + ((size_t)kc * 8 * MROWS + tm * 128) * (DH * 2);
  const char* wb = (const char*)wo + ((size_t)kc * 1024 * DM + tn * 64) * 2;
  auto issue = [&](int t) {
    const unsigned sb = L0 + (unsigned)(t % NST2) * STG2;
    const char* ot = ob + (size_t)(t >> 1) * MROWS * (DH * 2) + (t & 1) * 128;
    GL_O(ot, vo[0], sb + (2 * wid) * 1024);
    GL_O(ot, vo[1], sb + (2 * wid + 1) * 1024);
    GL_WO(wb + (size_t)t * 64 * DM * 2, vw, sb + 16384 + wid * 1024);
  };
#pragma unroll
  for (int t = 0; t < NST2 - 1; ++t) issue(t);

  const int q4 = (lane & 15) >> 2, p4 = lane & 3, g16 = (lane >> 4) & 1;
  unsigned ab[2], aw[2][2];
#pragma unroll
  for (int j = 0; j < 2; ++j) ab[j] = (unsigned)((32 * mt + l31) * 128 + (((2 * (2 * kp + j) + h) ^ ((l31 >> 1) & 7)) * 16));
#pragma unroll
  for (int nt = 0; nt < 2; ++nt)
#pragma unroll
    for (int t = 0; t < 2; ++t) {
      const int k = 32 * kp + 8 * h + 4 * t + q4;
      const int c = (4 * nt + 2 * (p4 & 1) + g16) ^ (((q4 >> 1) & 1) << 2);
      aw[nt][t] = (unsigned)(16384 + k * 128 + c * 16 + (p4 >> 1) * 8);
    }
  f32x16 acc[2] = {};
#pragma unroll
  for (int t = 0; t < 16; ++t) {
    if (t <= 11) wait_vm<12>(); else if (t == 12) wait_vm<9>(); else if (t == 13) wait_vm<6>(); else if (t == 14) wait_vm<3>(); else wait_vm<0>();
    bar();
    STAMP2(1 + t);
    if (t + NST2 - 1 < 16) issue(t + NST2 - 1);
    const unsigned sb = L0 + (unsigned)(t % NST2) * STG2;
#pragma unroll
    for (int j = 0; j < 2; ++j) {
      const bf16x8 bf = lds_b128(sb + ab[j]);
      acc[0] = MFMA(lds_tr2(sb + aw[0][0] + j * 2048, sb + aw[0][1] + j * 2048), bf, acc[0]);
      acc[1] = MFMA(lds_tr2(sb + aw[1][0] + j * 2048, sb + aw[1][1] + j * 2048), bf, acc[1]);
    }
  }
  bar();
  STAMP2(17); CSTAMP2(22);
  {
    f32x16 v, a;
    if (kp == 0) { a = acc[0]; v = acc[1]; } else { a = acc[1]; v = acc[0]; }
    const unsigned ow = L0 + wid * 4096 + lane * 16;
#pragma unroll
    for (int g = 0; g < 4; ++g) {
      f32x4 w = {v[4 * g], v[4 * g + 1], v[4 * g + 2], v[4 * g + 3]};
      *(LDSP f32x4*)(uintptr_t)(ow + g * 1024) = w;
    }
    bar();
    const unsigned orr = L0 + (wid ^ 4) * 4096 + lane * 16;
    float* dst = part + ((size_t)kc * MROWS + tm * 128 + 32 * mt + l31) * DM + tn * 64 + kp * 32 + 16 * h;
#pragma unroll
    for (int g = 0; g < 4; ++g) {
      const f32x4 w = *(const LDSP f32x4*)(uintptr_t)(orr + g * 1024);
      f32x4 r = {a[4 * g] + w[0], a[4 * g + 1] + w[1], a[4 * g + 2] + w[2], a[4 * g + 3] + w[3]};
      *(f32x4*)(dst + 4 * g) = r;
    }
  }
  STAMP2(18); CSTAMP2(23);
  STAMP2(19); CSTAMP2(21);
  STAMP_FLUSH2;
}

__global__ __launch_bounds__(256) void k3_reduce(const float* __restrict__ part, __hip_bfloat16* __restrict__ out) {
  const int i = (blockIdx.x * 256 + threadIdx.x) * 8;
  if (i >= MROWS * DM) return;
  float r[8] = {};
#pragma unroll
  for (int kc = 0; kc < 4; ++kc) {
    const f32x4 a = *(const f32x4*)(part + (size_t)kc * MROWS * DM + i);
    const f32x4 c = *(const f32x4*)(part + (size_t)kc * MROWS * DM + i + 4);
#pragma unroll
    for (int e = 0; e < 4; ++e) { r[e] += a[e]; r[4 + e] += c[e]; }
  }
  u32x4 w = {cvtpk(r[0], r[1]), cvtpk(r[2], r[3]), cvtpk(r[4], r[5]), cvtpk(r[6], r[7])};
  *(u32x4*)(out + i) = w;
}

extern "C" void kernel_launch(void* const* d_in, const int* in_sizes, int n_in, void* d_out, int out_size, void* d_ws,
                              size_t ws_size, hipStream_t stream) {
  const __hip_bfloat16* x = (const __hip_bfloat16*)d_in[0];
  const __hip_bfloat16* kc = (const __hip_bfloat16*)d_in[1];
  const __hip_bfloat16* vc = (const __hip_bfloat16*)d_in[2];
  const __hip_bfloat16* wq = (const __hip_bfloat16*)d_in[3];
  const __hip_bfloat16* wo = (const __hip_bfloat16*)d_in[4];
  __hip_bfloat16* o_ws = (__hip_bfloat16*)d_ws;
  float* part = (float*)((char*)d_ws + (size_t)8 * 1024 * 1024);
  hipFuncSetAttribute((const void*)k1_qattn, hipFuncAttributeMaxDynamicSharedMemorySize, K1_LDS);
  hipFuncSetAttribute((const void*)k2_oproj, hipFuncAttributeMaxDynamicSharedMemorySize, K2_LDS);
  hipLaunchKernelGGL(k1_qattn, dim3(256), dim3(512), K1_LDS, stream, x, kc, vc, wq, o_ws);
  hipLaunchKernelGGL(k2_oproj, dim3(256), dim3(512), K2_LDS, stream, o_ws, wo, part);
  hipLaunchKernelGGL(k3_reduce, dim3((MROWS * DM / 8 + 255) / 256), dim3(256), 0, stream, part, (__hip_bfloat16*)d_out);
}
```

```cpp
#include <hip/hip_runtime.h>
#include <hip/hip_bf16.h>
#include <stdint.h>

typedef __attribute__((ext_vector_type(8))) __bf16 bf16x8;
typedef __attribute__((ext_vector_type(4))) __bf16 bf16x4;
typedef __attribute__((ext_vector_type(2))) __bf16 bf16x2;
typedef __attribute__((ext_vector_type(16))) float f32x16;
typedef __attribute__((ext_vector_type(4))) float f32x4;
typedef __attribute__((ext_vector_type(2))) float f32x2;
typedef __attribute__((ext_vector_type(4))) unsigned u32x4;
#define LDSP __attribute__((address_space(3)))
#define DI __device__ __forceinline__

constexpr int NB = 8, SQ = 112, DM = 576, HQ = 32, HKV = 16, SK = 512, DH = 128;
constexpr int MROWS = NB * SQ;
constexpr int KO = HQ * DH;

DI unsigned cvtpk(float lo, float hi) {
  f32x2 v = {lo, hi};
  bf16x2 b = __builtin_convertvector(v, bf16x2);
  return __builtin_bit_cast(unsigned, b);
}
DI void glds16(const void* sbase, unsigned voff, unsigned lds_dst) {
  unsigned keep;
  asm volatile("s_mov_b32 %0, m0\n\ts_mov_b32 m0, %3\n\ts_nop 0\n\tglobal_load_lds_dwordx4 %1, %2\n\ts_mov_b32 m0, %0"
               : "=&s"(keep) : "v"(voff), "s"(sbase), "s"(lds_dst) : "memory");
}
DI void glds16nt(const void* sbase, unsigned voff, unsigned lds_dst) {
  unsigned keep;
  asm volatile("s_mov_b32 %0, m0\n\ts_mov_b32 m0, %3\n\ts_nop 0\n\tglobal_load_lds_dwordx4 %1, %2 nt\n\ts_mov_b32 m0, %0"
               : "=&s"(keep) : "v"(voff), "s"(sbase), "s"(lds_dst) : "memory");
}
#define GL_KV glds16nt
#define GL_XW glds16
#define GL_WO glds16
#define GL_O glds16
template <int N> DI void wait_vm() { asm volatile("s_waitcnt vmcnt(%0)" ::"i"(N) : "memory"); }
DI void bar() { asm volatile("s_waitcnt lgkmcnt(0)\n\ts_barrier" ::: "memory"); }
DI bf16x8 lds_b128(unsigned a) { return *(const LDSP bf16x8*)(uintptr_t)a; }
DI bf16x8 lds_tr2(unsigned a0, unsigned a1) {
  bf16x4 lo = __builtin_amdgcn_ds_read_tr16_b64_v4bf16((LDSP bf16x4*)(uintptr_t)a0);
  bf16x4 hi = __builtin_amdgcn_ds_read_tr16_b64_v4bf16((LDSP bf16x4*)(uintptr_t)a1);
  return __builtin_shufflevector(lo, hi, 0, 1, 2, 3, 4, 5, 6, 7);
}
DI bf16x8 pack8(const f32x16& x, int s) {
  u32x4 w = {cvtpk(x[8 * s], x[8 * s + 1]), cvtpk(x[8 * s + 2], x[8 * s + 3]), cvtpk(x[8 * s + 4], x[8 * s + 5]),
             cvtpk(x[8 * s + 6], x[8 * s + 7])};
  return __builtin_bit_cast(bf16x8, w);
}
#define STAMP(i)
#define CSTAMP(i)
#define STAMP_DECL
#define STAMP_FLUSH
#define STAMP2(i)
#define CSTAMP2(i)
#define STAMP_DECL2
#define STAMP_FLUSH2
#define MFMA(a, b, c) __builtin_amdgcn_mfma_f32_32x32x16_bf16((a), (b), (c), 0, 0, 0)

constexpr unsigned STG = 32768u;
constexpr unsigned MISC = 4u * STG;
constexpr int K1_LDS = 160 * 1024;
#define SCHED() __builtin_amdgcn_sched_barrier(0)
DI void rawbar() { asm volatile("s_barrier" ::: "memory"); }

__global__ __launch_bounds__(512, 2) void k1_qattn(const __hip_bfloat16* __restrict__ x, const __hip_bfloat16* __restrict__ kc,
                                                    const __hip_bfloat16* __restrict__ vc, const __hip_bfloat16* __restrict__ wq,
                                                    __hip_bfloat16* __restrict__ o_ws, unsigned* __restrict__ cnt) {
  extern __shared__ __attribute__((aligned(16))) char lds[];
  const int tid = threadIdx.x, lane = tid & 63;
  const int wid = __builtin_amdgcn_readfirstlane(tid >> 6);
  const int rg = wid & 3, kh = wid >> 2;
  if (tid == 0) cnt[blockIdx.x * 32] = 0u;
  const int l31 = lane & 31, h = lane >> 5;
  const int bid = blockIdx.x;
  const int hq = 4 * (bid & 7) + ((bid >> 3) & 3), b = bid >> 5;
  const int hkv = hq >> 1;
  STAMP_DECL;

  unsigned vx[2], vw[2], vkv[4];
#pragma unroll
  for (int e = 0; e < 2; ++e) {
    const int i = 2 * wid + e;
    int row = 8 * i + (lane >> 3);
    const int c = (lane & 7) ^ ((row >> 1) & 7);
    row = row < SQ ? row : SQ - 1;
    vx[e] = (unsigned)(row * (DM * 2) + c * 16);
    const int rw = 4 * i + (lane >> 4);
    const int cw = (lane & 15) ^ (((rw & 3) << 2) | ((rw >> 2) & 3));
    vw[e] = (unsigned)(rw * (KO * 2) + cw * 16);
  }
#pragma unroll
  for (int e = 0; e < 4; ++e) {
    const int r = 4 * (4 * (wid & 3) + e) + (lane >> 4);
    const int c = (lane & 15) ^ (((r & 3) << 2) | ((r >> 2) & 3));
    vkv[e] = (unsigned)(r * 256 + c * 16);
  }
  const char* xb = (const char*)x + (size_t)b * SQ * DM * 2;
  const char* wb = (const char*)wq + (size_t)hq * DH * 2;
  const size_t kvo = ((size_t)(b * HKV + hkv) * SK + 256 * kh) * DH * 2;
  const char* kb = (const char*)kc + kvo;
  const char* vb = (const char*)vc + kvo;

  auto issue1 = [&](int t, int e) {
    const unsigned sb = (unsigned)(t & 3) * STG;
    if (t < 9) {
      if (e < 2) GL_XW(xb + t * 128, vx[e], sb + (2 * wid + e) * 1024);
      else       GL_XW(wb + (size_t)t * 64 * KO * 2, vw[e - 2], sb + 16384 + (2 * wid + e - 2) * 1024);
    } else if (t < 13) GL_KV(kb + (t - 9) * 16384, vkv[e], sb + wid * 4096 + e * 1024);
    else if (t < 17)   GL_KV(vb + (t - 13) * 16384, vkv[e], sb + wid * 4096 + e * 1024);
  };
  STAMP(0); CSTAMP(20);
#pragma unroll
  for (int t = 0; t < 3; ++t)
#pragma unroll
    for (int e = 0; e < 4; ++e) issue1(t, e);

  const int q4 = (lane & 15) >> 2, p4 = lane & 3, g16 = (lane >> 4) & 1;
  unsigned ax[4];
#pragma unroll
  for (int ks = 0; ks < 4; ++ks) ax[ks] = (unsigned)((32 * rg + l31) * 128 + (((2 * ks + h) ^ ((l31 >> 1) & 7)) * 16));
  unsigned aw[2][2];
#pragma unroll
  for (int dl = 0; dl < 2; ++dl)
#pragma unroll
    for (int t = 0; t < 2; ++t) {
      const int dt = 2 * kh + dl;
      const int k = 8 * h + 4 * t + q4;
      const int c = (4 * dt + 2 * g16 + (p4 & 1)) ^ ((q4 << 2) | ((2 * h + t) & 3));
      aw[dl][t] = (unsigned)(16384 + k * 256 + c * 16 + (p4 >> 1) * 8);
    }
  unsigned ak[8];
#pragma unroll
  for (int ks = 0; ks < 8; ++ks)
    ak[ks] = (unsigned)(kh * 16384 + l31 * 256 + (((2 * ks + h) ^ (((l31 & 3) << 2) | ((l31 >> 2) & 3))) * 16));
  unsigned av[4][2];
#pragma unroll
  for (int dt = 0; dt < 4; ++dt)
#pragma unroll
    for (int t = 0; t < 2; ++t) {
      const int key = 8 * t + 4 * h + q4;
      const int c = (4 * dt + 2 * (p4 & 1) + g16) ^ ((q4 << 2) | ((2 * t + h) & 3));
      av[dt][t] = (unsigned)(kh * 16384 + key * 256 + c * 16 + (p4 >> 1) * 8);
    }

  bf16x8 f1[3][3];
  auto ld1 = [&](int G) {
    const unsigned sb = (unsigned)((G >> 2) & 3) * STG; const int ks = G & 3, r = G % 3;
    f1[r][0] = lds_b128(sb + ax[ks]);
    f1[r][1] = lds_tr2(sb + aw[0][0] + ks * 4096, sb + aw[0][1] + ks * 4096);
    f1[r][2] = lds_tr2(sb + aw[1][0] + ks * 4096, sb + aw[1][1] + ks * 4096);
  };
  bf16x8 f2[3][4];
  auto ld2 = [&](int G) {
    const unsigned sb = (unsigned)((9 + (G >> 2)) & 3) * STG + ((G >> 1) & 1) * 8192; const int r = G % 3;
#pragma unroll
    for (int j = 0; j < 4; ++j) f2[r][j] = lds_b128(sb + ak[4 * (G & 1) + j]);
  };
  auto ld3 = [&](int G) {
    const unsigned sb = (unsigned)((13 + (G >> 2)) & 3) * STG + ((G >> 1) & 1) * 8192 + (G & 1) * 4096; const int r = (G + 1) % 3;
#pragma unroll
    for (int dt = 0; dt < 4; ++dt) f2[r][dt] = lds_tr2(sb + av[dt][0], sb + av[dt][1]);
  };

  wait_vm<8>(); rawbar();
  ld1(0); ld1(1);
  SCHED();

  f32x16 qa[2] = {};
#pragma unroll
  for (int t = 0; t < 9; ++t) {
    wait_vm<4>(); rawbar();
    STAMP(1 + t);
#pragma unroll
    for (int ks = 0; ks < 4; ++ks) {
      const int G = 4 * t + ks;
      issue1(t + 3, ks);
      if (G + 2 < 36) ld1(G + 2); else ld2(G + 2 - 36);
      qa[0] = MFMA(f1[G % 3][1], f1[G % 3][0], qa[0]);
      qa[1] = MFMA(f1[G % 3][2], f1[G % 3][0], qa[1]);
      SCHED();
    }
  }
  bf16x8 qf[8];
  {
    bf16x8 own[4] = {pack8(qa[0], 0), pack8(qa[0], 1), pack8(qa[1], 0), pack8(qa[1], 1)};
    const unsigned qw = MISC + wid * 4096 + lane * 16;
#pragma unroll
    for (int f = 0; f < 4; ++f) *(LDSP bf16x8*)(uintptr_t)(qw + f * 1024) = own[f];
    wait_vm<4>(); bar();
    const unsigned qr = MISC + (wid ^ 4) * 4096 + lane * 16;
    if (kh == 0) {
#pragma unroll
      for (int f = 0; f < 4; ++f) { qf[f] = own[f]; qf[4 + f] = lds_b128(qr + f * 1024); }
    } else {
#pragma unroll
      for (int f = 0; f < 4; ++f) { qf[4 + f] = own[f]; qf[f] = lds_b128(qr + f * 1024); }
    }
  }
  SCHED();
  CSTAMP(22);
  const float CS = 0.08838834764831845f * 1.4426950408889634f;
  f32x16 s[8];
  bf16x8 pf[16];
  float m1 = 0.f, mb = 0.f, ls0 = 0.f, ls1 = 0.f;
  auto tilemax = [&](int t0) {
    float a = fmaxf(s[t0][0], s[t0][1]), c = fmaxf(s[t0 + 1][0], s[t0 + 1][1]);
    float d = fmaxf(s[t0 + 2][0], s[t0 + 2][1]), e = fmaxf(s[t0 + 3][0], s[t0 + 3][1]);
#pragma unroll
    for (int r = 2; r < 16; r += 2) {
      a = fmaxf(fmaxf(a, s[t0][r]), s[t0][r + 1]); c = fmaxf(fmaxf(c, s[t0 + 1][r]), s[t0 + 1][r + 1]);
      d = fmaxf(fmaxf(d, s[t0 + 2][r]), s[t0 + 2][r + 1]); e = fmaxf(fmaxf(e, s[t0 + 3][r]), s[t0 + 3][r + 1]);
    }
    float m = fmaxf(fmaxf(a, c), fmaxf(d, e));
    auto rr = __builtin_amdgcn_permlane32_swap(__float_as_uint(m), __float_as_uint(m), false, false);
    return fmaxf(__uint_as_float(rr[0]), __uint_as_float(rr[1]));
  };
  auto softfrag = [&](int f) {
    float p[8];
#pragma unroll
    for (int j = 0; j < 8; ++j) p[j] = __builtin_amdgcn_exp2f(fmaf(s[f >> 1][8 * (f & 1) + j], CS, -mb));
    ls0 += (p[0] + p[1]) + (p[2] + p[3]); ls1 += (p[4] + p[5]) + (p[6] + p[7]);
    u32x4 w = {cvtpk(p[0], p[1]), cvtpk(p[2], p[3]), cvtpk(p[4], p[5]), cvtpk(p[6], p[7])};
    pf[f] = __builtin_bit_cast(bf16x8, w);
  };
#pragma unroll
  for (int G = 0; G < 16; ++G) {
    const int i = G >> 2, t = 9 + i, kt = (G >> 1) & 1;
    if ((G & 3) == 0) {
      if (i > 0) { wait_vm<4>(); rawbar(); }
      STAMP(1 + t);
    }
    issue1(t + 3, G & 3);
    if (G + 2 < 16) ld2(G + 2); else ld3(G + 2 - 16);
    f32x16 acc;
    if (G & 1) acc = s[2 * i + kt]; else acc = f32x16{};
#pragma unroll
    for (int j = 0; j < 4; ++j) acc = MFMA(f2[G % 3][j], qf[4 * (G & 1) + j], acc);
    s[2 * i + kt] = acc;
    if (G == 8) { m1 = tilemax(0); mb = m1 * CS; }
    if (G >= 8) softfrag(G - 8);
    SCHED();
  }

  f32x16 oa[4] = {};
  float mx = 0.f;
#pragma unroll
  for (int G = 0; G < 16; ++G) {
    const int i = G >> 2, t = 13 + i;
    if ((G & 3) == 0) {
      if (i < 2) wait_vm<4>(); else if (i == 2) wait_vm<0>();
      if (i < 3) rawbar();
      STAMP(1 + t);
    }
    issue1(t + 3, G & 3);
    if (G + 2 < 16) ld3(G + 2);
    if (G == 8) {
      const float al = __builtin_amdgcn_exp2f((m1 - mx) * CS);
#pragma unroll
      for (int dt = 0; dt < 4; ++dt)
#pragma unroll
        for (int r = 0; r < 16; ++r) oa[dt][r] *= al;
    }
#pragma unroll
    for (int dt = 0; dt < 4; ++dt) oa[dt] = MFMA(f2[(G + 1) % 3][dt], pf[G], oa[dt]);
    if (G == 0) {
      mx = fmaxf(m1, tilemax(4)); mb = mx * CS;
      const float al = __builtin_amdgcn_exp2f((m1 - mx) * CS);
      ls0 *= al; ls1 *= al;
    }
    if (G < 8) softfrag(8 + G);
    SCHED();
  }
  float ls = ls0 + ls1;
  {
    auto rr = __builtin_amdgcn_permlane32_swap(__float_as_uint(ls), __float_as_uint(ls), false, false);
    ls = __uint_as_float(rr[0]) + __uint_as_float(rr[1]);
  }
  bar();
  STAMP(18); CSTAMP(23);
  {
    const unsigned ow = wid * 8192 + lane * 16;
    f32x16 keep[2], send[2];
    if (kh == 0) { keep[0] = oa[0]; keep[1] = oa[1]; send[0] = oa[2]; send[1] = oa[3]; }
    else         { keep[0] = oa[2]; keep[1] = oa[3]; send[0] = oa[0]; send[1] = oa[1]; }
#pragma unroll
    for (int dl = 0; dl < 2; ++dl) {
      const f32x16 v = send[dl];
#pragma unroll
      for (int g = 0; g < 4; ++g) {
        f32x4 w = {v[4 * g], v[4 * g + 1], v[4 * g + 2], v[4 * g + 3]};
        *(LDSP f32x4*)(uintptr_t)(ow + (dl * 4 + g) * 1024) = w;
      }
    }
    f32x2 ml = {mx, ls};
    *(LDSP f32x2*)(uintptr_t)(MISC + wid * 512 + lane * 8) = ml;
    bar();
    const f32x2 pml = *(const LDSP f32x2*)(uintptr_t)(MISC + (wid ^ 4) * 512 + lane * 8);
    const float mt = fmaxf(mx, pml[0]);
    const float f0 = __builtin_amdgcn_exp2f((mx - mt) * CS), f1_ = __builtin_amdgcn_exp2f((pml[0] - mt) * CS);
    const float inv = 1.f / (f0 * ls + f1_ * pml[1]);
    const float g0 = f0 * inv, g1 = f1_ * inv;
    const unsigned orr = (wid ^ 4) * 8192 + lane * 16;
    const int qrow = 32 * rg + l31;
    char* orow = (char*)o_ws + ((size_t)hq * MROWS + b * SQ + qrow) * (DH * 2) + kh * 128 + h * 32;
#pragma unroll
    for (int dl = 0; dl < 2; ++dl) {
      const f32x16 v = keep[dl];
      float r[16];
#pragma unroll
      for (int g = 0; g < 4; ++g) {
        const f32x4 w = *(const LDSP f32x4*)(uintptr_t)(orr + (dl * 4 + g) * 1024);
#pragma unroll
        for (int e = 0; e < 4; ++e) r[4 * g + e] = v[4 * g + e] * g0 + w[e] * g1;
      }
      if (qrow < SQ) {
        u32x4 w0 = {cvtpk(r[0], r[1]), cvtpk(r[2], r[3]), cvtpk(r[4], r[5]), cvtpk(r[6], r[7])};
        u32x4 w1 = {cvtpk(r[8], r[9]), cvtpk(r[10], r[11]), cvtpk(r[12], r[13]), cvtpk(r[14], r[15])};
        *(u32x4*)(orow + dl * 64) = w0;
        *(u32x4*)(orow + dl * 64 + 16) = w1;
      }
    }
  }
  STAMP(19); CSTAMP(21);
  STAMP_FLUSH;
}

constexpr unsigned STG2 = 24576u;
constexpr int NST2 = 6;
constexpr int K2_LDS = NST2 * STG2;
typedef __attribute__((address_space(1))) unsigned gu32;

__global__ __launch_bounds__(512, 2) void k2_oproj(const __hip_bfloat16* __restrict__ o_ws, const __hip_bfloat16* __restrict__ wo,
                                                    float* __restrict__ slab, unsigned* __restrict__ cnt,
                                                    __hip_bfloat16* __restrict__ out) {
  extern __shared__ __attribute__((aligned(16))) char lds[];
  const int tid = threadIdx.x, lane = tid & 63;
  const int wid = __builtin_amdgcn_readfirstlane(tid >> 6);
  const int mt = wid & 3, nh = wid >> 2;
  const int l31 = lane & 31, h = lane >> 5;
  const int bid = blockIdx.x;
  const int xcd = bid & 7, idx = bid >> 3;
  const int kc = xcd >> 1;
  const int tile = (xcd & 1) ? 32 + idx : idx;
  if (tile >= 63) return;
  const int tm = tile / 9, tn = tile % 9;
  STAMP_DECL2;
  STAMP2(0); CSTAMP2(20);

  unsigned vo[2], vw;
#pragma unroll
  for (int e = 0; e < 2; ++e) {
    const int row = 8 * (2 * wid + e) + (lane >> 3);
    const int c = (lane & 7) ^ ((row >> 1) & 7);
    vo[e] = (unsigned)(row * (DH * 2) + c * 16);
  }
  {
    const int row = 8 * wid + (lane >> 3);
    const int c = (lane & 7) ^ (((row >> 1) & 1) << 2);
    vw = (unsigned)(row * (DM * 2) + c * 16);
  }
  const char* ob = (const char*)o_ws + ((size_t)kc * 8 * MROWS + tm * 128) * (DH * 2);
  const char* wb = (const char*)wo + ((size_t)kc * 1024 * DM + tn * 64) * 2;
  auto issue = [&](int t, int e) {
    const unsigned sb = (unsigned)(t % NST2) * STG2;
    const char* ot = ob + (size_t)(t >> 1) * MROWS * (DH * 2) + (t & 1) * 128;
    if (e < 2) GL_O(ot, vo[e], sb + (2 * wid + e) * 1024);
    else       GL_WO(wb + (size_t)t * 64 * DM * 2, vw, sb + 16384 + wid * 1024);
  };
#pragma unroll
  for (int t = 0; t < NST2 - 1; ++t)
#pragma unroll
    for (int e = 0; e < 3; ++e) issue(t, e);

  const int q4 = (lane & 15) >> 2, p4 = lane & 3, g16 = (lane >> 4) & 1;
  unsigned ab[4], aw[2];
#pragma unroll
  for (int j = 0; j < 4; ++j) ab[j] = (unsigned)((32 * mt + l31) * 128 + (((2 * j + h) ^ ((l31 >> 1) & 7)) * 16));
#pragma unroll
  for (int t = 0; t < 2; ++t) {
    const int k = 8 * h + 4 * t + q4;
    const int c = (4 * nh + 2 * (p4 & 1) + g16) ^ (((q4 >> 1) & 1) << 2);
    aw[t] = (unsigned)(16384 + k * 128 + c * 16 + (p4 >> 1) * 8);
  }
  bf16x8 fa[2][4], fb[2][4];
  auto ld = [&](int t, int j) {
    const unsigned sb = (unsigned)(t % NST2) * STG2;
    fb[t & 1][j] = lds_b128(sb + ab[j]);
    fa[t & 1][j] = lds_tr2(sb + aw[0] + j * 2048, sb + aw[1] + j * 2048);
  };
  wait_vm<12>(); rawbar();
#pragma unroll
  for (int j = 0; j < 4; ++j) ld(0, j);
  SCHED();
  f32x16 acc = {};
#pragma unroll
  for (int t = 0; t < 16; ++t) {
    if (t <= 11) wait_vm<9>(); else if (t == 12) wait_vm<6>(); else if (t == 13) wait_vm<3>(); else if (t == 14) wait_vm<0>();
    if (t < 15) rawbar();
    STAMP2(1 + t);
#pragma unroll
    for (int j = 0; j < 4; ++j) {
      if (j < 3 && t + NST2 - 1 < 16) issue(t + NST2 - 1, j);
      if (t + 1 < 16) ld(t + 1, j);
      acc = MFMA(fa[t & 1][j], fb[t & 1][j], acc);
      SCHED();
    }
  }
  STAMP2(17); CSTAMP2(22);
  float* sl = slab + (size_t)tile * 4 * 4 * 2 * 1024;
  if (mt != kc) {
    const __amdgpu_buffer_rsrc_t rs = __builtin_amdgcn_make_buffer_rsrc((void*)(sl + ((mt * 4 + kc) * 2 + nh) * 1024), 0, 4096, 0x00020000);
#pragma unroll
    for (int g = 0; g < 4; ++g) {
      f32x4 w = {acc[4 * g], acc[4 * g + 1], acc[4 * g + 2], acc[4 * g + 3]};
      __builtin_amdgcn_raw_buffer_store_b128(__builtin_bit_cast(u32x4, w), rs, g * 1024 + lane * 16, 0, 16);
    }
  }
  asm volatile("s_waitcnt vmcnt(0)" ::: "memory");
  __syncthreads();
  if (wid == 0 && lane < 4 && lane != kc)
    __hip_atomic_fetch_add((gu32*)(cnt + (tile * 4 + lane) * 32), 1u, __ATOMIC_RELAXED, __HIP_MEMORY_SCOPE_AGENT);
  if (mt == kc) {
    gu32* flag = (gu32*)(cnt + (tile * 4 + kc) * 32);
    for (unsigned spins = 0; spins < (1u << 22); ++spins) {
      if (__hip_atomic_load(flag, __ATOMIC_RELAXED, __HIP_MEMORY_SCOPE_AGENT) >= 3u) break;
      __builtin_amdgcn_s_sleep(1);
    }
    asm volatile("" ::: "memory");
    u32x4 pv[3][4];
#pragma unroll
    for (int o = 0; o < 3; ++o) {
      const int src = o + (o >= kc);
      const __amdgpu_buffer_rsrc_t rs = __builtin_amdgcn_make_buffer_rsrc((void*)(sl + ((kc * 4 + src) * 2 + nh) * 1024), 0, 4096, 0x00020000);
#pragma unroll
      for (int g = 0; g < 4; ++g) pv[o][g] = __builtin_amdgcn_raw_buffer_load_b128(rs, g * 1024 + lane * 16, 0, 16);
    }
    float r[16];
#pragma unroll
    for (int g = 0; g < 4; ++g) {
      const f32x4 a = __builtin_bit_cast(f32x4, pv[0][g]), c = __builtin_bit_cast(f32x4, pv[1][g]), d = __builtin_bit_cast(f32x4, pv[2][g]);
#pragma unroll
      for (int e = 0; e < 4; ++e) {
        const float own = acc[4 * g + e];
        const float t0 = kc == 0 ? own : a[e];
        const float t1 = kc == 0 ? a[e] : (kc == 1 ? own : c[e]);
        const float t2 = kc <= 1 ? c[e] : (kc == 2 ? own : d[e]);
        const float t3 = kc == 3 ? own : d[e];
        r[4 * g + e] = ((t0 + t1) + t2) + t3;
      }
    }
    char* dst = (char*)out + ((size_t)(tm * 128 + 32 * mt + l31) * DM + tn * 64 + nh * 32 + 16 * h) * 2;
    u32x4 w0 = {cvtpk(r[0], r[1]), cvtpk(r[2], r[3]), cvtpk(r[4], r[5]), cvtpk(r[6], r[7])};
    u32x4 w1 = {cvtpk(r[8], r[9]), cvtpk(r[10], r[11]), cvtpk(r[12], r[13]), cvtpk(r[14], r[15])};
    *(u32x4*)dst = w0;
    *(u32x4*)(dst + 16) = w1;
  }
  STAMP2(18); CSTAMP2(23);
  STAMP2(19); CSTAMP2(21);
  STAMP_FLUSH2;
}

extern "C" void kernel_launch(void* const* d_in, const int* in_sizes, int n_in, void* d_out, int out_size, void* d_ws,
                              size_t ws_size, hipStream_t stream) {
  const __hip_bfloat16* x = (const __hip_bfloat16*)d_in[0];
  const __hip_bfloat16* kc = (const __hip_bfloat16*)d_in[1];
  const __hip_bfloat16* vc = (const __hip_bfloat16*)d_in[2];
  const __hip_bfloat16* wq = (const __hip_bfloat16*)d_in[3];
  const __hip_bfloat16* wo = (const __hip_bfloat16*)d_in[4];
  __hip_bfloat16* o_ws = (__hip_bfloat16*)d_ws;
  float* slab = (float*)((char*)d_ws + (size_t)8 * 1024 * 1024);
  unsigned* cnt = (unsigned*)((char*)d_ws + (size_t)24 * 1024 * 1024);
  hipFuncSetAttribute((const void*)k1_qattn, hipFuncAttributeMaxDynamicSharedMemorySize, K1_LDS);
  hipFuncSetAttribute((const void*)k2_oproj, hipFuncAttributeMaxDynamicSharedMemorySize, K2_LDS);
  hipLaunchKernelGGL(k1_qattn, dim3(256), dim3(512), K1_LDS, stream, x, kc, vc, wq, o_ws, cnt);
  hipLaunchKernelGGL(k2_oproj, dim3(256), dim3(512), K2_LDS, stream, o_ws, wo, slab, cnt, (__hip_bfloat16*)d_out);
}
```

```cpp
#include <hip/hip_runtime.h>
#include <hip/hip_bf16.h>
#include <stdint.h>

typedef __attribute__((ext_vector_type(8))) __bf16 bf16x8;
typedef __attribute__((ext_vector_type(4))) __bf16 bf16x4;
typedef __attribute__((ext_vector_type(2))) __bf16 bf16x2;
typedef __attribute__((ext_vector_type(16))) float f32x16;
typedef __attribute__((ext_vector_type(4))) float f32x4;
typedef __attribute__((ext_vector_type(2))) float f32x2;
typedef __attribute__((ext_vector_type(4))) unsigned u32x4;
#define LDSP __attribute__((address_space(3)))
#define DI __device__ __forceinline__

constexpr int NB = 8, SQ = 112, DM = 576, HQ = 32, HKV = 16, SK = 512, DH = 128;
constexpr int MROWS = NB * SQ;
constexpr int KO = HQ * DH;

DI unsigned cvtpk(float lo, float hi) {
  f32x2 v = {lo, hi};
  bf16x2 b = __builtin_convertvector(v, bf16x2);
  return __builtin_bit_cast(unsigned, b);
}
DI void glds16(const void* sbase, unsigned voff, unsigned lds_dst) {
  unsigned keep;
  asm volatile("s_mov_b32 %0, m0\n\ts_mov_b32 m0, %3\n\ts_nop 0\n\tglobal_load_lds_dwordx4 %1, %2\n\ts_mov_b32 m0, %0"
               : "=&s"(keep) : "v"(voff), "s"(sbase), "s"(lds_dst) : "memory");
}
DI void glds16nt(const void* sbase, unsigned voff, unsigned lds_dst) {
  unsigned keep;
  asm volatile("s_mov_b32 %0, m0\n\ts_mov_b32 m0, %3\n\ts_nop 0\n\tglobal_load_lds_dwordx4 %1, %2 nt\n\ts_mov_b32 m0, %0"
               : "=&s"(keep) : "v"(voff), "s"(sbase), "s"(lds_dst) : "memory");
}
DI void glds16sc1(const void* sbase, unsigned voff, unsigned lds_dst) {
  unsigned keep;
  asm volatile("s_mov_b32 %0, m0\n\ts_mov_b32 m0, %3\n\ts_nop 0\n\tglobal_load_lds_dwordx4 %1, %2 sc1\n\ts_mov_b32 m0, %0"
               : "=&s"(keep) : "v"(voff), "s"(sbase), "s"(lds_dst) : "memory");
}
#define GL_KV glds16nt
#define GL_XW glds16
#define GL_WO glds16
#define GL_O glds16
template <int N> DI void wait_vm() { asm volatile("s_waitcnt vmcnt(%0)" ::"i"(N) : "memory"); }
DI void bar() { asm volatile("s_waitcnt lgkmcnt(0)\n\ts_barrier" ::: "memory"); }
DI bf16x8 lds_b128(unsigned a) { return *(const LDSP bf16x8*)(uintptr_t)a; }
DI bf16x8 lds_tr2(unsigned a0, unsigned a1) {
  bf16x4 lo = __builtin_amdgcn_ds_read_tr16_b64_v4bf16((LDSP bf16x4*)(uintptr_t)a0);
  bf16x4 hi = __builtin_amdgcn_ds_read_tr16_b64_v4bf16((LDSP bf16x4*)(uintptr_t)a1);
  return __builtin_shufflevector(lo, hi, 0, 1, 2, 3, 4, 5, 6, 7);
}
DI bf16x8 pack8(const f32x16& x, int s) {
  u32x4 w = {cvtpk(x[8 * s], x[8 * s + 1]), cvtpk(x[8 * s + 2], x[8 * s + 3]), cvtpk(x[8 * s + 4], x[8 * s + 5]),
             cvtpk(x[8 * s + 6], x[8 * s + 7])};
  return __builtin_bit_cast(bf16x8, w);
}
#define STAMP(i)
#define CSTAMP(i)
#define STAMP_DECL
#define STAMP_FLUSH
#define STAMP2(i)
#define CSTAMP2(i)
#define STAMP_DECL2
#define STAMP_FLUSH2
#define MFMA(a, b, c) __builtin_amdgcn_mfma_f32_32x32x16_bf16((a), (b), (c), 0, 0, 0)

constexpr unsigned STG = 32768u;
constexpr unsigned MISC = 4u * STG;
constexpr int K1_LDS = 160 * 1024;
#define SCHED() __builtin_amdgcn_sched_barrier(0)
DI void rawbar() { asm volatile("s_barrier" ::: "memory"); }

typedef __attribute__((address_space(1))) unsigned long long gu64;
extern "C" __device__ unsigned long long hw_dispatch_id() __asm("llvm.amdgcn.dispatch.id");
constexpr unsigned WO_LO = 112u * 1024u;
DI unsigned wo_slot(int t) { const int r = t % 6; return r < 4 ? MISC + 8192u * r : WO_LO + 8192u * (r - 4); }
DI void wait_flags(gu64* f, bool on, unsigned long long tag) {
  for (unsigned spins = 0; spins < (1u << 20); ++spins) {
    const unsigned long long v = on ? __hip_atomic_load(f, __ATOMIC_RELAXED, __HIP_MEMORY_SCOPE_AGENT) : tag;
    if (__all(v == tag)) break;
    __builtin_amdgcn_s_sleep(1);
  }
  asm volatile("" ::: "memory");
}

__global__ __launch_bounds__(512, 2) void k_fused(const __hip_bfloat16* __restrict__ x, const __hip_bfloat16* __restrict__ kc,
                                                   const __hip_bfloat16* __restrict__ vc, const __hip_bfloat16* __restrict__ wq,
                                                   const __hip_bfloat16* __restrict__ wo, __hip_bfloat16* __restrict__ o_ws,
                                                   float* __restrict__ slab, unsigned long long* __restrict__ flags,
                                                   __hip_bfloat16* __restrict__ out) {
  extern __shared__ __attribute__((aligned(16))) char lds[];
  const int tid = threadIdx.x, lane = tid & 63;
  const int wid = __builtin_amdgcn_readfirstlane(tid >> 6);
  const int rg = wid & 3, kh = wid >> 2;
  const int l31 = lane & 31, h = lane >> 5;
  const int bid = blockIdx.x;
  const int hq = 4 * (bid & 7) + ((bid >> 3) & 3), b = bid >> 5;
  const int hkv = hq >> 1;
  STAMP_DECL;

  unsigned vx[2], vw[2], vkv[4];
#pragma unroll
  for (int e = 0; e < 2; ++e) {
    const int i = 2 * wid + e;
    int row = 8 * i + (lane >> 3);
    const int c = (lane & 7) ^ ((row >> 1) & 7);
    row = row < SQ ? row : SQ - 1;
    vx[e] = (unsigned)(row * (DM * 2) + c * 16);
    const int rw = 4 * i + (lane >> 4);
    const int cw = (lane & 15) ^ (((rw & 3) << 2) | ((rw >> 2) & 3));
    vw[e] = (unsigned)(rw * (KO * 2) + cw * 16);
  }
#pragma unroll
  for (int e = 0; e < 4; ++e) {
    const int r = 4 * (4 * (wid & 3) + e) + (lane >> 4);
    const int c = (lane & 15) ^ (((r & 3) << 2) | ((r >> 2) & 3));
    vkv[e] = (unsigned)(r * 256 + c * 16);
  }
  const char* xb = (const char*)x + (size_t)b * SQ * DM * 2;
  const char* wb = (const char*)wq + (size_t)hq * DH * 2;
  const size_t kvo = ((size_t)(b * HKV + hkv) * SK + 256 * kh) * DH * 2;
  const char* kb = (const char*)kc + kvo;
  const char* vb = (const char*)vc + kvo;

  auto issue1 = [&](int t, int e) {
    const unsigned sb = (unsigned)(t & 3) * STG;
    if (t < 9) {
      if (e < 2) GL_XW(xb + t * 128, vx[e], sb + (2 * wid + e) * 1024);
      else       GL_XW(wb + (size_t)t * 64 * KO * 2, vw[e - 2], sb + 16384 + (2 * wid + e - 2) * 1024);
    } else if (t < 13) GL_KV(kb + (t - 9) * 16384, vkv[e], sb + wid * 4096 + e * 1024);
    else if (t < 17)   GL_KV(vb + (t - 13) * 16384, vkv[e], sb + wid * 4096 + e * 1024);
  };
  STAMP(0); CSTAMP(20);
#pragma unroll
  for (int t = 0; t < 3; ++t)
#pragma unroll
    for (int e = 0; e < 4; ++e) issue1(t, e);

  const int q4 = (lane & 15) >> 2, p4 = lane & 3, g16 = (lane >> 4) & 1;
  unsigned ax[4];
#pragma unroll
  for (int ks = 0; ks < 4; ++ks) ax[ks] = (unsigned)((32 * rg + l31) * 128 + (((2 * ks + h) ^ ((l31 >> 1) & 7)) * 16));
  unsigned aw[2][2];
#pragma unroll
  for (int dl = 0; dl < 2; ++dl)
#pragma unroll
    for (int t = 0; t < 2; ++t) {
      const int dt = 2 * kh + dl;
      const int k = 8 * h + 4 * t + q4;
      const int c = (4 * dt + 2 * g16 + (p4 & 1)) ^ ((q4 << 2) | ((2 * h + t) & 3));
      aw[dl][t] = (unsigned)(16384 + k * 256 + c * 16 + (p4 >> 1) * 8);
    }
  unsigned ak[8];
#pragma unroll
  for (int ks = 0; ks < 8; ++ks)
    ak[ks] = (unsigned)(kh * 16384 + l31 * 256 + (((2 * ks + h) ^ (((l31 & 3) << 2) | ((l31 >> 2) & 3))) * 16));
  unsigned av[4][2];
#pragma unroll
  for (int dt = 0; dt < 4; ++dt)
#pragma unroll
    for (int t = 0; t < 2; ++t) {
      const int key = 8 * t + 4 * h + q4;
      const int c = (4 * dt + 2 * (p4 & 1) + g16) ^ ((q4 << 2) | ((2 * t + h) & 3));
      av[dt][t] = (unsigned)(kh * 16384 + key * 256 + c * 16 + (p4 >> 1) * 8);
    }

  bf16x8 f1[3][3];
  auto ld1 = [&](int G) {
    const unsigned sb = (unsigned)((G >> 2) & 3) * STG; const int ks = G & 3, r = G % 3;
    f1[r][0] = lds_b128(sb + ax[ks]);
    f1[r][1] = lds_tr2(sb + aw[0][0] + ks * 4096, sb + aw[0][1] + ks * 4096);
    f1[r][2] = lds_tr2(sb + aw[1][0] + ks * 4096, sb + aw[1][1] + ks * 4096);
  };
  bf16x8 f2[3][4];
  auto ld2 = [&](int G) {
    const unsigned sb = (unsigned)((9 + (G >> 2)) & 3) * STG + ((G >> 1) & 1) * 8192; const int r = G % 3;
#pragma unroll
    for (int j = 0; j < 4; ++j) f2[r][j] = lds_b128(sb + ak[4 * (G & 1) + j]);
  };
  auto ld3 = [&](int G) {
    const unsigned sb = (unsigned)((13 + (G >> 2)) & 3) * STG + ((G >> 1) & 1) * 8192 + (G & 1) * 4096; const int r = (G + 1) % 3;
#pragma unroll
    for (int dt = 0; dt < 4; ++dt) f2[r][dt] = lds_tr2(sb + av[dt][0], sb + av[dt][1]);
  };

  wait_vm<8>(); rawbar();
  ld1(0); ld1(1);
  SCHED();

  f32x16 qa[2] = {};
#pragma unroll
  for (int t = 0; t < 9; ++t) {
    wait_vm<4>(); rawbar();
    STAMP(1 + t);
#pragma unroll
    for (int ks = 0; ks < 4; ++ks) {
      const int G = 4 * t + ks;
      issue1(t + 3, ks);
      if (G + 2 < 36) ld1(G + 2); else ld2(G + 2 - 36);
      qa[0] = MFMA(f1[G % 3][1], f1[G % 3][0], qa[0]);
      qa[1] = MFMA(f1[G % 3][2], f1[G % 3][0], qa[1]);
      SCHED();
    }
  }
  bf16x8 qf[8];
  {
    bf16x8 own[4] = {pack8(qa[0], 0), pack8(qa[0], 1), pack8(qa[1], 0), pack8(qa[1], 1)};
    const unsigned qw = MISC + wid * 4096 + lane * 16;
#pragma unroll
    for (int f = 0; f < 4; ++f) *(LDSP bf16x8*)(uintptr_t)(qw + f * 1024) = own[f];
    wait_vm<4>(); bar();
    const unsigned qr = MISC + (wid ^ 4) * 4096 + lane * 16;
    if (kh == 0) {
#pragma unroll
      for (int f = 0; f < 4; ++f) { qf[f] = own[f]; qf[4 + f] = lds_b128(qr + f * 1024); }
    } else {
#pragma unroll
      for (int f = 0; f < 4; ++f) { qf[4 + f] = own[f]; qf[f] = lds_b128(qr + f * 1024); }
    }
  }
  SCHED();
  CSTAMP(22);
  const float CS = 0.08838834764831845f * 1.4426950408889634f;
  f32x16 s[8];
  bf16x8 pf[16];
  float m1 = 0.f, mb = 0.f, ls0 = 0.f, ls1 = 0.f;
  auto tilemax = [&](int t0) {
    float a = fmaxf(s[t0][0], s[t0][1]), c = fmaxf(s[t0 + 1][0], s[t0 + 1][1]);
    float d = fmaxf(s[t0 + 2][0], s[t0 + 2][1]), e = fmaxf(s[t0 + 3][0], s[t0 + 3][1]);
#pragma unroll
    for (int r = 2; r < 16; r += 2) {
      a = fmaxf(fmaxf(a, s[t0][r]), s[t0][r + 1]); c = fmaxf(fmaxf(c, s[t0 + 1][r]), s[t0 + 1][r + 1]);
      d = fmaxf(fmaxf(d, s[t0 + 2][r]), s[t0 + 2][r + 1]); e = fmaxf(fmaxf(e, s[t0 + 3][r]), s[t0 + 3][r + 1]);
    }
    float m = fmaxf(fmaxf(a, c), fmaxf(d, e));
    auto rr = __builtin_amdgcn_permlane32_swap(__float_as_uint(m), __float_as_uint(m), false, false);
    return fmaxf(__uint_as_float(rr[0]), __uint_as_float(rr[1]));
  };
  auto softfrag = [&](int f) {
    float p[8];
#pragma unroll
    for (int j = 0; j < 8; ++j) p[j] = __builtin_amdgcn_exp2f(fmaf(s[f >> 1][8 * (f & 1) + j], CS, -mb));
    ls0 += (p[0] + p[1]) + (p[2] + p[3]); ls1 += (p[4] + p[5]) + (p[6] + p[7]);
    u32x4 w = {cvtpk(p[0], p[1]), cvtpk(p[2], p[3]), cvtpk(p[4], p[5]), cvtpk(p[6], p[7])};
    pf[f] = __builtin_bit_cast(bf16x8, w);
  };
#pragma unroll
  for (int G = 0; G < 16; ++G) {
    const int i = G >> 2, t = 9 + i, kt = (G >> 1) & 1;
    if ((G & 3) == 0) {
      if (i > 0) { wait_vm<4>(); rawbar(); }
      STAMP(1 + t);
    }
    issue1(t + 3, G & 3);
    if (G + 2 < 16) ld2(G + 2); else ld3(G + 2 - 16);
    f32x16 acc;
    if (G & 1) acc = s[2 * i + kt]; else acc = f32x16{};
#pragma unroll
    for (int j = 0; j < 4; ++j) acc = MFMA(f2[G % 3][j], qf[4 * (G & 1) + j], acc);
    s[2 * i + kt] = acc;
    if (G == 8) { m1 = tilemax(0); mb = m1 * CS; }
    if (G >= 8) softfrag(G - 8);
    SCHED();
  }

  const int kq = (bid & 7) >> 1;
  const int tile0 = (bid & 1) ? 32 + (bid >> 3) : (bid >> 3);
  const bool activeB = tile0 < 63;
  const int tile = activeB ? tile0 : 0;
  const int tm = tile / 9, tn = tile % 9;
  const char* wob = (const char*)wo + ((size_t)kq * 1024 * DM + tn * 64) * 2;
  unsigned vwo;
  {
    const int row = 8 * wid + (lane >> 3);
    const int c = (lane & 7) ^ (((row >> 1) & 1) << 2);
    vwo = (unsigned)(row * (DM * 2) + c * 16);
  }
  auto issueW = [&](int t) { glds16(wob + (size_t)t * 64 * DM * 2, vwo, wo_slot(t) + wid * 1024); };

  f32x16 oa[4] = {};
  float mx = 0.f;
#pragma unroll
  for (int G = 0; G < 16; ++G) {
    const int i = G >> 2, t = 13 + i;
    if ((G & 3) == 0) {
      if (i < 2) wait_vm<4>(); else if (i == 2) wait_vm<4>();
      if (i < 3) rawbar();
      STAMP(1 + t);
    }
    issue1(t + 3, G & 3);
    if (i == 1) issueW(G & 3);
    if (G + 2 < 16) ld3(G + 2);
    if (G == 8) {
      const float al = __builtin_amdgcn_exp2f((m1 - mx) * CS);
#pragma unroll
      for (int dt = 0; dt < 4; ++dt)
#pragma unroll
        for (int r = 0; r < 16; ++r) oa[dt][r] *= al;
    }
#pragma unroll
    for (int dt = 0; dt < 4; ++dt) oa[dt] = MFMA(f2[(G + 1) % 3][dt], pf[G], oa[dt]);
    if (G == 0) {
      mx = fmaxf(m1, tilemax(4)); mb = mx * CS;
      const float al = __builtin_amdgcn_exp2f((m1 - mx) * CS);
      ls0 *= al; ls1 *= al;
    }
    if (G < 8) softfrag(8 + G);
    SCHED();
  }
  float ls = ls0 + ls1;
  {
    auto rr = __builtin_amdgcn_permlane32_swap(__float_as_uint(ls), __float_as_uint(ls), false, false);
    ls = __uint_as_float(rr[0]) + __uint_as_float(rr[1]);
  }
  const unsigned long long tag = ((hw_dispatch_id() + 1ull) * 0x9E3779B97F4A7C15ull) ^ (unsigned long long)(uintptr_t)__builtin_amdgcn_dispatch_ptr();
  gu64* flagA = (gu64*)flags;
  gu64* flagB = (gu64*)flags + 512;
  bar();
  STAMP(18); CSTAMP(23);
  issueW(4);
  {
    constexpr unsigned MLB = 64u * 1024u, OST = 72u * 1024u;
    const unsigned ow = wid * 8192 + lane * 16;
    f32x16 keep[2], send[2];
    if (kh == 0) { keep[0] = oa[0]; keep[1] = oa[1]; send[0] = oa[2]; send[1] = oa[3]; }
    else         { keep[0] = oa[2]; keep[1] = oa[3]; send[0] = oa[0]; send[1] = oa[1]; }
#pragma unroll
    for (int dl = 0; dl < 2; ++dl) {
      const f32x16 v = send[dl];
#pragma unroll
      for (int g = 0; g < 4; ++g) {
        f32x4 w = {v[4 * g], v[4 * g + 1], v[4 * g + 2], v[4 * g + 3]};
        *(LDSP f32x4*)(uintptr_t)(ow + (dl * 4 + g) * 1024) = w;
      }
    }
    f32x2 ml = {mx, ls};
    *(LDSP f32x2*)(uintptr_t)(MLB + wid * 512 + lane * 8) = ml;
    bar();
    const f32x2 pml = *(const LDSP f32x2*)(uintptr_t)(MLB + (wid ^ 4) * 512 + lane * 8);
    const float mt = fmaxf(mx, pml[0]);
    const float f0 = __builtin_amdgcn_exp2f((mx - mt) * CS), f1_ = __builtin_amdgcn_exp2f((pml[0] - mt) * CS);
    const float inv = 1.f / (f0 * ls + f1_ * pml[1]);
    const float g0 = f0 * inv, g1 = f1_ * inv;
    const unsigned orr = (wid ^ 4) * 8192 + lane * 16;
    const int qrow = 32 * rg + l31;
#pragma unroll
    for (int dl = 0; dl < 2; ++dl) {
      const f32x16 v = keep[dl];
      float r[16];
#pragma unroll
      for (int g = 0; g < 4; ++g) {
        const f32x4 w = *(const LDSP f32x4*)(uintptr_t)(orr + (dl * 4 + g) * 1024);
#pragma unroll
        for (int e = 0; e < 4; ++e) r[4 * g + e] = v[4 * g + e] * g0 + w[e] * g1;
      }
      const u32x4 w0 = {cvtpk(r[0], r[1]), cvtpk(r[2], r[3]), cvtpk(r[4], r[5]), cvtpk(r[6], r[7])};
      const u32x4 w1 = {cvtpk(r[8], r[9]), cvtpk(r[10], r[11]), cvtpk(r[12], r[13]), cvtpk(r[14], r[15])};
      const int c0 = kh * 8 + dl * 4 + h * 2;
      *(LDSP u32x4*)(uintptr_t)(OST + qrow * 256 + ((c0 ^ (qrow & 15)) * 16)) = w0;
      *(LDSP u32x4*)(uintptr_t)(OST + qrow * 256 + (((c0 + 1) ^ (qrow & 15)) * 16)) = w1;
    }
    bar();
    const __amdgpu_buffer_rsrc_t rs = __builtin_amdgcn_make_buffer_rsrc((char*)o_ws + ((size_t)hq * MROWS + b * SQ) * (DH * 2), 0, SQ * DH * 2, 0x00020000);
#pragma unroll
    for (int k = 0; k < 4; ++k) {
      const int i = wid + 8 * k;
      if (i < 28) {
        const int row = 4 * i + (lane >> 4), pz = lane & 15;
        const u32x4 w = *(const LDSP u32x4*)(uintptr_t)(OST + i * 1024 + lane * 16);
        __builtin_amdgcn_raw_buffer_store_b128(w, rs, row * 256 + ((pz ^ (row & 15)) * 16), 0, 16);
      }
    }
  }
  asm volatile("s_waitcnt vmcnt(0)" ::: "memory");
  __syncthreads();
  if (tid == 0) __hip_atomic_store(flagA + (b * 4 + (hq >> 3)) * 8 + (hq & 7), tag, __ATOMIC_RELAXED, __HIP_MEMORY_SCOPE_AGENT);
  STAMP(19); CSTAMP(21);
  if (!activeB) { STAMP_FLUSH; return; }

  {
    const int mt = wid & 3, nh = wid >> 2;
    unsigned vo[2];
#pragma unroll
    for (int e = 0; e < 2; ++e) {
      const int row = 8 * (2 * wid + e) + (lane >> 3);
      const int c = (lane & 7) ^ ((row >> 1) & 7);
      vo[e] = (unsigned)(row * (DH * 2) + c * 16);
    }
    const char* ob = (const char*)o_ws + ((size_t)kq * 8 * MROWS + tm * 128) * (DH * 2);
    auto issueO = [&](int t, int e) {
      const char* ot = ob + (size_t)(t >> 1) * MROWS * (DH * 2) + (t & 1) * 128;
      glds16sc1(ot, vo[e], (unsigned)(t % 6) * 16384u + (2 * wid + e) * 1024);
    };
    const int q4 = (lane & 15) >> 2, p4 = lane & 3, g16 = (lane >> 4) & 1;
    unsigned ab[4], aw[2];
#pragma unroll
    for (int j = 0; j < 4; ++j) ab[j] = (unsigned)((32 * mt + l31) * 128 + (((2 * j + h) ^ ((l31 >> 1) & 7)) * 16));
#pragma unroll
    for (int t = 0; t < 2; ++t) {
      const int k = 8 * h + 4 * t + q4;
      const int c = (4 * nh + 2 * (p4 & 1) + g16) ^ (((q4 >> 1) & 1) << 2);
      aw[t] = (unsigned)(k * 128 + c * 16 + (p4 >> 1) * 8);
    }
    if (wid == 0) {
      const int b0 = (tm * 128) / SQ, b1 = (tm * 128 + 127) / SQ;
      wait_flags(flagA + (((lane & 8) ? b1 : b0) * 4 + kq) * 8 + (lane & 7), lane < 16, tag);
    }
    rawbar();
    STAMP(26);
#pragma unroll
    for (int t = 0; t < 5; ++t) { issueO(t, 0); issueO(t, 1); }

    bf16x8 fa[2][4], fb[2][4];
    auto ld = [&](int t, int j) {
      fb[t & 1][j] = lds_b128((unsigned)(t % 6) * 16384u + ab[j]);
      fa[t & 1][j] = lds_tr2(wo_slot(t) + aw[0] + j * 2048, wo_slot(t) + aw[1] + j * 2048);
    };
    wait_vm<8>(); rawbar();
    STAMP(27);
#pragma unroll
    for (int j = 0; j < 4; ++j) ld(0, j);
    SCHED();
    f32x16 acc = {};
#pragma unroll
    for (int t = 0; t < 16; ++t) {
      if (t == 0) wait_vm<6>(); else if (t == 1) wait_vm<7>(); else if (t == 2) wait_vm<8>(); else if (t <= 11) wait_vm<9>();
      else if (t == 12) wait_vm<6>(); else if (t == 13) wait_vm<3>(); else if (t == 14) wait_vm<0>();
      if (t < 15) rawbar();
#pragma unroll
      for (int j = 0; j < 4; ++j) {
        if (t + 5 < 16) { if (j < 2) issueO(t + 5, j); else if (j == 2) issueW(t + 5); }
        if (t + 1 < 16) ld(t + 1, j);
        acc = MFMA(fa[t & 1][j], fb[t & 1][j], acc);
        SCHED();
      }
    }
    STAMP(24);
    float* sl = slab + (size_t)tile * 4 * 4 * 2 * 1024;
    if (mt != kq) {
      const __amdgpu_buffer_rsrc_t rs = __builtin_amdgcn_make_buffer_rsrc((void*)(sl + ((mt * 4 + kq) * 2 + nh) * 1024), 0, 4096, 0x00020000);
#pragma unroll
      for (int g = 0; g < 4; ++g) {
        f32x4 w = {acc[4 * g], acc[4 * g + 1], acc[4 * g + 2], acc[4 * g + 3]};
        __builtin_amdgcn_raw_buffer_store_b128(__builtin_bit_cast(u32x4, w), rs, g * 1024 + lane * 16, 0, 16);
      }
    }
    asm volatile("s_waitcnt vmcnt(0)" ::: "memory");
    __syncthreads();
    if (wid == 0 && lane < 4 && lane != kq)
      __hip_atomic_store(flagB + (tile * 4 + lane) * 4 + kq, tag, __ATOMIC_RELAXED, __HIP_MEMORY_SCOPE_AGENT);
    if (mt == kq) {
      wait_flags(flagB + (tile * 4 + kq) * 4 + (lane & 3), lane < 4 && lane != kq, tag);
      u32x4 pv[3][4];
#pragma unroll
      for (int o = 0; o < 3; ++o) {
        const int src = o + (o >= kq);
        const __amdgpu_buffer_rsrc_t rs = __builtin_amdgcn_make_buffer_rsrc((void*)(sl + ((kq * 4 + src) * 2 + nh) * 1024), 0, 4096, 0x00020000);
#pragma unroll
        for (int g = 0; g < 4; ++g) pv[o][g] = __builtin_amdgcn_raw_buffer_load_b128(rs, g * 1024 + lane * 16, 0, 16);
      }
      float r[16];
#pragma unroll
      for (int g = 0; g < 4; ++g) {
        const f32x4 a = __builtin_bit_cast(f32x4, pv[0][g]), c = __builtin_bit_cast(f32x4, pv[1][g]), d = __builtin_bit_cast(f32x4, pv[2][g]);
#pragma unroll
        for (int e = 0; e < 4; ++e) {
          const float own = acc[4 * g + e];
          const float t0 = kq == 0 ? own : a[e];
          const float t1 = kq == 0 ? a[e] : (kq == 1 ? own : c[e]);
          const float t2 = kq <= 1 ? c[e] : (kq == 2 ? own : d[e]);
          const float t3 = kq == 3 ? own : d[e];
          r[4 * g + e] = ((t0 + t1) + t2) + t3;
        }
      }
      char* dst = (char*)out + ((size_t)(tm * 128 + 32 * mt + l31) * DM + tn * 64 + nh * 32 + 16 * h) * 2;
      u32x4 w0 = {cvtpk(r[0], r[1]), cvtpk(r[2], r[3]), cvtpk(r[4], r[5]), cvtpk(r[6], r[7])};
      u32x4 w1 = {cvtpk(r[8], r[9]), cvtpk(r[10], r[11]), cvtpk(r[12], r[13]), cvtpk(r[14], r[15])};
      *(u32x4*)dst = w0;
      *(u32x4*)(dst + 16) = w1;
    }
  }
  STAMP(25);
  STAMP_FLUSH;
}

extern "C" void kernel_launch(void* const* d_in, const int* in_sizes, int n_in, void* d_out, int out_size, void* d_ws,
                              size_t ws_size, hipStream_t stream) {
  const __hip_bfloat16* x = (const __hip_bfloat16*)d_in[0];
  const __hip_bfloat16* kc = (const __hip_bfloat16*)d_in[1];
  const __hip_bfloat16* vc = (const __hip_bfloat16*)d_in[2];
  const __hip_bfloat16* wq = (const __hip_bfloat16*)d_in[3];
  const __hip_bfloat16* wo = (const __hip_bfloat16*)d_in[4];
  __hip_bfloat16* o_ws = (__hip_bfloat16*)d_ws;
  float* slab = (float*)((char*)d_ws + (size_t)8 * 1024 * 1024);
  unsigned long long* flags = (unsigned long long*)((char*)d_ws + (size_t)24 * 1024 * 1024);
  hipFuncSetAttribute((const void*)k_fused, hipFuncAttributeMaxDynamicSharedMemorySize, K1_LDS);
  hipLaunchKernelGGL(k_fused, dim3(256), dim3(512), K1_LDS, stream, x, kc, vc, wq, wo, o_ws, slab, flags, (__hip_bfloat16*)d_out);
}
```

```cpp
#include <hip/hip_runtime.h>
#include <hip/hip_bf16.h>
#include <stdint.h>

typedef __attribute__((ext_vector_type(8))) __bf16 bf16x8;
typedef __attribute__((ext_vector_type(4))) __bf16 bf16x4;
typedef __attribute__((ext_vector_type(2))) __bf16 bf16x2;
typedef __attribute__((ext_vector_type(16))) float f32x16;
typedef __attribute__((ext_vector_type(4))) float f32x4;
typedef __attribute__((ext_vector_type(2))) float f32x2;
typedef __attribute__((ext_vector_type(4))) unsigned u32x4;
#define LDSP __attribute__((address_space(3)))
#define DI __device__ __forceinline__

constexpr int NB = 8, SQ = 112, DM = 576, HQ = 32, HKV = 16, SK = 512, DH = 128;
constexpr int MROWS = NB * SQ;
constexpr int KO = HQ * DH;

DI unsigned cvtpk(float lo, float hi) {
  f32x2 v = {lo, hi};
  bf16x2 b = __builtin_convertvector(v, bf16x2);
  return __builtin_bit_cast(unsigned, b);
}
DI void glds16(const void* sbase, unsigned voff, unsigned lds_dst) {
  unsigned keep;
  asm volatile("s_mov_b32 %0, m0\n\ts_mov_b32 m0, %3\n\ts_nop 0\n\tglobal_load_lds_dwordx4 %1, %2\n\ts_mov_b32 m0, %0"
               : "=&s"(keep) : "v"(voff), "s"(sbase), "s"(lds_dst) : "memory");
}
DI void glds16nt(const void* sbase, unsigned voff, unsigned lds_dst) {
  unsigned keep;
  asm volatile("s_mov_b32 %0, m0\n\ts_mov_b32 m0, %3\n\ts_nop 0\n\tglobal_load_lds_dwordx4 %1, %2 nt\n\ts_mov_b32 m0, %0"
               : "=&s"(keep) : "v"(voff), "s"(sbase), "s"(lds_dst) : "memory");
}
DI void glds16sc1(const void* sbase, unsigned voff, unsigned lds_dst) {
  unsigned keep;
  asm volatile("s_mov_b32 %0, m0\n\ts_mov_b32 m0, %3\n\ts_nop 0\n\tglobal_load_lds_dwordx4 %1, %2 sc1\n\ts_mov_b32 m0, %0"
               : "=&s"(keep) : "v"(voff), "s"(sbase), "s"(lds_dst) : "memory");
}
template <int J> DI void glds16_if(const void* sbase, unsigned voff, unsigned lds_dst, int sel) {
  unsigned keep;
  asm volatile("s_cmp_lg_u32 %4, %5\n\ts_cbranch_scc1 .Lgl%=\n\ts_mov_b32 %0, m0\n\ts_mov_b32 m0, %3\n\ts_nop 0\n\tglobal_load_lds_dwordx4 %1, %2\n\ts_mov_b32 m0, %0\n.Lgl%=:"
               : "=&s"(keep) : "v"(voff), "s"(sbase), "s"(lds_dst), "s"(sel), "i"(J) : "memory", "scc");
}
template <int J> DI void glds16nt_if(const void* sbase, unsigned voff, unsigned lds_dst, int sel) {
  unsigned keep;
  asm volatile("s_cmp_lg_u32 %4, %5\n\ts_cbranch_scc1 .Lgl%=\n\ts_mov_b32 %0, m0\n\ts_mov_b32 m0, %3\n\ts_nop 0\n\tglobal_load_lds_dwordx4 %1, %2 nt\n\ts_mov_b32 m0, %0\n.Lgl%=:"
               : "=&s"(keep) : "v"(voff), "s"(sbase), "s"(lds_dst), "s"(sel), "i"(J) : "memory", "scc");
}
template <bool NT, int J> DI void gl_t(const void* sbase, unsigned voff, unsigned lds_dst, int sel) {
  if constexpr (J < 0) { if constexpr (NT) glds16nt(sbase, voff, lds_dst); else glds16(sbase, voff, lds_dst); }
  else { if constexpr (NT) glds16nt_if<J>(sbase, voff, lds_dst, sel); else glds16_if<J>(sbase, voff, lds_dst, sel); }
}
template <int V> struct IC { static constexpr int value = V; };
template <int I, int N, class F> DI void sfor(F&& f) { if constexpr (I < N) { f(IC<I>{}); sfor<I + 1, N>(f); } }
DI void gl_sel(bool nt, int j, const void* sbase, unsigned voff, unsigned lds_dst, int sel) {
  if (nt) {
    if (j < 0) glds16nt(sbase, voff, lds_dst);
    else if (j == 0) glds16nt_if<0>(sbase, voff, lds_dst, sel); else if (j == 1) glds16nt_if<1>(sbase, voff, lds_dst, sel);
    else if (j == 2) glds16nt_if<2>(sbase, voff, lds_dst, sel); else glds16nt_if<3>(sbase, voff, lds_dst, sel);
  } else {
    if (j < 0) glds16(sbase, voff, lds_dst);
    else if (j == 0) glds16_if<0>(sbase, voff, lds_dst, sel); else if (j == 1) glds16_if<1>(sbase, voff, lds_dst, sel);
    else if (j == 2) glds16_if<2>(sbase, voff, lds_dst, sel); else glds16_if<3>(sbase, voff, lds_dst, sel);
  }
}
DI void set_m0(unsigned base, unsigned imm) { asm volatile("s_add_u32 m0, %0, %1\n\ts_nop 0" ::"s"(base), "i"(imm) : "scc"); }
template <int OFF, int POL> DI void gld(const void* sbase, unsigned voff) {
  if constexpr (POL == 0) asm volatile("global_load_lds_dwordx4 %0, %1 offset:%2" ::"v"(voff), "s"(sbase), "i"(OFF) : "memory");
  else if constexpr (POL == 1) asm volatile("global_load_lds_dwordx4 %0, %1 offset:%2 nt" ::"v"(voff), "s"(sbase), "i"(OFF) : "memory");
  else asm volatile("global_load_lds_dwordx4 %0, %1 offset:%2 sc1" ::"v"(voff), "s"(sbase), "i"(OFF) : "memory");
}
template <int N> DI void wait_lgkm() { __builtin_amdgcn_s_waitcnt(0xC07F | (N << 8)); }
#define GL_KV glds16nt
#define GL_XW glds16
#define GL_WO glds16
#define GL_O glds16
template <int N> DI void wait_vm() { asm volatile("s_waitcnt vmcnt(%0)" ::"i"(N) : "memory"); }
DI void bar() { asm volatile("s_waitcnt lgkmcnt(0)\n\ts_barrier" ::: "memory"); }
DI bf16x8 lds_b128(unsigned a) { return *(const LDSP bf16x8*)(uintptr_t)a; }
DI bf16x8 lds_tr2(unsigned a0, unsigned a1) {
  bf16x4 lo = __builtin_amdgcn_ds_read_tr16_b64_v4bf16((LDSP bf16x4*)(uintptr_t)a0);
  bf16x4 hi = __builtin_amdgcn_ds_read_tr16_b64_v4bf16((LDSP bf16x4*)(uintptr_t)a1);
  return __builtin_shufflevector(lo, hi, 0, 1, 2, 3, 4, 5, 6, 7);
}
DI bf16x8 pack8(const f32x16& x, int s) {
  u32x4 w = {cvtpk(x[8 * s], x[8 * s + 1]), cvtpk(x[8 * s + 2], x[8 * s + 3]), cvtpk(x[8 * s + 4], x[8 * s + 5]),
             cvtpk(x[8 * s + 6], x[8 * s + 7])};
  return __builtin_bit_cast(bf16x8, w);
}
#define STAMP(i)
#define CSTAMP(i)
#define STAMP_DECL
#define STAMP_FLUSH
#define STAMP2(i)
#define CSTAMP2(i)
#define STAMP_DECL2
#define STAMP_FLUSH2
#define MFMA(a, b, c) __builtin_amdgcn_mfma_f32_32x32x16_bf16((a), (b), (c), 0, 0, 0)

constexpr unsigned SUB = 16384u;
constexpr unsigned RGB = 65536u;
constexpr unsigned MISC = 131072u;
#define STAG1 0
#define STAG2 0
#define WO4 ((wid & 1) + 2 * (wid >> 2))
#define PIPE_MU 1
#define NT1 3
struct SmSched {
  int mu[129];
  int tm[128];
  int rs[128];
  constexpr SmSched() : mu{}, tm{}, rs{} {
    int n[128] = {};
    for (int m = 0; m < 128; ++m) { tm[m] = -1; rs[m] = -1; }
    for (int j = 0; j < 8; ++j) tm[8 * j + ((j == NT1 - 1 || j == 7) ? 9 : 10)] = j;
    const int sw = 64 + 8 * NT1;
    for (int d = 0; d < 4; ++d) rs[sw - 2 + d] = d;
    {
      const int a = 8 * NT1 + 3, e = 64; int av = 0, acc = 0;
      for (int m = a; m < e; ++m) if (tm[m] < 0) ++av;
      for (int m = a; m < e; ++m) if (tm[m] < 0) { acc += 8 * NT1; if (acc >= av) { acc -= av; ++n[m]; } }
    }
    {
      const int a = 67, e = 123; int av = 0, acc = 0;
      for (int m = a; m < e; ++m) if (rs[m] < 0) ++av;
      for (int m = a; m < e; ++m) if (rs[m] < 0) { acc += 64 - 8 * NT1; if (acc >= av) { acc -= av; ++n[m]; } }
    }
    int k = 0;
    for (int m = 0; m < 128; ++m) { mu[m] = k; k += n[m]; }
    mu[128] = k;
  }
  constexpr bool ok() const {
    if (mu[128] != 64) return false;
    for (int m = 0; m < 128; ++m)
      for (int k = mu[m]; k < mu[m + 1]; ++k) if (m + 1 >= 64 + 4 * (k >> 2)) return false;
    return true;
  }
};
constexpr SmSched SM{};
static_assert(SM.ok(), "softmax schedule misses a deadline");
constexpr int K1_LDS = 160 * 1024;
#define SCHED() __builtin_amdgcn_sched_barrier(0)
DI void rawbar() { asm volatile("s_barrier" ::: "memory"); }

typedef __attribute__((address_space(1))) unsigned long long gu64;
extern "C" __device__ unsigned long long hw_dispatch_id() __asm("llvm.amdgcn.dispatch.id");
DI constexpr unsigned wo_slot(int t) {
  constexpr unsigned pb[8] = {128u, 144u, 16u, 80u, 0u, 64u, 32u, 48u};
  return pb[t >> 1] * 1024u + (unsigned)(t & 1) * 8192u;
}
DI void wait_flags(gu64* f, bool on, unsigned long long tag) {
  for (unsigned spins = 0; spins < (1u << 20); ++spins) {
    const unsigned long long v = on ? __hip_atomic_load(f, __ATOMIC_RELAXED, __HIP_MEMORY_SCOPE_AGENT) : tag;
    if (__all(v == tag)) break;
    __builtin_amdgcn_s_sleep(1);
  }
  asm volatile("" ::: "memory");
}

__global__ __launch_bounds__(512, 2) void k_fused(const __hip_bfloat16* __restrict__ x, const __hip_bfloat16* __restrict__ kc,
                                                   const __hip_bfloat16* __restrict__ vc, const __hip_bfloat16* __restrict__ wq,
                                                   const __hip_bfloat16* __restrict__ wo, __hip_bfloat16* o_ws,
                                                   float* slab, unsigned long long* flags,
                                                   __hip_bfloat16* __restrict__ out) {
  extern __shared__ __attribute__((aligned(16))) char lds[];
  const int tid = threadIdx.x, lane = tid & 63;
  const int wid = __builtin_amdgcn_readfirstlane(tid >> 6);
  const int rg = wid & 3, kh = wid >> 2;
  const int l31 = lane & 31, h = lane >> 5;
  const int bid = blockIdx.x;
  const int hq = 4 * (bid & 7) + ((bid >> 3) & 3), b = bid >> 5;
  const int hkv = hq >> 1;
  STAMP_DECL;

  unsigned vx[2], vw[2], vkv[4];
#pragma unroll
  for (int e = 0; e < 2; ++e) {
    const int i = 2 * wid + e;
    int row = 8 * i + (lane >> 3);
    const int c = (lane & 7) ^ ((row >> 1) & 7);
    row = row < SQ ? row : SQ - 1;
    vx[e] = (unsigned)(row * (DM * 2) + c * 16 - e * 1024 + 1024);
    const int rw = 4 * i + (lane >> 4);
    const int cw = (lane & 15) ^ (((rw & 3) << 2) | ((rw >> 2) & 3));
    vw[e] = (unsigned)(rw * (KO * 2) + cw * 16 - e * 1024 + 1024);
  }
#pragma unroll
  for (int e = 0; e < 4; ++e) {
    const int r = 4 * (4 * (wid & 3) + e) + (lane >> 4);
    const int c = (lane & 15) ^ (((r & 3) << 2) | ((r >> 2) & 3));
    vkv[e] = (unsigned)((r - 4 * e) * 256 + c * 16);
  }
  const char* xb = (const char*)x + (size_t)b * SQ * DM * 2 - 1024;
  const char* wb = (const char*)wq + (size_t)hq * DH * 2 - 1024;
  const unsigned dX = (unsigned)wid * 2048u, dK = (unsigned)(kh * RGB + rg * 4096), dW = (unsigned)wid * 1024u;
  const size_t kvo = ((size_t)(b * HKV + hkv) * SK + 256 * kh) * DH * 2;
  const char* kb = (const char*)kc + kvo;
  const char* vb = (const char*)vc + kvo;

  auto issue1 = [&](auto Tc, auto Ec) {
    constexpr int t = decltype(Tc)::value, e = decltype(Ec)::value;
    constexpr unsigned sb = (unsigned)(t & 3) * SUB;
    if constexpr (t < 9) {
      if constexpr (e == 0) set_m0(dX, sb);
      if constexpr (e == 2) set_m0(dX, RGB + sb);
      if constexpr (e < 2) gld<e * 1024, 0>(xb + t * 128, vx[e]);
      else                 gld<(e - 2) * 1024, 0>(wb + (size_t)t * 64 * KO * 2, vw[e - 2]);
    } else if constexpr (t < 17) {
      if constexpr (e == 0) set_m0(dK, sb);
      if constexpr (t < 13) gld<e * 1024, 1>(kb + (t - 9) * 16384, vkv[e]);
      else                  gld<e * 1024, 1>(vb + (t - 13) * 16384, vkv[e]);
    }
  };
  STAMP(0); CSTAMP(20);
  sfor<0, 3>([&](auto Tc) { sfor<0, 4>([&](auto Ec) { issue1(Tc, Ec); }); });

  const int q4 = (lane & 15) >> 2, p4 = lane & 3, g16 = (lane >> 4) & 1;
  unsigned ax[4];
#pragma unroll
  for (int ks = 0; ks < 4; ++ks) ax[ks] = (unsigned)((32 * rg + l31) * 128 + (((2 * ks + h) ^ ((l31 >> 1) & 7)) * 16));
  unsigned aw[2][2];
#pragma unroll
  for (int dl = 0; dl < 2; ++dl)
#pragma unroll
    for (int t = 0; t < 2; ++t) {
      const int dt = 2 * kh + dl;
      const int k = 8 * h + 4 * t + q4;
      const int c = (4 * dt + 2 * g16 + (p4 & 1)) ^ ((q4 << 2) | ((2 * h + t) & 3));
      aw[dl][t] = (unsigned)(RGB + k * 256 + c * 16 + (p4 >> 1) * 8);
    }
  unsigned ak[8];
#pragma unroll
  for (int ks = 0; ks < 8; ++ks)
    ak[ks] = (unsigned)(kh * RGB + l31 * 256 + (((2 * ks + h) ^ (((l31 & 3) << 2) | ((l31 >> 2) & 3))) * 16));
  unsigned av[4][2];
#pragma unroll
  for (int dt = 0; dt < 4; ++dt)
#pragma unroll
    for (int t = 0; t < 2; ++t) {
      const int key = 8 * t + 4 * h + q4;
      const int c = (4 * dt + 2 * (p4 & 1) + g16) ^ ((q4 << 2) | ((2 * t + h) & 3));
      av[dt][t] = (unsigned)(kh * RGB + key * 256 + c * 16 + (p4 >> 1) * 8);
    }

  bf16x8 f1[3][3];
  auto ld1 = [&](int G) {
    const unsigned sb = (unsigned)((G >> 2) & 3) * SUB; const int ks = G & 3, r = G % 3;
    f1[r][0] = lds_b128(sb + ax[ks]);
    f1[r][1] = lds_tr2(sb + aw[0][0] + ks * 4096, sb + aw[0][1] + ks * 4096);
    f1[r][2] = lds_tr2(sb + aw[1][0] + ks * 4096, sb + aw[1][1] + ks * 4096);
  };
  bf16x8 f2[3][4];
  auto ld2 = [&](int G) {
    const unsigned sb = (unsigned)((9 + (G >> 2)) & 3) * SUB + ((G >> 1) & 1) * 8192; const int r = G % 3;
#pragma unroll
    for (int j = 0; j < 4; ++j) f2[r][j] = lds_b128(sb + ak[4 * (G & 1) + j]);
  };
  auto ld3 = [&](int G) {
    const unsigned sb = (unsigned)((13 + (G >> 2)) & 3) * SUB + ((G >> 1) & 1) * 8192 + (G & 1) * 4096; const int r = (G + 1) % 3;
#pragma unroll
    for (int dt = 0; dt < 4; ++dt) f2[r][dt] = lds_tr2(sb + av[dt][0], sb + av[dt][1]);
  };

  wait_vm<8>(); rawbar();
  ld1(0); ld1(1);
  SCHED();

  f32x16 qa[2] = {};
  sfor<0, 9>([&](auto Tc) {
    constexpr int t = decltype(Tc)::value;
    wait_vm<4>(); rawbar();
    STAMP(1 + t);
    sfor<0, 4>([&](auto Kc) {
      constexpr int ks = decltype(Kc)::value;
      constexpr int G = 4 * t + ks;
      issue1(IC<t + 3>{}, Kc);
      wait_lgkm<5>();
      if constexpr (G + 2 < 36) ld1(G + 2); else ld2(G + 2 - 36);
      qa[0] = MFMA(f1[G % 3][1], f1[G % 3][0], qa[0]);
      qa[1] = MFMA(f1[G % 3][2], f1[G % 3][0], qa[1]);
      SCHED();
    });
  });
  bf16x8 qf[8];
  {
    bf16x8 own[4] = {pack8(qa[0], 0), pack8(qa[0], 1), pack8(qa[1], 0), pack8(qa[1], 1)};
    const unsigned qw = MISC + wid * 4096 + lane * 16;
#pragma unroll
    for (int f = 0; f < 4; ++f) *(LDSP bf16x8*)(uintptr_t)(qw + f * 1024) = own[f];
    wait_vm<4>(); bar();
    const unsigned qr = MISC + (wid ^ 4) * 4096 + lane * 16;
    if (kh == 0) {
#pragma unroll
      for (int f = 0; f < 4; ++f) { qf[f] = own[f]; qf[4 + f] = lds_b128(qr + f * 1024); }
    } else {
#pragma unroll
      for (int f = 0; f < 4; ++f) { qf[4 + f] = own[f]; qf[f] = lds_b128(qr + f * 1024); }
    }
  }
  SCHED();
  CSTAMP(22);
  const float CS = 0.08838834764831845f * 1.4426950408889634f;
  const int wo4 = WO4;
  f32x16 s[8];
  unsigned pw[16][4];
  float m1 = 0.f, mx = 0.f, mb = 0.f, ls0 = 0.f, ls1 = 0.f, al = 0.f;
  float rm0 = -__builtin_inff(), rm1 = -__builtin_inff();

  const int kq = (bid & 7) >> 1;
  const int tile0 = (bid & 1) ? 32 + (bid >> 3) : (bid >> 3);
  const bool activeB = tile0 < 63;
  const int tile = activeB ? tile0 : 0;
  const int tm = tile / 9, tn = tile % 9;
  const char* wob = (const char*)wo + ((size_t)kq * 1024 * DM + tn * 64) * 2;
  unsigned vwo;
  {
    const int row = 8 * wid + (lane >> 3);
    const int c = (lane & 7) ^ (((row >> 1) & 1) << 2);
    vwo = (unsigned)(row * (DM * 2) + c * 16);
  }
  auto issueWr = [&](int t) { set_m0(dW, wo_slot(t)); gld<0, 0>(wob + (size_t)t * 64 * DM * 2, vwo); };

  f32x16 oa[4];
  float pe[2][2];
  auto vwork = [&](auto Mc) {
    constexpr int m = decltype(Mc)::value;
    if constexpr (SM.tm[m] >= 0) {
      constexpr int j = SM.tm[m];
#pragma unroll
      for (int r = 0; r < 16; r += 4) {
        rm0 = fmaxf(fmaxf(rm0, s[j][r]), s[j][r + 1]);
        rm1 = fmaxf(fmaxf(rm1, s[j][r + 2]), s[j][r + 3]);
      }
      if constexpr (j == NT1 - 1 || j == 7) {
        float m_ = fmaxf(rm0, rm1);
        auto rr = __builtin_amdgcn_permlane32_swap(__float_as_uint(m_), __float_as_uint(m_), false, false);
        m_ = fmaxf(__uint_as_float(rr[0]), __uint_as_float(rr[1]));
        if constexpr (j == 7) {
          mx = m_; mb = mx * CS;
          al = __builtin_amdgcn_exp2f((m1 - mx) * CS);
          ls0 *= al; ls1 *= al;
        } else { m1 = m_; mb = m1 * CS; }
      }
    }
    if constexpr (SM.rs[m] >= 0) {
      constexpr int d = SM.rs[m];
#pragma unroll
      for (int r = 0; r < 16; ++r) oa[d][r] *= al;
    }
    if constexpr (m > 0) {
      sfor<SM.mu[m - 1], SM.mu[m]>([&](auto Kc) {
        constexpr int k = decltype(Kc)::value, f = k >> 2, q = k & 3;
        ls0 += pe[k & 1][0]; ls1 += pe[k & 1][1];
        pw[f][q] = cvtpk(pe[k & 1][0], pe[k & 1][1]);
      });
    }
    sfor<SM.mu[m], SM.mu[m + 1]>([&](auto Kc) {
      constexpr int k = decltype(Kc)::value, f = k >> 2, q = k & 3;
      pe[k & 1][0] = __builtin_amdgcn_exp2f(fmaf(s[f >> 1][8 * (f & 1) + 2 * q], CS, -mb));
      pe[k & 1][1] = __builtin_amdgcn_exp2f(fmaf(s[f >> 1][8 * (f & 1) + 2 * q + 1], CS, -mb));
    });
  };
  sfor<0, 16>([&](auto Gc) {
    constexpr int G = decltype(Gc)::value, i = G >> 2, t = 9 + i, kt = (G >> 1) & 1;
    if constexpr ((G & 3) == 0) {
      if constexpr (i > 0) { wait_vm<4>(); rawbar(); }
      STAMP(1 + t);
    }
    issue1(IC<t + 3>{}, IC<(G & 3)>{});
    if constexpr (G == 15) wait_lgkm<8>(); else if constexpr (G > 0) wait_lgkm<4>();
    if constexpr (G + 2 < 16) ld2(G + 2); else ld3(G + 2 - 16);
    sfor<0, 4>([&](auto Jc) {
      constexpr int j = decltype(Jc)::value;
      if constexpr ((G & 1) == 0 && j == 0) s[2 * i + kt] = MFMA(f2[G % 3][0], qf[0], f32x16{});
      else s[2 * i + kt] = MFMA(f2[G % 3][j], qf[4 * (G & 1) + j], s[2 * i + kt]);
      vwork(IC<4 * G + j>{});
      SCHED();
    });
  });
  sfor<0, 16>([&](auto Gc) {
    constexpr int G = decltype(Gc)::value, i = G >> 2, t = 13 + i;
    if constexpr ((G & 3) == 0) {
      if constexpr (i < 3) { wait_vm<4>(); rawbar(); }
      STAMP(1 + t);
    }
    issue1(IC<t + 3>{}, IC<(G & 3)>{});
    if constexpr (i == 1 || i == 2) issueWr(4 * (i - 1) + (G & 3));
    if constexpr (G < 15) wait_lgkm<8>(); else wait_lgkm<0>();
    if constexpr (G + 2 < 16) ld3(G + 2);
    const u32x4 pwv = {pw[G][0], pw[G][1], pw[G][2], pw[G][3]};
    sfor<0, 4>([&](auto Jc) {
      constexpr int dt = decltype(Jc)::value;
      if constexpr (G == 0) oa[dt] = MFMA(f2[(G + 1) % 3][dt], __builtin_bit_cast(bf16x8, pwv), f32x16{});
      else oa[dt] = MFMA(f2[(G + 1) % 3][dt], __builtin_bit_cast(bf16x8, pwv), oa[dt]);
      vwork(IC<64 + 4 * G + dt>{});
      SCHED();
    });
  });
  float ls = ls0 + ls1;
  {
    auto rr = __builtin_amdgcn_permlane32_swap(__float_as_uint(ls), __float_as_uint(ls), false, false);
    ls = __uint_as_float(rr[0]) + __uint_as_float(rr[1]);
  }
  const unsigned long long tag = ((hw_dispatch_id() + 1ull) * 0x9E3779B97F4A7C15ull) ^ (unsigned long long)(uintptr_t)__builtin_amdgcn_dispatch_ptr();
  gu64* flagA = (gu64*)flags;
  gu64* flagB = (gu64*)flags + 512;
  bar();
  STAMP(18); CSTAMP(23);
  issueWr(8); issueWr(9); issueWr(10);
  {
    constexpr unsigned MLB = 72u * 1024u;
    const unsigned xw = (unsigned)(32768 + kh * RGB + rg * 8192) + lane * 16, xr = (unsigned)(32768 + (kh ^ 1) * RGB + rg * 8192) + lane * 16;
    auto put = [&](const f32x16& v, int dl) {
#pragma unroll
      for (int g = 0; g < 4; ++g) {
        f32x4 w = {v[4 * g], v[4 * g + 1], v[4 * g + 2], v[4 * g + 3]};
        *(LDSP f32x4*)(uintptr_t)(xw + (dl * 4 + g) * 1024) = w;
      }
    };
    if (kh == 0) { put(oa[2], 0); put(oa[3], 1); } else { put(oa[0], 0); put(oa[1], 1); }
    f32x2 ml = {mx, ls};
    *(LDSP f32x2*)(uintptr_t)(MLB + wid * 512 + lane * 8) = ml;
    bar();
    STAMP2(1);
    const f32x2 pml = *(const LDSP f32x2*)(uintptr_t)(MLB + (wid ^ 4) * 512 + lane * 8);
    const float mt = fmaxf(mx, pml[0]);
    const float f0 = __builtin_amdgcn_exp2f((mx - mt) * CS), f1_ = __builtin_amdgcn_exp2f((pml[0] - mt) * CS);
    const float inv = 1.f / (f0 * ls + f1_ * pml[1]);
    const float g0 = f0 * inv, g1 = f1_ * inv;
    const int qrow = 32 * rg + l31, R = b * SQ + qrow;
    const __amdgpu_buffer_rsrc_t rs = __builtin_amdgcn_make_buffer_rsrc((char*)o_ws + (size_t)hq * MROWS * (DH * 2), 0, MROWS * DH * 2, 0x00020000);
    const int so = (R >> 5) * 8192 + (4 * kh + h) * 1024 + (R & 31) * 16;
    auto fin = [&](const f32x16& v, int dl) {
      float r[16];
#pragma unroll
      for (int g = 0; g < 4; ++g) {
        const f32x4 w = *(const LDSP f32x4*)(uintptr_t)(xr + (dl * 4 + g) * 1024);
#pragma unroll
        for (int e = 0; e < 4; ++e) r[4 * g + e] = v[4 * g + e] * g0 + w[e] * g1;
      }
      const u32x4 w0 = {cvtpk(r[0], r[1]), cvtpk(r[2], r[3]), cvtpk(r[4], r[5]), cvtpk(r[6], r[7])};
      const u32x4 w1 = {cvtpk(r[8], r[9]), cvtpk(r[10], r[11]), cvtpk(r[12], r[13]), cvtpk(r[14], r[15])};
      if (qrow < SQ) {
        __builtin_amdgcn_raw_buffer_store_b128(w0, rs, so + dl * 2048, 0, 16);
        __builtin_amdgcn_raw_buffer_store_b128(w1, rs, so + dl * 2048 + 512, 0, 16);
      }
    };
    if (kh == 0) { fin(oa[0], 0); fin(oa[1], 1); } else { fin(oa[2], 0); fin(oa[3], 1); }
  }
  STAMP2(3);
  asm volatile("s_waitcnt vmcnt(0)" ::: "memory");
  STAMP2(4);
  __syncthreads();
  STAMP2(5);
  if (tid == 0) __hip_atomic_store(flagA + (b * 4 + (hq >> 3)) * 8 + (hq & 7), tag, __ATOMIC_RELAXED, __HIP_MEMORY_SCOPE_AGENT);
  STAMP(19); CSTAMP(21);
  if (!activeB) { STAMP_FLUSH; return; }

  {
    const int mt = wid & 3, k2 = wid >> 2, nh = k2;
#pragma unroll
    for (int t = 11; t < 16; ++t) issueWr(t);
    const int q4 = (lane & 15) >> 2, p4 = lane & 3, g16 = (lane >> 4) & 1;
    unsigned aw[2][2];
#pragma unroll
    for (int n2 = 0; n2 < 2; ++n2)
#pragma unroll
      for (int t = 0; t < 2; ++t) {
        const int k = 8 * h + 4 * t + q4;
        const int c = (4 * n2 + 2 * (p4 & 1) + g16) ^ (((q4 >> 1) & 1) << 2);
        aw[n2][t] = (unsigned)(k2 * 8192 + k * 128 + c * 16 + (p4 >> 1) * 8);
      }
    const int T = 4 * tm + mt;
    const int b0 = (32 * T) / SQ, b1 = (32 * T + 31) / SQ;
    gu64* myf = flagA + (((lane & 8) ? b1 : b0) * 4 + kq) * 8 + (lane & 7);
    unsigned rdy = 0;
    auto need = [&](int sI) {
      if (!((rdy >> sI) & 1u)) {
        for (unsigned spins = 0; spins < (1u << 20); ++spins) {
          const unsigned long long v = lane < 16 ? __hip_atomic_load(myf, __ATOMIC_RELAXED, __HIP_MEMORY_SCOPE_AGENT) : tag;
          const unsigned m = (unsigned)__ballot(v == tag);
          rdy = m & (m >> 8) & 0xffu;
          if ((rdy >> sI) & 1u) break;
          __builtin_amdgcn_s_sleep(1);
        }
        asm volatile("" ::: "memory");
      }
    };
    const __amdgpu_buffer_rsrc_t ro = __builtin_amdgcn_make_buffer_rsrc((char*)o_ws + (size_t)kq * 8 * MROWS * (DH * 2), 0, 8 * MROWS * DH * 2, 0x00020000);
    u32x4 of[8][4];
    const int ovo = T * 8192 + k2 * 4096 + lane * 16;
    f32x16 acc[2] = {};
    bf16x8 fa[2][2][2];
    auto ldw = [&](int q) {
      const unsigned base = wo_slot(2 * (q >> 2)) + (q & 3) * 2048;
      fa[q & 1][0][0] = lds_tr2(base + aw[0][0], base + aw[0][1]);
      fa[q & 1][1][0] = lds_tr2(base + aw[1][0], base + aw[1][1]);
    };
    ldw(0);
#define OLAG 3
#pragma unroll
    for (int sI = 0; sI < 8 + OLAG; ++sI) {
      if (sI < 8) {
        need(sI);
        if (sI == 0) { STAMP(26); }
#pragma unroll
        for (int j = 0; j < 4; ++j) of[sI][j] = __builtin_amdgcn_raw_buffer_load_b128(ro, ovo + j * 1024, sI * MROWS * (DH * 2), 16);
        SCHED();
      }
      if (sI >= OLAG) {
#pragma unroll
        for (int j = 0; j < 4; ++j) {
          const int q = 4 * (sI - OLAG) + j;
          if (q == 19) rawbar();
          if (q == 0) { STAMP(27); }
          if (q + 1 < 32) ldw(q + 1);
          const bf16x8 ob = __builtin_bit_cast(bf16x8, of[q >> 2][q & 3]);
          acc[0] = MFMA(fa[q & 1][0][0], ob, acc[0]);
          acc[1] = MFMA(fa[q & 1][1][0], ob, acc[1]);
          SCHED();
        }
      }
    }
    STAMP(24);
    {
      const unsigned ew = 96u * 1024u + wid * 4096 + lane * 16, er = 96u * 1024u + (wid ^ 4) * 4096 + lane * 16;
      auto put = [&](const f32x16& v) {
#pragma unroll
        for (int g = 0; g < 4; ++g) {
          f32x4 w = {v[4 * g], v[4 * g + 1], v[4 * g + 2], v[4 * g + 3]};
          *(LDSP f32x4*)(uintptr_t)(ew + g * 1024) = w;
        }
      };
      if (k2 == 0) put(acc[1]); else put(acc[0]);
      bar();
      auto get = [&](const f32x16& own, bool first) {
        f32x16 o;
#pragma unroll
        for (int g = 0; g < 4; ++g) {
          const f32x4 w = *(const LDSP f32x4*)(uintptr_t)(er + g * 1024);
#pragma unroll
          for (int e = 0; e < 4; ++e) o[4 * g + e] = own[4 * g + e] + w[e];
        }
        return o;
      };
      if (k2 == 0) acc[0] = get(acc[0], true); else acc[0] = get(acc[1], false);
    }
    float* sl = slab + (size_t)tile * 4 * 4 * 2 * 1024;
    if (mt != kq) {
      const __amdgpu_buffer_rsrc_t rs = __builtin_amdgcn_make_buffer_rsrc((void*)(sl + ((mt * 4 + kq) * 2 + nh) * 1024), 0, 4096, 0x00020000);
#pragma unroll
      for (int g = 0; g < 4; ++g) {
        f32x4 w = {acc[0][4 * g], acc[0][4 * g + 1], acc[0][4 * g + 2], acc[0][4 * g + 3]};
        __builtin_amdgcn_raw_buffer_store_b128(__builtin_bit_cast(u32x4, w), rs, g * 1024 + lane * 16, 0, 16);
      }
    }
    asm volatile("s_waitcnt vmcnt(0)" ::: "memory");
    __syncthreads();
    if (wid == 0 && lane < 4 && lane != kq)
      __hip_atomic_store(flagB + (tile * 4 + lane) * 4 + kq, tag, __ATOMIC_RELAXED, __HIP_MEMORY_SCOPE_AGENT);
    if (mt == kq) {
      wait_flags(flagB + (tile * 4 + kq) * 4 + (lane & 3), lane < 4 && lane != kq, tag);
      u32x4 pv[3][4];
#pragma unroll
      for (int o = 0; o < 3; ++o) {
        const int src = o + (o >= kq);
        const __amdgpu_buffer_rsrc_t rs = __builtin_amdgcn_make_buffer_rsrc((void*)(sl + ((kq * 4 + src) * 2 + nh) * 1024), 0, 4096, 0x00020000);
#pragma unroll
        for (int g = 0; g < 4; ++g) pv[o][g] = __builtin_amdgcn_raw_buffer_load_b128(rs, g * 1024 + lane * 16, 0, 16);
      }
      float r[16];
#pragma unroll
      for (int g = 0; g < 4; ++g) {
        const f32x4 a = __builtin_bit_cast(f32x4, pv[0][g]), c = __builtin_bit_cast(f32x4, pv[1][g]), d = __builtin_bit_cast(f32x4, pv[2][g]);
#pragma unroll
        for (int e = 0; e < 4; ++e) {
          const float own = acc[0][4 * g + e];
          const float t0 = kq == 0 ? own : a[e];
          const float t1 = kq == 0 ? a[e] : (kq == 1 ? own : c[e]);
          const float t2 = kq <= 1 ? c[e] : (kq == 2 ? own : d[e]);
          const float t3 = kq == 3 ? own : d[e];
          r[4 * g + e] = ((t0 + t1) + t2) + t3;
        }
      }
      char* dst = (char*)out + ((size_t)(tm * 128 + 32 * mt + l31) * DM + tn * 64 + nh * 32 + 16 * h) * 2;
      u32x4 w0 = {cvtpk(r[0], r[1]), cvtpk(r[2], r[3]), cvtpk(r[4], r[5]), cvtpk(r[6], r[7])};
      u32x4 w1 = {cvtpk(r[8], r[9]), cvtpk(r[10], r[11]), cvtpk(r[12], r[13]), cvtpk(r[14], r[15])};
      *(u32x4*)dst = w0;
      *(u32x4*)(dst + 16) = w1;
    }
  }
  STAMP(25);
  STAMP_FLUSH;
}

extern "C" void kernel_launch(void* const* d_in, const int* in_sizes, int n_in, void* d_out, int out_size, void* d_ws,
                              size_t ws_size, hipStream_t stream) {
  const __hip_bfloat16* x = (const __hip_bfloat16*)d_in[0];
  const __hip_bfloat16* kc = (const __hip_bfloat16*)d_in[1];
  const __hip_bfloat16* vc = (const __hip_bfloat16*)d_in[2];
  const __hip_bfloat16* wq = (const __hip_bfloat16*)d_in[3];
  const __hip_bfloat16* wo = (const __hip_bfloat16*)d_in[4];
  __hip_bfloat16* o_ws = (__hip_bfloat16*)d_ws;
  float* slab = (float*)((char*)d_ws + (size_t)8 * 1024 * 1024);
  unsigned long long* flags = (unsigned long long*)((char*)d_ws + (size_t)24 * 1024 * 1024);
  hipFuncSetAttribute((const void*)k_fused, hipFuncAttributeMaxDynamicSharedMemorySize, K1_LDS);
  hipLaunchKernelGGL(k_fused, dim3(256), dim3(512), K1_LDS, stream, x, kc, vc, wq, wo, o_ws, slab, flags, (__hip_bfloat16*)d_out);
}
```

```cpp
#include <hip/hip_runtime.h>
#include <hip/hip_bf16.h>
#include <stdint.h>

typedef __attribute__((ext_vector_type(8))) __bf16 bf16x8;
typedef __attribute__((ext_vector_type(4))) __bf16 bf16x4;
typedef __attribute__((ext_vector_type(2))) __bf16 bf16x2;
typedef __attribute__((ext_vector_type(16))) float f32x16;
typedef __attribute__((ext_vector_type(4))) float f32x4;
typedef __attribute__((ext_vector_type(2))) float f32x2;
typedef __attribute__((ext_vector_type(4))) unsigned u32x4;
#define LDSP __attribute__((address_space(3)))
#define DI __device__ __forceinline__

constexpr int NB = 8, SQ = 112, DM = 576, HQ = 32, HKV = 16, SK = 512, DH = 128;
constexpr int MROWS = NB * SQ;
constexpr int KO = HQ * DH;

DI unsigned cvtpk(float lo, float hi) {
  f32x2 v = {lo, hi};
  bf16x2 b = __builtin_convertvector(v, bf16x2);
  return __builtin_bit_cast(unsigned, b);
}
DI void glds16(const void* sbase, unsigned voff, unsigned lds_dst) {
  unsigned keep;
  asm volatile("s_mov_b32 %0, m0\n\ts_mov_b32 m0, %3\n\ts_nop 0\n\tglobal_load_lds_dwordx4 %1, %2\n\ts_mov_b32 m0, %0"
               : "=&s"(keep) : "v"(voff), "s"(sbase), "s"(lds_dst) : "memory");
}
DI void glds16nt(const void* sbase, unsigned voff, unsigned lds_dst) {
  unsigned keep;
  asm volatile("s_mov_b32 %0, m0\n\ts_mov_b32 m0, %3\n\ts_nop 0\n\tglobal_load_lds_dwordx4 %1, %2 nt\n\ts_mov_b32 m0, %0"
               : "=&s"(keep) : "v"(voff), "s"(sbase), "s"(lds_dst) : "memory");
}
DI void glds16sc1(const void* sbase, unsigned voff, unsigned lds_dst) {
  unsigned keep;
  asm volatile("s_mov_b32 %0, m0\n\ts_mov_b32 m0, %3\n\ts_nop 0\n\tglobal_load_lds_dwordx4 %1, %2 sc1\n\ts_mov_b32 m0, %0"
               : "=&s"(keep) : "v"(voff), "s"(sbase), "s"(lds_dst) : "memory");
}
template <int J> DI void glds16_if(const void* sbase, unsigned voff, unsigned lds_dst, int sel) {
  unsigned keep;
  asm volatile("s_cmp_lg_u32 %4, %5\n\ts_cbranch_scc1 .Lgl%=\n\ts_mov_b32 %0, m0\n\ts_mov_b32 m0, %3\n\ts_nop 0\n\tglobal_load_lds_dwordx4 %1, %2\n\ts_mov_b32 m0, %0\n.Lgl%=:"
               : "=&s"(keep) : "v"(voff), "s"(sbase), "s"(lds_dst), "s"(sel), "i"(J) : "memory", "scc");
}
template <int J> DI void glds16nt_if(const void* sbase, unsigned voff, unsigned lds_dst, int sel) {
  unsigned keep;
  asm volatile("s_cmp_lg_u32 %4, %5\n\ts_cbranch_scc1 .Lgl%=\n\ts_mov_b32 %0, m0\n\ts_mov_b32 m0, %3\n\ts_nop 0\n\tglobal_load_lds_dwordx4 %1, %2 nt\n\ts_mov_b32 m0, %0\n.Lgl%=:"
               : "=&s"(keep) : "v"(voff), "s"(sbase), "s"(lds_dst), "s"(sel), "i"(J) : "memory", "scc");
}
template <bool NT, int J> DI void gl_t(const void* sbase, unsigned voff, unsigned lds_dst, int sel) {
  if constexpr (J < 0) { if constexpr (NT) glds16nt(sbase, voff, lds_dst); else glds16(sbase, voff, lds_dst); }
  else { if constexpr (NT) glds16nt_if<J>(sbase, voff, lds_dst, sel); else glds16_if<J>(sbase, voff, lds_dst, sel); }
}
template <int V> struct IC { static constexpr int value = V; };
template <int I, int N, class F> DI void sfor(F&& f) { if constexpr (I < N) { f(IC<I>{}); sfor<I + 1, N>(f); } }
DI void gl_sel(bool nt, int j, const void* sbase, unsigned voff, unsigned lds_dst, int sel) {
  if (nt) {
    if (j < 0) glds16nt(sbase, voff, lds_dst);
    else if (j == 0) glds16nt_if<0>(sbase, voff, lds_dst, sel); else if (j == 1) glds16nt_if<1>(sbase, voff, lds_dst, sel);
    else if (j == 2) glds16nt_if<2>(sbase, voff, lds_dst, sel); else glds16nt_if<3>(sbase, voff, lds_dst, sel);
  } else {
    if (j < 0) glds16(sbase, voff, lds_dst);
    else if (j == 0) glds16_if<0>(sbase, voff, lds_dst, sel); else if (j == 1) glds16_if<1>(sbase, voff, lds_dst, sel);
    else if (j == 2) glds16_if<2>(sbase, voff, lds_dst, sel); else glds16_if<3>(sbase, voff, lds_dst, sel);
  }
}
DI void set_m0(unsigned base, unsigned imm) { asm volatile("s_add_u32 m0, %0, %1\n\ts_nop 0" ::"s"(base), "i"(imm) : "scc"); }
template <int OFF, int POL> DI void gld(const void* sbase, unsigned voff) {
  if constexpr (POL == 0) asm volatile("global_load_lds_dwordx4 %0, %1 offset:%2" ::"v"(voff), "s"(sbase), "i"(OFF) : "memory");
  else if constexpr (POL == 1) asm volatile("global_load_lds_dwordx4 %0, %1 offset:%2 nt" ::"v"(voff), "s"(sbase), "i"(OFF) : "memory");
  else asm volatile("global_load_lds_dwordx4 %0, %1 offset:%2 sc1" ::"v"(voff), "s"(sbase), "i"(OFF) : "memory");
}
template <int N> DI void wait_lgkm() { __builtin_amdgcn_s_waitcnt(0xC07F | (N << 8)); }
#define GL_KV glds16nt
#define GL_XW glds16
#define GL_WO glds16
#define GL_O glds16
template <int N> DI void wait_vm() { asm volatile("s_waitcnt vmcnt(%0)" ::"i"(N) : "memory"); }
DI void bar() { asm volatile("s_waitcnt lgkmcnt(0)\n\ts_barrier" ::: "memory"); }
DI bf16x8 lds_b128(unsigned a) { return *(const LDSP bf16x8*)(uintptr_t)a; }
DI bf16x8 lds_tr2(unsigned a0, unsigned a1) {
  bf16x4 lo = __builtin_amdgcn_ds_read_tr16_b64_v4bf16((LDSP bf16x4*)(uintptr_t)a0);
  bf16x4 hi = __builtin_amdgcn_ds_read_tr16_b64_v4bf16((LDSP bf16x4*)(uintptr_t)a1);
  return __builtin_shufflevector(lo, hi, 0, 1, 2, 3, 4, 5, 6, 7);
}
DI bf16x8 pack8(const f32x16& x, int s) {
  u32x4 w = {cvtpk(x[8 * s], x[8 * s + 1]), cvtpk(x[8 * s + 2], x[8 * s + 3]), cvtpk(x[8 * s + 4], x[8 * s + 5]),
             cvtpk(x[8 * s + 6], x[8 * s + 7])};
  return __builtin_bit_cast(bf16x8, w);
}
#define STAMP(i)
#define CSTAMP(i)
#define STAMP_DECL
#define STAMP_FLUSH
#define STAMP2(i)
#define CSTAMP2(i)
#define STAMP_DECL2
#define STAMP_FLUSH2
#define MFMA(a, b, c) __builtin_amdgcn_mfma_f32_32x32x16_bf16((a), (b), (c), 0, 0, 0)

constexpr unsigned SUB = 16384u;
constexpr unsigned RGB = 65536u;
constexpr unsigned MISC = 131072u;
#define STAG1 0
#define STAG2 0
#define WO4 ((wid & 1) + 2 * (wid >> 2))
#define PIPE_MU 1
#define NT1 3
struct SmSched {
  int mu[129];
  int tm[128];
  int rs[128];
  constexpr SmSched() : mu{}, tm{}, rs{} {
    int n[128] = {};
    for (int m = 0; m < 128; ++m) { tm[m] = -1; rs[m] = -1; }
    for (int j = 0; j < 8; ++j) tm[8 * j + (j == 0 ? 14 : (j == NT1 - 1 || j == 7) ? 9 : 10)] = j;
    const int sw = 64 + 8 * NT1;
    for (int d = 0; d < 4; ++d) rs[sw - 2 + d] = d;
    {
      const int a = 8 * NT1 + 3, e = 64; int av = 0, acc = 0;
      for (int m = a; m < e; ++m) if (tm[m] < 0) ++av;
      for (int m = a; m < e; ++m) if (tm[m] < 0) { acc += 8 * NT1; if (acc >= av) { acc -= av; ++n[m]; } }
    }
    {
      const int a = 67, e = 123; int av = 0, acc = 0;
      for (int m = a; m < e; ++m) if (rs[m] < 0) ++av;
      for (int m = a; m < e; ++m) if (rs[m] < 0) { acc += 64 - 8 * NT1; if (acc >= av) { acc -= av; ++n[m]; } }
    }
    int k = 0;
    for (int m = 0; m < 128; ++m) { mu[m] = k; k += n[m]; }
    mu[128] = k;
  }
  constexpr bool ok() const {
    if (mu[128] != 64) return false;
    for (int m = 0; m < 128; ++m)
      for (int k = mu[m]; k < mu[m + 1]; ++k) if (m + 1 >= 64 + 4 * (k >> 2)) return false;
    return true;
  }
};
constexpr SmSched SM{};
static_assert(SM.ok(), "softmax schedule misses a deadline");
constexpr int K1_LDS = 160 * 1024;
#define SCHED() __builtin_amdgcn_sched_barrier(0)
DI void rawbar() { asm volatile("s_barrier" ::: "memory"); }

typedef __attribute__((address_space(1))) unsigned long long gu64;
extern "C" __device__ unsigned long long hw_dispatch_id() __asm("llvm.amdgcn.dispatch.id");
DI constexpr unsigned wo_slot(int t) {
  constexpr unsigned pb[8] = {128u, 144u, 16u, 80u, 0u, 64u, 32u, 48u};
  return pb[t >> 1] * 1024u + (unsigned)(t & 1) * 8192u;
}
DI void wait_flags(gu64* f, bool on, unsigned long long tag) {
  for (unsigned spins = 0; spins < (1u << 20); ++spins) {
    const unsigned long long v = on ? __hip_atomic_load(f, __ATOMIC_RELAXED, __HIP_MEMORY_SCOPE_AGENT) : tag;
    if (__all(v == tag)) break;
    __builtin_amdgcn_s_sleep(1);
  }
  asm volatile("" ::: "memory");
}

__global__ __launch_bounds__(512, 2) void k_fused(const __hip_bfloat16* __restrict__ x, const __hip_bfloat16* __restrict__ kc,
                                                   const __hip_bfloat16* __restrict__ vc, const __hip_bfloat16* __restrict__ wq,
                                                   const __hip_bfloat16* __restrict__ wo, __hip_bfloat16* o_ws,
                                                   float* slab, unsigned long long* flags,
                                                   __hip_bfloat16* __restrict__ out) {
  extern __shared__ __attribute__((aligned(16))) char lds[];
  const int tid = threadIdx.x, lane = tid & 63;
  const int wid = __builtin_amdgcn_readfirstlane(tid >> 6);
  const int rg = wid & 3, kh = wid >> 2;
  const int l31 = lane & 31, h = lane >> 5;
  const int bid = blockIdx.x;
  const int hq = 4 * (bid & 7) + ((bid >> 3) & 3), b = bid >> 5;
  const int hkv = hq >> 1;
  STAMP_DECL;

  unsigned vx[2], vw[2], vkv[4];
#pragma unroll
  for (int e = 0; e < 2; ++e) {
    const int i = 2 * wid + e;
    int row = 8 * i + (lane >> 3);
    const int c = (lane & 7) ^ ((row >> 1) & 7);
    row = row < SQ ? row : SQ - 1;
    vx[e] = (unsigned)(row * (DM * 2) + c * 16 - e * 1024 + 1024);
    const int rw = 4 * i + (lane >> 4);
    const int cw = (lane & 15) ^ (((rw & 3) << 2) | ((rw >> 2) & 3));
    vw[e] = (unsigned)(rw * (KO * 2) + cw * 16 - e * 1024 + 1024);
  }
#pragma unroll
  for (int e = 0; e < 4; ++e) {
    const int r = 4 * (4 * (wid & 3) + e) + (lane >> 4);
    const int c = (lane & 15) ^ (((r & 3) << 2) | ((r >> 2) & 3));
    vkv[e] = (unsigned)((r - 4 * e) * 256 + c * 16);
  }
  const char* xb = (const char*)x + (size_t)b * SQ * DM * 2 - 1024;
  const char* wb = (const char*)wq + (size_t)hq * DH * 2 - 1024;
  const unsigned dX = (unsigned)wid * 2048u, dK = (unsigned)(kh * RGB + rg * 4096), dW = (unsigned)wid * 1024u;
  const size_t kvo = ((size_t)(b * HKV + hkv) * SK + 256 * kh) * DH * 2;
  const char* kb = (const char*)kc + kvo;
  const char* vb = (const char*)vc + kvo;

  auto issue1 = [&](auto Tc, auto Ec) {
    constexpr int t = decltype(Tc)::value, e = decltype(Ec)::value;
    constexpr unsigned sb = (unsigned)(t & 3) * SUB;
    if constexpr (t < 9) {
      if constexpr (e == 0) set_m0(dX, sb);
      if constexpr (e == 2) set_m0(dX, RGB + sb);
      if constexpr (e < 2) gld<e * 1024, 0>(xb + t * 128, vx[e]);
      else                 gld<(e - 2) * 1024, 0>(wb + (size_t)t * 64 * KO * 2, vw[e - 2]);
    } else if constexpr (t < 17) {
      if constexpr (e == 0) set_m0(dK, sb);
      if constexpr (t < 13) gld<e * 1024, 1>(kb + (t - 9) * 16384, vkv[e]);
      else                  gld<e * 1024, 1>(vb + (t - 13) * 16384, vkv[e]);
    }
  };
  STAMP(0); CSTAMP(20);
  sfor<0, 3>([&](auto Tc) { sfor<0, 4>([&](auto Ec) { issue1(Tc, Ec); }); });

  const int q4 = (lane & 15) >> 2, p4 = lane & 3, g16 = (lane >> 4) & 1;
  unsigned ax[4];
#pragma unroll
  for (int ks = 0; ks < 4; ++ks) ax[ks] = (unsigned)((32 * rg + l31) * 128 + (((2 * ks + h) ^ ((l31 >> 1) & 7)) * 16));
  unsigned aw[2][2];
#pragma unroll
  for (int dl = 0; dl < 2; ++dl)
#pragma unroll
    for (int t = 0; t < 2; ++t) {
      const int dt = 2 * kh + dl;
      const int k = 8 * h + 4 * t + q4;
      const int c = (4 * dt + 2 * g16 + (p4 & 1)) ^ ((q4 << 2) | ((2 * h + t) & 3));
      aw[dl][t] = (unsigned)(RGB + k * 256 + c * 16 + (p4 >> 1) * 8);
    }
  unsigned ak[8];
#pragma unroll
  for (int ks = 0; ks < 8; ++ks)
    ak[ks] = (unsigned)(kh * RGB + l31 * 256 + (((2 * ks + h) ^ (8 * kh) ^ (((l31 & 3) << 2) | ((l31 >> 2) & 3))) * 16));
  unsigned av[4][2];
#pragma unroll
  for (int dt = 0; dt < 4; ++dt)
#pragma unroll
    for (int t = 0; t < 2; ++t) {
      const int key = 8 * t + 4 * h + q4;
      const int c = (4 * dt + 2 * (p4 & 1) + g16) ^ ((q4 << 2) | ((2 * t + h) & 3));
      av[dt][t] = (unsigned)(kh * RGB + key * 256 + c * 16 + (p4 >> 1) * 8);
    }

  bf16x8 f1[3][3];
  auto ld1 = [&](int G) {
    const unsigned sb = (unsigned)((G >> 2) & 3) * SUB; const int ks = G & 3, r = G % 3;
    f1[r][0] = lds_b128(sb + ax[ks]);
    f1[r][1] = lds_tr2(sb + aw[0][0] + ks * 4096, sb + aw[0][1] + ks * 4096);
    f1[r][2] = lds_tr2(sb + aw[1][0] + ks * 4096, sb + aw[1][1] + ks * 4096);
  };
  bf16x8 f2[3][4];
  auto ld2 = [&](int G) {
    const int kt = G < 4 ? (G & 1) : ((G >> 1) & 1), hf = G < 4 ? (G >> 1) : (G & 1);
    const unsigned sb = (unsigned)((9 + (G >> 2)) & 3) * SUB + kt * 8192; const int r = G % 3;
#pragma unroll
    for (int j = 0; j < 4; ++j) f2[r][j] = lds_b128(sb + ak[4 * hf + j]);
  };
  auto ld3 = [&](int G) {
    const unsigned sb = (unsigned)((13 + (G >> 2)) & 3) * SUB + ((G >> 1) & 1) * 8192 + (G & 1) * 4096; const int r = (G + 1) % 3;
#pragma unroll
    for (int dt = 0; dt < 4; ++dt) f2[r][dt] = lds_tr2(sb + av[dt][0], sb + av[dt][1]);
  };

  wait_vm<8>(); rawbar();
  ld1(0); ld1(1);
  SCHED();

  f32x16 qa[2] = {};
  sfor<0, 9>([&](auto Tc) {
    constexpr int t = decltype(Tc)::value;
    if constexpr (t < 8) wait_vm<4>();
    rawbar();
    STAMP(1 + t);
    sfor<0, 4>([&](auto Kc) {
      constexpr int ks = decltype(Kc)::value;
      constexpr int G = 4 * t + ks;
      if constexpr (G == 34) { wait_vm<6>(); rawbar(); }
      issue1(IC<t + 3>{}, Kc);
      wait_lgkm<5>();
      if constexpr (G + 2 < 36) ld1(G + 2); else ld2(G + 2 - 36);
      qa[0] = MFMA(f1[G % 3][1], f1[G % 3][0], qa[0]);
      qa[1] = MFMA(f1[G % 3][2], f1[G % 3][0], qa[1]);
      SCHED();
    });
  });
  bf16x8 qf[8];
  qf[0] = pack8(qa[0], 0); qf[1] = pack8(qa[0], 1); qf[2] = pack8(qa[1], 0); qf[3] = pack8(qa[1], 1);
  {
    const unsigned qw = MISC + wid * 4096 + lane * 16;
#pragma unroll
    for (int f = 0; f < 4; ++f) *(LDSP bf16x8*)(uintptr_t)(qw + f * 1024) = qf[f];
  }
  SCHED();
  CSTAMP(22);
  const float CS = 0.08838834764831845f * 1.4426950408889634f;
  const int wo4 = WO4;
  f32x16 s[8];
  unsigned pw[16][4];
  float m1 = 0.f, mx = 0.f, mb = 0.f, ls0 = 0.f, ls1 = 0.f, al = 0.f;
  float rm0 = -__builtin_inff(), rm1 = -__builtin_inff();

  const int kq = (bid & 7) >> 1;
  const int tile0 = (bid & 1) ? 32 + (bid >> 3) : (bid >> 3);
  const bool activeB = tile0 < 63;
  const int tile = activeB ? tile0 : 0;
  const int tm = tile / 9, tn = tile % 9;
  const char* wob = (const char*)wo + ((size_t)kq * 1024 * DM + tn * 64) * 2;
  unsigned vwo;
  {
    const int row = 8 * wid + (lane >> 3);
    const int c = (lane & 7) ^ (((row >> 1) & 1) << 2);
    vwo = (unsigned)(row * (DM * 2) + c * 16);
  }
  auto issueWr = [&](int t) { set_m0(dW, wo_slot(t)); gld<0, 0>(wob + (size_t)t * 64 * DM * 2, vwo); };

  f32x16 oa[4];
  float pe[2][2];
  auto vwork = [&](auto Mc) {
    constexpr int m = decltype(Mc)::value;
    if constexpr (SM.tm[m] >= 0) {
      constexpr int j = SM.tm[m];
#pragma unroll
      for (int r = 0; r < 16; r += 4) {
        rm0 = fmaxf(fmaxf(rm0, s[j][r]), s[j][r + 1]);
        rm1 = fmaxf(fmaxf(rm1, s[j][r + 2]), s[j][r + 3]);
      }
      if constexpr (j == NT1 - 1 || j == 7) {
        float m_ = fmaxf(rm0, rm1);
        auto rr = __builtin_amdgcn_permlane32_swap(__float_as_uint(m_), __float_as_uint(m_), false, false);
        m_ = fmaxf(__uint_as_float(rr[0]), __uint_as_float(rr[1]));
        if constexpr (j == 7) {
          mx = m_; mb = mx * CS;
          al = __builtin_amdgcn_exp2f((m1 - mx) * CS);
          ls0 *= al; ls1 *= al;
        } else { m1 = m_; mb = m1 * CS; }
      }
    }
    if constexpr (SM.rs[m] >= 0) {
      constexpr int d = SM.rs[m];
#pragma unroll
      for (int r = 0; r < 16; ++r) oa[d][r] *= al;
    }
    if constexpr (m > 0) {
      sfor<SM.mu[m - 1], SM.mu[m]>([&](auto Kc) {
        constexpr int k = decltype(Kc)::value, f = k >> 2, q = k & 3;
        ls0 += pe[k & 1][0]; ls1 += pe[k & 1][1];
        pw[f][q] = cvtpk(pe[k & 1][0], pe[k & 1][1]);
      });
    }
    sfor<SM.mu[m], SM.mu[m + 1]>([&](auto Kc) {
      constexpr int k = decltype(Kc)::value, f = k >> 2, q = k & 3;
      pe[k & 1][0] = __builtin_amdgcn_exp2f(fmaf(s[f >> 1][8 * (f & 1) + 2 * q], CS, -mb));
      pe[k & 1][1] = __builtin_amdgcn_exp2f(fmaf(s[f >> 1][8 * (f & 1) + 2 * q + 1], CS, -mb));
    });
  };
  sfor<0, 16>([&](auto Gc) {
    constexpr int G = decltype(Gc)::value, i = G >> 2, t = 9 + i;
    constexpr int kt = G < 4 ? (G & 1) : ((G >> 1) & 1), hf = G < 4 ? (G >> 1) : (G & 1);
    if constexpr ((G & 3) == 0) {
      if constexpr (i > 0) { wait_vm<4>(); rawbar(); }
      STAMP(1 + t);
    }
    if constexpr (G == 1) {
      bar();
      const unsigned qr = MISC + (wid ^ 4) * 4096 + lane * 16;
#pragma unroll
      for (int f = 0; f < 4; ++f) qf[4 + f] = lds_b128(qr + f * 1024);
      issue1(IC<12>{}, IC<0>{});
    }
    if constexpr (G == 2) { wait_vm<6>(); rawbar(); }
    if constexpr (G > 0) issue1(IC<t + 3>{}, IC<(G & 3)>{});
    if constexpr (G == 15) wait_lgkm<8>(); else if constexpr (G == 1) wait_lgkm<8>(); else wait_lgkm<4>();
    if constexpr (G + 2 < 16) ld2(G + 2); else ld3(G + 2 - 16);
    sfor<0, 4>([&](auto Jc) {
      constexpr int j = decltype(Jc)::value;
      if constexpr (hf == 0 && j == 0) s[2 * i + kt] = MFMA(f2[G % 3][0], qf[0], f32x16{});
      else s[2 * i + kt] = MFMA(f2[G % 3][j], qf[4 * hf + j], s[2 * i + kt]);
      vwork(IC<4 * G + j>{});
      SCHED();
    });
  });
  sfor<0, 16>([&](auto Gc) {
    constexpr int G = decltype(Gc)::value, i = G >> 2, t = 13 + i;
    if constexpr ((G & 3) == 0) {
      if constexpr (i < 3) { wait_vm<4>(); rawbar(); }
      STAMP(1 + t);
    }
    issue1(IC<t + 3>{}, IC<(G & 3)>{});
    if constexpr (i == 1 || i == 2) issueWr(4 * (i - 1) + (G & 3));
    if constexpr (G < 15) wait_lgkm<8>(); else wait_lgkm<0>();
    if constexpr (G + 2 < 16) ld3(G + 2);
    const u32x4 pwv = {pw[G][0], pw[G][1], pw[G][2], pw[G][3]};
    sfor<0, 4>([&](auto Jc) {
      constexpr int dt = decltype(Jc)::value;
      if constexpr (G == 0) oa[dt] = MFMA(f2[(G + 1) % 3][dt], __builtin_bit_cast(bf16x8, pwv), f32x16{});
      else oa[dt] = MFMA(f2[(G + 1) % 3][dt], __builtin_bit_cast(bf16x8, pwv), oa[dt]);
      vwork(IC<64 + 4 * G + dt>{});
      SCHED();
    });
  });
  float ls = ls0 + ls1;
  {
    auto rr = __builtin_amdgcn_permlane32_swap(__float_as_uint(ls), __float_as_uint(ls), false, false);
    ls = __uint_as_float(rr[0]) + __uint_as_float(rr[1]);
  }
  const unsigned long long tag = ((hw_dispatch_id() + 1ull) * 0x9E3779B97F4A7C15ull) ^ (unsigned long long)(uintptr_t)__builtin_amdgcn_dispatch_ptr();
  gu64* flagA = (gu64*)flags;
  gu64* flagB = (gu64*)flags + 512;
  bar();
  STAMP(18); CSTAMP(23);
  issueWr(8); issueWr(9); issueWr(10);
  {
    constexpr unsigned MLB = 72u * 1024u;
    const unsigned xw = (unsigned)(32768 + kh * RGB + rg * 8192) + lane * 16, xr = (unsigned)(32768 + (kh ^ 1) * RGB + rg * 8192) + lane * 16;
    auto put = [&](const f32x16& v, int dl) {
#pragma unroll
      for (int g = 0; g < 4; ++g) {
        f32x4 w = {v[4 * g], v[4 * g + 1], v[4 * g + 2], v[4 * g + 3]};
        *(LDSP f32x4*)(uintptr_t)(xw + (dl * 4 + g) * 1024) = w;
      }
    };
    if (kh == 0) { put(oa[2], 0); put(oa[3], 1); } else { put(oa[0], 0); put(oa[1], 1); }
    f32x2 ml = {mx, ls};
    *(LDSP f32x2*)(uintptr_t)(MLB + wid * 512 + lane * 8) = ml;
    bar();
    STAMP2(1);
    const f32x2 pml = *(const LDSP f32x2*)(uintptr_t)(MLB + (wid ^ 4) * 512 + lane * 8);
    const float mt = fmaxf(mx, pml[0]);
    const float f0 = __builtin_amdgcn_exp2f((mx - mt) * CS), f1_ = __builtin_amdgcn_exp2f((pml[0] - mt) * CS);
    const float inv = 1.f / (f0 * ls + f1_ * pml[1]);
    const float g0 = f0 * inv, g1 = f1_ * inv;
    const int qrow = 32 * rg + l31, R = b * SQ + qrow;
    const __amdgpu_buffer_rsrc_t rs = __builtin_amdgcn_make_buffer_rsrc((char*)o_ws + (size_t)hq * MROWS * (DH * 2), 0, MROWS * DH * 2, 0x00020000);
    const int so = (R >> 5) * 8192 + (4 * kh + h) * 1024 + (R & 31) * 16;
    auto fin = [&](const f32x16& v, int dl) {
      float r[16];
#pragma unroll
      for (int g = 0; g < 4; ++g) {
        const f32x4 w = *(const LDSP f32x4*)(uintptr_t)(xr + (dl * 4 + g) * 1024);
#pragma unroll
        for (int e = 0; e < 4; ++e) r[4 * g + e] = v[4 * g + e] * g0 + w[e] * g1;
      }
      const u32x4 w0 = {cvtpk(r[0], r[1]), cvtpk(r[2], r[3]), cvtpk(r[4], r[5]), cvtpk(r[6], r[7])};
      const u32x4 w1 = {cvtpk(r[8], r[9]), cvtpk(r[10], r[11]), cvtpk(r[12], r[13]), cvtpk(r[14], r[15])};
      if (qrow < SQ) {
        __builtin_amdgcn_raw_buffer_store_b128(w0, rs, so + dl * 2048, 0, 16);
        __builtin_amdgcn_raw_buffer_store_b128(w1, rs, so + dl * 2048 + 512, 0, 16);
      }
    };
    if (kh == 0) { fin(oa[0], 0); fin(oa[1], 1); } else { fin(oa[2], 0); fin(oa[3], 1); }
  }
  STAMP2(3);
  asm volatile("s_waitcnt vmcnt(0)" ::: "memory");
  STAMP2(4);
  __syncthreads();
  STAMP2(5);
  if (tid == 0) __hip_atomic_store(flagA + (b * 4 + (hq >> 3)) * 8 + (hq & 7), tag, __ATOMIC_RELAXED, __HIP_MEMORY_SCOPE_AGENT);
  STAMP(19); CSTAMP(21);
  if (!activeB) { STAMP_FLUSH; return; }

  {
    const int mt = wid & 3, k2 = wid >> 2, nh = k2;
#pragma unroll
    for (int t = 11; t < 16; ++t) issueWr(t);
    const int q4 = (lane & 15) >> 2, p4 = lane & 3, g16 = (lane >> 4) & 1;
    unsigned aw[2][2];
#pragma unroll
    for (int n2 = 0; n2 < 2; ++n2)
#pragma unroll
      for (int t = 0; t < 2; ++t) {
        const int k = 8 * h + 4 * t + q4;
        const int c = (4 * n2 + 2 * (p4 & 1) + g16) ^ (((q4 >> 1) & 1) << 2);
        aw[n2][t] = (unsigned)(k2 * 8192 + k * 128 + c * 16 + (p4 >> 1) * 8);
      }
    const int T = 4 * tm + mt;
    const int b0 = (32 * T) / SQ, b1 = (32 * T + 31) / SQ;
    gu64* myf = flagA + (((lane & 8) ? b1 : b0) * 4 + kq) * 8 + (lane & 7);
    unsigned rdy = 0;
    auto need = [&](int sI) {
      if (!((rdy >> sI) & 1u)) {
        for (unsigned spins = 0; spins < (1u << 20); ++spins) {
          const unsigned long long v = lane < 16 ? __hip_atomic_load(myf, __ATOMIC_RELAXED, __HIP_MEMORY_SCOPE_AGENT) : tag;
          const unsigned m = (unsigned)__ballot(v == tag);
          rdy = m & (m >> 8) & 0xffu;
          if ((rdy >> sI) & 1u) break;
          __builtin_amdgcn_s_sleep(1);
        }
        asm volatile("" ::: "memory");
      }
    };
    const __amdgpu_buffer_rsrc_t ro = __builtin_amdgcn_make_buffer_rsrc((char*)o_ws + (size_t)kq * 8 * MROWS * (DH * 2), 0, 8 * MROWS * DH * 2, 0x00020000);
    u32x4 of[8][4];
    const int ovo = T * 8192 + k2 * 4096 + lane * 16;
    f32x16 acc[2] = {};
    bf16x8 fa[2][2][2];
    auto ldw = [&](int q) {
      const unsigned base = wo_slot(2 * (q >> 2)) + (q & 3) * 2048;
      fa[q & 1][0][0] = lds_tr2(base + aw[0][0], base + aw[0][1]);
      fa[q & 1][1][0] = lds_tr2(base + aw[1][0], base + aw[1][1]);
    };
    ldw(0);
#define OLAG 3
#pragma unroll
    for (int sI = 0; sI < 8 + OLAG; ++sI) {
      if (sI < 8) {
        need(sI);
        if (sI == 0) { STAMP(26); }
#pragma unroll
        for (int j = 0; j < 4; ++j) of[sI][j] = __builtin_amdgcn_raw_buffer_load_b128(ro, ovo + j * 1024, sI * MROWS * (DH * 2), 16);
        SCHED();
      }
      if (sI >= OLAG) {
#pragma unroll
        for (int j = 0; j < 4; ++j) {
          const int q = 4 * (sI - OLAG) + j;
          if (q == 19) rawbar();
          if (q == 0) { STAMP(27); }
          if (q + 1 < 32) ldw(q + 1);
          const bf16x8 ob = __builtin_bit_cast(bf16x8, of[q >> 2][q & 3]);
          acc[0] = MFMA(fa[q & 1][0][0], ob, acc[0]);
          acc[1] = MFMA(fa[q & 1][1][0], ob, acc[1]);
          SCHED();
        }
      }
    }
    STAMP(24);
    {
      const unsigned ew = 96u * 1024u + wid * 4096 + lane * 16, er = 96u * 1024u + (wid ^ 4) * 4096 + lane * 16;
      auto put = [&](const f32x16& v) {
#pragma unroll
        for (int g = 0; g < 4; ++g) {
          f32x4 w = {v[4 * g], v[4 * g + 1], v[4 * g + 2], v[4 * g + 3]};
          *(LDSP f32x4*)(uintptr_t)(ew + g * 1024) = w;
        }
      };
      if (k2 == 0) put(acc[1]); else put(acc[0]);
      bar();
      auto get = [&](const f32x16& own, bool first) {
        f32x16 o;
#pragma unroll
        for (int g = 0; g < 4; ++g) {
          const f32x4 w = *(const LDSP f32x4*)(uintptr_t)(er + g * 1024);
#pragma unroll
          for (int e = 0; e < 4; ++e) o[4 * g + e] = own[4 * g + e] + w[e];
        }
        return o;
      };
      if (k2 == 0) acc[0] = get(acc[0], true); else acc[0] = get(acc[1], false);
    }
    float* sl = slab + (size_t)tile * 4 * 4 * 2 * 1024;
    if (mt != kq) {
      const __amdgpu_buffer_rsrc_t rs = __builtin_amdgcn_make_buffer_rsrc((void*)(sl + ((mt * 4 + kq) * 2 + nh) * 1024), 0, 4096, 0x00020000);
#pragma unroll
      for (int g = 0; g < 4; ++g) {
        f32x4 w = {acc[0][4 * g], acc[0][4 * g + 1], acc[0][4 * g + 2], acc[0][4 * g + 3]};
        __builtin_amdgcn_raw_buffer_store_b128(__builtin_bit_cast(u32x4, w), rs, g * 1024 + lane * 16, 0, 16);
      }
    }
    asm volatile("s_waitcnt vmcnt(0)" ::: "memory");
    __syncthreads();
    if (wid == 0 && lane < 4 && lane != kq)
      __hip_atomic_store(flagB + (tile * 4 + lane) * 4 + kq, tag, __ATOMIC_RELAXED, __HIP_MEMORY_SCOPE_AGENT);
    if (mt == kq) {
      wait_flags(flagB + (tile * 4 + kq) * 4 + (lane & 3), lane < 4 && lane != kq, tag);
      u32x4 pv[3][4];
#pragma unroll
      for (int o = 0; o < 3; ++o) {
        const int src = o + (o >= kq);
        const __amdgpu_buffer_rsrc_t rs = __builtin_amdgcn_make_buffer_rsrc((void*)(sl + ((kq * 4 + src) * 2 + nh) * 1024), 0, 4096, 0x00020000);
#pragma unroll
        for (int g = 0; g < 4; ++g) pv[o][g] = __builtin_amdgcn_raw_buffer_load_b128(rs, g * 1024 + lane * 16, 0, 16);
      }
      float r[16];
#pragma unroll
      for (int g = 0; g < 4; ++g) {
        const f32x4 a = __builtin_bit_cast(f32x4, pv[0][g]), c = __builtin_bit_cast(f32x4, pv[1][g]), d = __builtin_bit_cast(f32x4, pv[2][g]);
#pragma unroll
        for (int e = 0; e < 4; ++e) {
          const float own = acc[0][4 * g + e];
          const float t0 = kq == 0 ? own : a[e];
          const float t1 = kq == 0 ? a[e] : (kq == 1 ? own : c[e]);
          const float t2 = kq <= 1 ? c[e] : (kq == 2 ? own : d[e]);
          const float t3 = kq == 3 ? own : d[e];
          r[4 * g + e] = ((t0 + t1) + t2) + t3;
        }
      }
      char* dst = (char*)out + ((size_t)(tm * 128 + 32 * mt + l31) * DM + tn * 64 + nh * 32 + 16 * h) * 2;
      u32x4 w0 = {cvtpk(r[0], r[1]), cvtpk(r[2], r[3]), cvtpk(r[4], r[5]), cvtpk(r[6], r[7])};
      u32x4 w1 = {cvtpk(r[8], r[9]), cvtpk(r[10], r[11]), cvtpk(r[12], r[13]), cvtpk(r[14], r[15])};
      *(u32x4*)dst = w0;
      *(u32x4*)(dst + 16) = w1;
    }
  }
  STAMP(25);
  STAMP_FLUSH;
}

extern "C" void kernel_launch(void* const* d_in, const int* in_sizes, int n_in, void* d_out, int out_size, void* d_ws,
                              size_t ws_size, hipStream_t stream) {
  const __hip_bfloat16* x = (const __hip_bfloat16*)d_in[0];
  const __hip_bfloat16* kc = (const __hip_bfloat16*)d_in[1];
  const __hip_bfloat16* vc = (const __hip_bfloat16*)d_in[2];
  const __hip_bfloat16* wq = (const __hip_bfloat16*)d_in[3];
  const __hip_bfloat16* wo = (const __hip_bfloat16*)d_in[4];
  __hip_bfloat16* o_ws = (__hip_bfloat16*)d_ws;
  float* slab = (float*)((char*)d_ws + (size_t)8 * 1024 * 1024);
  unsigned long long* flags = (unsigned long long*)((char*)d_ws + (size_t)24 * 1024 * 1024);
  hipFuncSetAttribute((const void*)k_fused, hipFuncAttributeMaxDynamicSharedMemorySize, K1_LDS);
  hipLaunchKernelGGL(k_fused, dim3(256), dim3(512), K1_LDS, stream, x, kc, vc, wq, wo, o_ws, slab, flags, (__hip_bfloat16*)d_out);
}
```

```cpp
#include <hip/hip_runtime.h>
#include <hip/hip_bf16.h>
#include <stdint.h>

typedef __attribute__((ext_vector_type(8))) __bf16 bf16x8;
typedef __attribute__((ext_vector_type(4))) __bf16 bf16x4;
typedef __attribute__((ext_vector_type(2))) __bf16 bf16x2;
typedef __attribute__((ext_vector_type(16))) float f32x16;
typedef __attribute__((ext_vector_type(4))) float f32x4;
typedef __attribute__((ext_vector_type(2))) float f32x2;
typedef __attribute__((ext_vector_type(4))) unsigned u32x4;
#define LDSP __attribute__((address_space(3)))
#define DI __device__ __forceinline__

constexpr int NB = 8, SQ = 112, DM = 576, HQ = 32, HKV = 16, SK = 512, DH = 128;
constexpr int MROWS = NB * SQ;
constexpr int KO = HQ * DH;

DI unsigned cvtpk(float lo, float hi) {
  f32x2 v = {lo, hi};
  bf16x2 b = __builtin_convertvector(v, bf16x2);
  return __builtin_bit_cast(unsigned, b);
}
DI void glds16(const void* sbase, unsigned voff, unsigned lds_dst) {
  unsigned keep;
  asm volatile("s_mov_b32 %0, m0\n\ts_mov_b32 m0, %3\n\ts_nop 0\n\tglobal_load_lds_dwordx4 %1, %2\n\ts_mov_b32 m0, %0"
               : "=&s"(keep) : "v"(voff), "s"(sbase), "s"(lds_dst) : "memory");
}
DI void glds16nt(const void* sbase, unsigned voff, unsigned lds_dst) {
  unsigned keep;
  asm volatile("s_mov_b32 %0, m0\n\ts_mov_b32 m0, %3\n\ts_nop 0\n\tglobal_load_lds_dwordx4 %1, %2 nt\n\ts_mov_b32 m0, %0"
               : "=&s"(keep) : "v"(voff), "s"(sbase), "s"(lds_dst) : "memory");
}
DI void glds16sc1(const void* sbase, unsigned voff, unsigned lds_dst) {
  unsigned keep;
  asm volatile("s_mov_b32 %0, m0\n\ts_mov_b32 m0, %3\n\ts_nop 0\n\tglobal_load_lds_dwordx4 %1, %2 sc1\n\ts_mov_b32 m0, %0"
               : "=&s"(keep) : "v"(voff), "s"(sbase), "s"(lds_dst) : "memory");
}
template <int J> DI void glds16_if(const void* sbase, unsigned voff, unsigned lds_dst, int sel) {
  unsigned keep;
  asm volatile("s_cmp_lg_u32 %4, %5\n\ts_cbranch_scc1 .Lgl%=\n\ts_mov_b32 %0, m0\n\ts_mov_b32 m0, %3\n\ts_nop 0\n\tglobal_load_lds_dwordx4 %1, %2\n\ts_mov_b32 m0, %0\n.Lgl%=:"
               : "=&s"(keep) : "v"(voff), "s"(sbase), "s"(lds_dst), "s"(sel), "i"(J) : "memory", "scc");
}
template <int J> DI void glds16nt_if(const void* sbase, unsigned voff, unsigned lds_dst, int sel) {
  unsigned keep;
  asm volatile("s_cmp_lg_u32 %4, %5\n\ts_cbranch_scc1 .Lgl%=\n\ts_mov_b32 %0, m0\n\ts_mov_b32 m0, %3\n\ts_nop 0\n\tglobal_load_lds_dwordx4 %1, %2 nt\n\ts_mov_b32 m0, %0\n.Lgl%=:"
               : "=&s"(keep) : "v"(voff), "s"(sbase), "s"(lds_dst), "s"(sel), "i"(J) : "memory", "scc");
}
template <bool NT, int J> DI void gl_t(const void* sbase, unsigned voff, unsigned lds_dst, int sel) {
  if constexpr (J < 0) { if constexpr (NT) glds16nt(sbase, voff, lds_dst); else glds16(sbase, voff, lds_dst); }
  else { if constexpr (NT) glds16nt_if<J>(sbase, voff, lds_dst, sel); else glds16_if<J>(sbase, voff, lds_dst, sel); }
}
template <int V> struct IC { static constexpr int value = V; };
template <int I, int N, class F> DI void sfor(F&& f) { if constexpr (I < N) { f(IC<I>{}); sfor<I + 1, N>(f); } }
DI void gl_sel(bool nt, int j, const void* sbase, unsigned voff, unsigned lds_dst, int sel) {
  if (nt) {
    if (j < 0) glds16nt(sbase, voff, lds_dst);
    else if (j == 0) glds16nt_if<0>(sbase, voff, lds_dst, sel); else if (j == 1) glds16nt_if<1>(sbase, voff, lds_dst, sel);
    else if (j == 2) glds16nt_if<2>(sbase, voff, lds_dst, sel); else glds16nt_if<3>(sbase, voff, lds_dst, sel);
  } else {
    if (j < 0) glds16(sbase, voff, lds_dst);
    else if (j == 0) glds16_if<0>(sbase, voff, lds_dst, sel); else if (j == 1) glds16_if<1>(sbase, voff, lds_dst, sel);
    else if (j == 2) glds16_if<2>(sbase, voff, lds_dst, sel); else glds16_if<3>(sbase, voff, lds_dst, sel);
  }
}
DI void set_m0(unsigned base, unsigned imm) { asm volatile("s_add_u32 m0, %0, %1\n\ts_nop 0" ::"s"(base), "i"(imm) : "scc"); }
template <int OFF, int POL> DI void gld(const void* sbase, unsigned voff) {
  if constexpr (POL == 0) asm volatile("global_load_lds_dwordx4 %0, %1 offset:%2" ::"v"(voff), "s"(sbase), "i"(OFF) : "memory");
  else if constexpr (POL == 1) asm volatile("global_load_lds_dwordx4 %0, %1 offset:%2 nt" ::"v"(voff), "s"(sbase), "i"(OFF) : "memory");
  else asm volatile("global_load_lds_dwordx4 %0, %1 offset:%2 sc1" ::"v"(voff), "s"(sbase), "i"(OFF) : "memory");
}
template <int N> DI void wait_lgkm() { __builtin_amdgcn_s_waitcnt(0xC07F | (N << 8)); }
#define GL_KV glds16nt
#define GL_XW glds16
#define GL_WO glds16
#define GL_O glds16
template <int N> DI void wait_vm() { asm volatile("s_waitcnt vmcnt(%0)" ::"i"(N) : "memory"); }
DI void bar() { asm volatile("s_waitcnt lgkmcnt(0)\n\ts_barrier" ::: "memory"); }
DI bf16x8 lds_b128(unsigned a) { return *(const LDSP bf16x8*)(uintptr_t)a; }
DI bf16x8 lds_tr2(unsigned a0, unsigned a1) {
  bf16x4 lo = __builtin_amdgcn_ds_read_tr16_b64_v4bf16((LDSP bf16x4*)(uintptr_t)a0);
  bf16x4 hi = __builtin_amdgcn_ds_read_tr16_b64_v4bf16((LDSP bf16x4*)(uintptr_t)a1);
  return __builtin_shufflevector(lo, hi, 0, 1, 2, 3, 4, 5, 6, 7);
}
DI bf16x8 pack8(const f32x16& x, int s) {
  u32x4 w = {cvtpk(x[8 * s], x[8 * s + 1]), cvtpk(x[8 * s + 2], x[8 * s + 3]), cvtpk(x[8 * s + 4], x[8 * s + 5]),
             cvtpk(x[8 * s + 6], x[8 * s + 7])};
  return __builtin_bit_cast(bf16x8, w);
}
#define STAMP(i)
#define CSTAMP(i)
#define STAMP_DECL
#define STAMP_FLUSH
#define STAMP2(i)
#define CSTAMP2(i)
#define STAMP_DECL2
#define STAMP_FLUSH2
#define MFMA(a, b, c) __builtin_amdgcn_mfma_f32_32x32x16_bf16((a), (b), (c), 0, 0, 0)

constexpr unsigned SUB = 16384u;
constexpr unsigned RGB = 65536u;
constexpr unsigned MISC = 131072u;
#define STAG1 0
#define STAG2 0
#define WO4 ((wid & 1) + 2 * (wid >> 2))
#define PIPE_MU 1
#define NT1 3
struct SmSched {
  int mu[129];
  int tm[128];
  int rs[128];
  constexpr SmSched() : mu{}, tm{}, rs{} {
    int n[128] = {};
    for (int m = 0; m < 128; ++m) { tm[m] = -1; rs[m] = -1; }
    for (int j = 0; j < 8; ++j) tm[8 * j + (j == 0 ? 14 : (j == NT1 - 1 || j == 7) ? 9 : 10)] = j;
    const int sw = 64 + 8 * NT1;
    for (int d = 0; d < 4; ++d) rs[sw - 2 + d] = d;
    {
      const int a = 8 * NT1 + 3, e = 64; int av = 0, acc = 0;
      for (int m = a; m < e; ++m) if (tm[m] < 0) ++av;
      for (int m = a; m < e; ++m) if (tm[m] < 0) { acc += 8 * NT1; if (acc >= av) { acc -= av; ++n[m]; } }
    }
    {
      const int a = 67, e = 123; int av = 0, acc = 0;
      for (int m = a; m < e; ++m) if (rs[m] < 0) ++av;
      for (int m = a; m < e; ++m) if (rs[m] < 0) { acc += 64 - 8 * NT1; if (acc >= av) { acc -= av; ++n[m]; } }
    }
    int k = 0;
    for (int m = 0; m < 128; ++m) { mu[m] = k; k += n[m]; }
    mu[128] = k;
  }
  constexpr bool ok() const {
    if (mu[128] != 64) return false;
    for (int m = 0; m < 128; ++m)
      for (int k = mu[m]; k < mu[m + 1]; ++k) if (m + 1 >= 64 + 4 * (k >> 2)) return false;
    return true;
  }
};
constexpr SmSched SM{};
static_assert(SM.ok(), "softmax schedule misses a deadline");
constexpr int K1_LDS = 160 * 1024;
#define SCHED() __builtin_amdgcn_sched_barrier(0)
DI void rawbar() { asm volatile("s_barrier" ::: "memory"); }

typedef __attribute__((address_space(1))) unsigned long long gu64;
extern "C" __device__ unsigned long long hw_dispatch_id() __asm("llvm.amdgcn.dispatch.id");
DI constexpr unsigned wo_slot(int t) {
  constexpr unsigned pb[8] = {128u, 144u, 16u, 80u, 0u, 64u, 32u, 48u};
  return pb[t >> 1] * 1024u + (unsigned)(t & 1) * 8192u;
}
DI void wait_flags(gu64* f, bool on, unsigned long long tag) {
  for (unsigned spins = 0; spins < (1u << 20); ++spins) {
    const unsigned long long v = on ? __hip_atomic_load(f, __ATOMIC_RELAXED, __HIP_MEMORY_SCOPE_AGENT) : tag;
    if (__all(v == tag)) break;
    __builtin_amdgcn_s_sleep(1);
  }
  asm volatile("" ::: "memory");
}

__global__ __launch_bounds__(512, 2) void k_fused(const __hip_bfloat16* __restrict__ x, const __hip_bfloat16* __restrict__ kc,
                                                   const __hip_bfloat16* __restrict__ vc, const __hip_bfloat16* __restrict__ wq,
                                                   const __hip_bfloat16* __restrict__ wo, __hip_bfloat16* o_ws,
                                                   float* slab, unsigned long long* flags,
                                                   __hip_bfloat16* __restrict__ out) {
  extern __shared__ __attribute__((aligned(16))) char lds[];
  const int tid = threadIdx.x, lane = tid & 63;
  const int wid = __builtin_amdgcn_readfirstlane(tid >> 6);
  const int rg = wid & 3, kh = wid >> 2;
  const int l31 = lane & 31, h = lane >> 5;
  const int bid = blockIdx.x;
  const int hq = 4 * (bid & 7) + ((bid >> 3) & 3), b = bid >> 5;
  const int hkv = hq >> 1;
  STAMP_DECL;

  unsigned vx[2], vw[2], vkv[4];
#pragma unroll
  for (int e = 0; e < 2; ++e) {
    const int i = 2 * wid + e;
    int row = 8 * i + (lane >> 3);
    const int c = (lane & 7) ^ ((row >> 1) & 7);
    row = row < SQ ? row : SQ - 1;
    vx[e] = (unsigned)(row * (DM * 2) + c * 16 - e * 1024 + 1024);
    const int rw = 4 * i + (lane >> 4);
    const int cw = (lane & 15) ^ (((rw & 3) << 2) | ((rw >> 2) & 3));
    vw[e] = (unsigned)(rw * (KO * 2) + cw * 16 - e * 1024 + 1024);
  }
#pragma unroll
  for (int e = 0; e < 4; ++e) {
    const int r = 4 * (4 * (wid & 3) + e) + (lane >> 4);
    const int c = (lane & 15) ^ (((r & 3) << 2) | ((r >> 2) & 3));
    vkv[e] = (unsigned)((r - 4 * e) * 256 + c * 16);
  }
  const char* xb = (const char*)x + (size_t)b * SQ * DM * 2 - 1024;
  const char* wb = (const char*)wq + (size_t)hq * DH * 2 - 1024;
  const unsigned dX = (unsigned)wid * 2048u, dK = (unsigned)(kh * RGB + rg * 4096), dW = (unsigned)wid * 1024u;
  const size_t kvo = ((size_t)(b * HKV + hkv) * SK + 256 * kh) * DH * 2;
  const char* kb = (const char*)kc + kvo;
  const char* vb = (const char*)vc + kvo;

  auto issue1 = [&](auto Tc, auto Ec) {
    constexpr int t = decltype(Tc)::value, e = decltype(Ec)::value;
    constexpr unsigned sb = (unsigned)(t & 3) * SUB;
    if constexpr (t < 9) {
      if constexpr (e == 0) set_m0(dX, sb);
      if constexpr (e == 2) set_m0(dX, RGB + sb);
      if constexpr (e < 2) gld<e * 1024, 0>(xb + t * 128, vx[e]);
      else                 gld<(e - 2) * 1024, 0>(wb + (size_t)t * 64 * KO * 2, vw[e - 2]);
    } else if constexpr (t < 17) {
      if constexpr (e == 0) set_m0(dK, sb);
      if constexpr (t < 13) gld<e * 1024, 1>(kb + (t - 9) * 16384, vkv[e]);
      else                  gld<e * 1024, 1>(vb + (t - 13) * 16384, vkv[e]);
    }
  };
  STAMP(0); CSTAMP(20);
  sfor<0, 3>([&](auto Tc) { sfor<0, 4>([&](auto Ec) { issue1(Tc, Ec); }); });

  const int q4 = (lane & 15) >> 2, p4 = lane & 3, g16 = (lane >> 4) & 1;
  unsigned ax[4];
#pragma unroll
  for (int ks = 0; ks < 4; ++ks) ax[ks] = (unsigned)((32 * rg + l31) * 128 + (((2 * ks + h) ^ ((l31 >> 1) & 7)) * 16));
  unsigned aw[2][2];
#pragma unroll
  for (int dl = 0; dl < 2; ++dl)
#pragma unroll
    for (int t = 0; t < 2; ++t) {
      const int dt = 2 * kh + dl;
      const int k = 8 * h + 4 * t + q4;
      const int c = (4 * dt + 2 * g16 + (p4 & 1)) ^ ((q4 << 2) | ((2 * h + t) & 3));
      aw[dl][t] = (unsigned)(RGB + k * 256 + c * 16 + (p4 >> 1) * 8);
    }
  unsigned ak[8];
#pragma unroll
  for (int ks = 0; ks < 8; ++ks)
    ak[ks] = (unsigned)(kh * RGB + l31 * 256 + (((2 * ks + h) ^ (8 * kh) ^ (((l31 & 3) << 2) | ((l31 >> 2) & 3))) * 16));
  unsigned av[4][2];
#pragma unroll
  for (int dt = 0; dt < 4; ++dt)
#pragma unroll
    for (int t = 0; t < 2; ++t) {
      const int key = 8 * t + 4 * h + q4;
      const int c = (4 * dt + 2 * (p4 & 1) + g16) ^ ((q4 << 2) | ((2 * t + h) & 3));
      av[dt][t] = (unsigned)(kh * RGB + key * 256 + c * 16 + (p4 >> 1) * 8);
    }

  bf16x8 f1[3][3];
  auto ld1 = [&](int G) {
    const unsigned sb = (unsigned)((G >> 2) & 3) * SUB; const int ks = G & 3, r = G % 3;
    f1[r][0] = lds_b128(sb + ax[ks]);
    f1[r][1] = lds_tr2(sb + aw[0][0] + ks * 4096, sb + aw[0][1] + ks * 4096);
    f1[r][2] = lds_tr2(sb + aw[1][0] + ks * 4096, sb + aw[1][1] + ks * 4096);
  };
  bf16x8 f2[3][4];
  auto ld2 = [&](int G) {
    const int kt = G < 4 ? (G & 1) : ((G >> 1) & 1), hf = G < 4 ? (G >> 1) : (G & 1);
    const unsigned sb = (unsigned)((9 + (G >> 2)) & 3) * SUB + kt * 8192; const int r = G % 3;
#pragma unroll
    for (int j = 0; j < 4; ++j) f2[r][j] = lds_b128(sb + ak[4 * hf + j]);
  };
  auto ld3 = [&](int G) {
    const unsigned sb = (unsigned)((13 + (G >> 2)) & 3) * SUB + ((G >> 1) & 1) * 8192 + (G & 1) * 4096; const int r = (G + 1) % 3;
#pragma unroll
    for (int dt = 0; dt < 4; ++dt) f2[r][dt] = lds_tr2(sb + av[dt][0], sb + av[dt][1]);
  };

  wait_vm<8>(); rawbar();
  ld1(0); ld1(1);
  SCHED();

  f32x16 qa[2] = {};
  sfor<0, 9>([&](auto Tc) {
    constexpr int t = decltype(Tc)::value;
    if constexpr (t < 8) wait_vm<4>();
    rawbar();
    STAMP(1 + t);
    sfor<0, 4>([&](auto Kc) {
      constexpr int ks = decltype(Kc)::value;
      constexpr int G = 4 * t + ks;
      if constexpr (G == 34) { wait_vm<6>(); rawbar(); }
      issue1(IC<t + 3>{}, Kc);
      wait_lgkm<5>();
      if constexpr (G + 2 < 36) ld1(G + 2); else ld2(G + 2 - 36);
      qa[0] = MFMA(f1[G % 3][1], f1[G % 3][0], qa[0]);
      qa[1] = MFMA(f1[G % 3][2], f1[G % 3][0], qa[1]);
      SCHED();
    });
  });
  bf16x8 qf[8];
  qf[0] = pack8(qa[0], 0); qf[1] = pack8(qa[0], 1); qf[2] = pack8(qa[1], 0); qf[3] = pack8(qa[1], 1);
  {
    const unsigned qw = MISC + wid * 4096 + lane * 16;
#pragma unroll
    for (int f = 0; f < 4; ++f) *(LDSP bf16x8*)(uintptr_t)(qw + f * 1024) = qf[f];
  }
  SCHED();
  CSTAMP(22);
  const float CS = 0.08838834764831845f * 1.4426950408889634f;
  const int wo4 = WO4;
  f32x16 s[8];
  unsigned pw[16][4];
  float m1 = 0.f, mx = 0.f, mb = 0.f, ls0 = 0.f, ls1 = 0.f, al = 0.f;
  float rm0 = -__builtin_inff(), rm1 = -__builtin_inff();

  const int kq = (bid & 7) >> 1;
  const int tile0 = (bid & 1) ? 32 + (bid >> 3) : (bid >> 3);
  const bool activeB = tile0 < 63;
  const int tile = activeB ? tile0 : 0;
  const int tm = tile / 9, tn = tile % 9;
  const char* wob = (const char*)wo + ((size_t)kq * 1024 * DM + tn * 64) * 2;
  unsigned vwo;
  {
    const int row = 8 * wid + (lane >> 3);
    const int c = (lane & 7) ^ ((row >> 1) & 1);
    vwo = (unsigned)(row * (DM * 2) + c * 16);
  }
  auto issueWr = [&](int t) { set_m0(dW, wo_slot(t)); gld<0, 0>(wob + (size_t)t * 64 * DM * 2, vwo); };
  constexpr unsigned EMPTY = 0xFFFFFFFFu;
  const __amdgpu_buffer_rsrc_t rsl = __builtin_amdgcn_make_buffer_rsrc((char*)slab + (size_t)tile * 131072, 0, activeB ? 131072 : 0, 0x00020000);
  const int svo = kq * 8192 + wid * 1024 + lane * 16;

  f32x16 oa[4];
  float pe[2][2];
  auto vwork = [&](auto Mc) {
    constexpr int m = decltype(Mc)::value;
    if constexpr (SM.tm[m] >= 0) {
      constexpr int j = SM.tm[m];
#pragma unroll
      for (int r = 0; r < 16; r += 4) {
        rm0 = fmaxf(fmaxf(rm0, s[j][r]), s[j][r + 1]);
        rm1 = fmaxf(fmaxf(rm1, s[j][r + 2]), s[j][r + 3]);
      }
      if constexpr (j == NT1 - 1 || j == 7) {
        float m_ = fmaxf(rm0, rm1);
        auto rr = __builtin_amdgcn_permlane32_swap(__float_as_uint(m_), __float_as_uint(m_), false, false);
        m_ = fmaxf(__uint_as_float(rr[0]), __uint_as_float(rr[1]));
        if constexpr (j == 7) {
          mx = m_; mb = mx * CS;
          al = __builtin_amdgcn_exp2f((m1 - mx) * CS);
          ls0 *= al; ls1 *= al;
        } else { m1 = m_; mb = m1 * CS; }
      }
    }
    if constexpr (SM.rs[m] >= 0) {
      constexpr int d = SM.rs[m];
#pragma unroll
      for (int r = 0; r < 16; ++r) oa[d][r] *= al;
    }
    if constexpr (m > 0) {
      sfor<SM.mu[m - 1], SM.mu[m]>([&](auto Kc) {
        constexpr int k = decltype(Kc)::value, f = k >> 2, q = k & 3;
        ls0 += pe[k & 1][0]; ls1 += pe[k & 1][1];
        pw[f][q] = cvtpk(pe[k & 1][0], pe[k & 1][1]);
      });
    }
    sfor<SM.mu[m], SM.mu[m + 1]>([&](auto Kc) {
      constexpr int k = decltype(Kc)::value, f = k >> 2, q = k & 3;
      pe[k & 1][0] = __builtin_amdgcn_exp2f(fmaf(s[f >> 1][8 * (f & 1) + 2 * q], CS, -mb));
      pe[k & 1][1] = __builtin_amdgcn_exp2f(fmaf(s[f >> 1][8 * (f & 1) + 2 * q + 1], CS, -mb));
    });
  };
  sfor<0, 16>([&](auto Gc) {
    constexpr int G = decltype(Gc)::value, i = G >> 2, t = 9 + i;
    constexpr int kt = G < 4 ? (G & 1) : ((G >> 1) & 1), hf = G < 4 ? (G >> 1) : (G & 1);
    if constexpr ((G & 3) == 0) {
      if constexpr (i == 2) { wait_vm<7>(); rawbar(); } else if constexpr (i > 0) { wait_vm<4>(); rawbar(); }
      STAMP(1 + t);
    }
    if constexpr (G == 4) {
      const u32x4 e4 = {EMPTY, EMPTY, EMPTY, EMPTY};
#pragma unroll
      for (int o = 0; o < 3; ++o) __builtin_amdgcn_raw_buffer_store_b128(e4, rsl, svo, (o + (o >= kq)) * 32768, 16);
    }
    if constexpr (G == 1) {
      bar();
      const unsigned qr = MISC + (wid ^ 4) * 4096 + lane * 16;
#pragma unroll
      for (int f = 0; f < 4; ++f) qf[4 + f] = lds_b128(qr + f * 1024);
      issue1(IC<12>{}, IC<0>{});
    }
    if constexpr (G == 2) { wait_vm<6>(); rawbar(); }
    if constexpr (G > 0) issue1(IC<t + 3>{}, IC<(G & 3)>{});
    if constexpr (G == 15) wait_lgkm<8>(); else if constexpr (G == 1) wait_lgkm<8>(); else wait_lgkm<4>();
    if constexpr (G + 2 < 16) ld2(G + 2); else ld3(G + 2 - 16);
    sfor<0, 4>([&](auto Jc) {
      constexpr int j = decltype(Jc)::value;
      if constexpr (hf == 0 && j == 0) s[2 * i + kt] = MFMA(f2[G % 3][0], qf[0], f32x16{});
      else s[2 * i + kt] = MFMA(f2[G % 3][j], qf[4 * hf + j], s[2 * i + kt]);
      vwork(IC<4 * G + j>{});
      SCHED();
    });
  });
  sfor<0, 16>([&](auto Gc) {
    constexpr int G = decltype(Gc)::value, i = G >> 2, t = 13 + i;
    if constexpr ((G & 3) == 0) {
      if constexpr (i < 3) { wait_vm<4>(); rawbar(); }
      STAMP(1 + t);
    }
    issue1(IC<t + 3>{}, IC<(G & 3)>{});
    if constexpr (i == 1 || i == 2) issueWr(4 * (i - 1) + (G & 3));
    if constexpr (G < 15) wait_lgkm<8>(); else wait_lgkm<0>();
    if constexpr (G + 2 < 16) ld3(G + 2);
    const u32x4 pwv = {pw[G][0], pw[G][1], pw[G][2], pw[G][3]};
    sfor<0, 4>([&](auto Jc) {
      constexpr int dt = decltype(Jc)::value;
      if constexpr (G == 0) oa[dt] = MFMA(f2[(G + 1) % 3][dt], __builtin_bit_cast(bf16x8, pwv), f32x16{});
      else oa[dt] = MFMA(f2[(G + 1) % 3][dt], __builtin_bit_cast(bf16x8, pwv), oa[dt]);
      vwork(IC<64 + 4 * G + dt>{});
      SCHED();
    });
  });
  float ls = ls0 + ls1;
  {
    auto rr = __builtin_amdgcn_permlane32_swap(__float_as_uint(ls), __float_as_uint(ls), false, false);
    ls = __uint_as_float(rr[0]) + __uint_as_float(rr[1]);
  }
  const unsigned long long tag = ((hw_dispatch_id() + 1ull) * 0x9E3779B97F4A7C15ull) ^ (unsigned long long)(uintptr_t)__builtin_amdgcn_dispatch_ptr();
  gu64* flagA = (gu64*)flags;
  gu64* flagB = (gu64*)flags + 512;
  bar();
  STAMP(18); CSTAMP(23);
  issueWr(8); issueWr(9); issueWr(10);
  {
    constexpr unsigned MLB = 72u * 1024u;
    const unsigned xw = (unsigned)(32768 + kh * RGB + rg * 8192) + lane * 16, xr = (unsigned)(32768 + (kh ^ 1) * RGB + rg * 8192) + lane * 16;
    auto put = [&](const f32x16& v, int dl) {
#pragma unroll
      for (int g = 0; g < 4; ++g) {
        f32x4 w = {v[4 * g], v[4 * g + 1], v[4 * g + 2], v[4 * g + 3]};
        *(LDSP f32x4*)(uintptr_t)(xw + (dl * 4 + g) * 1024) = w;
      }
    };
    if (kh == 0) { put(oa[2], 0); put(oa[3], 1); } else { put(oa[0], 0); put(oa[1], 1); }
    f32x2 ml = {mx, ls};
    *(LDSP f32x2*)(uintptr_t)(MLB + wid * 512 + lane * 8) = ml;
    bar();
    STAMP2(1);
    const f32x2 pml = *(const LDSP f32x2*)(uintptr_t)(MLB + (wid ^ 4) * 512 + lane * 8);
    const float mt = fmaxf(mx, pml[0]);
    const float f0 = __builtin_amdgcn_exp2f((mx - mt) * CS), f1_ = __builtin_amdgcn_exp2f((pml[0] - mt) * CS);
    const float inv = 1.f / (f0 * ls + f1_ * pml[1]);
    const float g0 = f0 * inv, g1 = f1_ * inv;
    const int qrow = 32 * rg + l31, R = b * SQ + qrow;
    const __amdgpu_buffer_rsrc_t rs = __builtin_amdgcn_make_buffer_rsrc((char*)o_ws + (size_t)hq * MROWS * (DH * 2), 0, MROWS * DH * 2, 0x00020000);
    const int so = (R >> 5) * 8192 + (4 * kh + h) * 1024 + (R & 31) * 16;
    auto fin = [&](const f32x16& v, int dl) {
      float r[16];
#pragma unroll
      for (int g = 0; g < 4; ++g) {
        const f32x4 w = *(const LDSP f32x4*)(uintptr_t)(xr + (dl * 4 + g) * 1024);
#pragma unroll
        for (int e = 0; e < 4; ++e) r[4 * g + e] = v[4 * g + e] * g0 + w[e] * g1;
      }
      const u32x4 w0 = {cvtpk(r[0], r[1]), cvtpk(r[2], r[3]), cvtpk(r[4], r[5]), cvtpk(r[6], r[7])};
      const u32x4 w1 = {cvtpk(r[8], r[9]), cvtpk(r[10], r[11]), cvtpk(r[12], r[13]), cvtpk(r[14], r[15])};
      if (qrow < SQ) {
        __builtin_amdgcn_raw_buffer_store_b128(w0, rs, so + dl * 2048, 0, 16);
        __builtin_amdgcn_raw_buffer_store_b128(w1, rs, so + dl * 2048 + 512, 0, 16);
      }
    };
    if (kh == 0) { fin(oa[0], 0); fin(oa[1], 1); } else { fin(oa[2], 0); fin(oa[3], 1); }
  }
  STAMP2(3);
  asm volatile("s_waitcnt vmcnt(0)" ::: "memory");
  STAMP2(4);
  __syncthreads();
  STAMP2(5);
  if (tid == 0) __hip_atomic_store(flagA + (b * 4 + (hq >> 3)) * 8 + (hq & 7), tag, __ATOMIC_RELAXED, __HIP_MEMORY_SCOPE_AGENT);
  STAMP(19); CSTAMP(21);
  if (!activeB) { STAMP_FLUSH; return; }

  {
    const int mt = wid & 3, k2 = wid >> 2, nh = k2;
#pragma unroll
    for (int t = 11; t < 16; ++t) issueWr(t);
    const int q4 = (lane & 15) >> 2, p4 = lane & 3, g16 = (lane >> 4) & 1;
    unsigned aw[2][2];
#pragma unroll
    for (int n2 = 0; n2 < 2; ++n2)
#pragma unroll
      for (int t = 0; t < 2; ++t) {
        const int k = 8 * h + 4 * t + q4;
        const int c = (4 * g16 + 2 * (p4 >> 1) + n2) ^ ((q4 >> 1) & 1);
        aw[n2][t] = (unsigned)(k2 * 8192 + k * 128 + c * 16 + (p4 & 1) * 8);
      }
    const int T = 4 * tm + mt;
    const int b0 = (32 * T) / SQ, b1 = (32 * T + 31) / SQ;
    gu64* myf = lane < 16 ? flagA + (((lane & 8) ? b1 : b0) * 4 + kq) * 8 + (lane & 7) : flagA + (b * 4 + (lane & 3)) * 8 + (hq & 7);
    unsigned rdy = 0;
    auto need = [&](unsigned msk) {
      if ((rdy & msk) != msk) {
        for (unsigned spins = 0; spins < (1u << 20); ++spins) {
          const unsigned long long v = lane < 20 ? __hip_atomic_load(myf, __ATOMIC_RELAXED, __HIP_MEMORY_SCOPE_AGENT) : tag;
          const unsigned m = (unsigned)__ballot(v == tag);
          rdy = (m & (m >> 8) & 0xffu) | ((m >> 8) & 0xf00u);
          if ((rdy & msk) == msk) break;
          __builtin_amdgcn_s_sleep(1);
        }
        asm volatile("" ::: "memory");
      }
    };
    const __amdgpu_buffer_rsrc_t ro = __builtin_amdgcn_make_buffer_rsrc((char*)o_ws + (size_t)kq * 8 * MROWS * (DH * 2), 0, 8 * MROWS * DH * 2, 0x00020000);
    u32x4 of[8][4];
    const int ovo = T * 8192 + k2 * 4096 + lane * 16;
    f32x16 acc[2] = {};
    bf16x8 fa[2][2][2];
    auto ldw = [&](int q) {
      const unsigned base = wo_slot(2 * (q >> 2)) + (q & 3) * 2048;
      fa[q & 1][0][0] = lds_tr2(base + aw[0][0], base + aw[0][1]);
      fa[q & 1][1][0] = lds_tr2(base + aw[1][0], base + aw[1][1]);
    };
    ldw(0);
#define OLAG 3
#pragma unroll
    for (int sI = 0; sI < 8 + OLAG; ++sI) {
      if (sI < 8) {
        need(1u << sI);
        if (sI == 0) { STAMP(26); }
#pragma unroll
        for (int j = 0; j < 4; ++j) of[sI][j] = __builtin_amdgcn_raw_buffer_load_b128(ro, ovo + j * 1024, sI * MROWS * (DH * 2), 16);
        SCHED();
      }
      if (sI >= OLAG) {
#pragma unroll
        for (int j = 0; j < 4; ++j) {
          const int q = 4 * (sI - OLAG) + j;
          if (q == 19) rawbar();
          if (q == 0) { STAMP(27); }
          if (q + 1 < 32) ldw(q + 1);
          const bf16x8 ob = __builtin_bit_cast(bf16x8, of[q >> 2][q & 3]);
          acc[0] = MFMA(fa[q & 1][0][0], ob, acc[0]);
          acc[1] = MFMA(fa[q & 1][1][0], ob, acc[1]);
          SCHED();
        }
      }
    }
    STAMP(24);
    {
      const unsigned ew = 96u * 1024u + wid * 4096 + lane * 16, er = 96u * 1024u + (wid ^ 4) * 4096 + lane * 16;
      auto put = [&](const f32x16& v) {
#pragma unroll
        for (int g = 0; g < 4; ++g) {
          f32x4 w = {v[4 * g], v[4 * g + 1], v[4 * g + 2], v[4 * g + 3]};
          *(LDSP f32x4*)(uintptr_t)(ew + g * 1024) = w;
        }
      };
      if (k2 == 0) put(acc[1]); else put(acc[0]);
      bar();
      auto get = [&](const f32x16& own, bool first) {
        f32x16 o;
#pragma unroll
        for (int g = 0; g < 4; ++g) {
          const f32x4 w = *(const LDSP f32x4*)(uintptr_t)(er + g * 1024);
#pragma unroll
          for (int e = 0; e < 4; ++e) o[4 * g + e] = own[4 * g + e] + w[e];
        }
        return o;
      };
      if (k2 == 0) acc[0] = get(acc[0], true); else acc[0] = get(acc[1], false);
    }
    {
#pragma unroll
      for (int g = 0; g < 4; ++g)
        if (g != kq) {
          u32x4 w;
#pragma unroll
          for (int e = 0; e < 4; ++e) w[e] = min(__float_as_uint(acc[0][4 * g + e]), EMPTY - 1u);
          __builtin_amdgcn_raw_buffer_store_b128(w, rsl, svo, g * 32768, 16);
        }
      float own[4];
#pragma unroll
      for (int e = 0; e < 4; ++e) own[e] = kq == 0 ? acc[0][e] : kq == 1 ? acc[0][4 + e] : kq == 2 ? acc[0][8 + e] : acc[0][12 + e];
      need(0xf00u);
      const int lvo = kq * 32768 + wid * 1024 + lane * 16;
      u32x4 pv[3];
      for (unsigned spins = 0; spins < (1u << 20); ++spins) {
#pragma unroll
        for (int o = 0; o < 3; ++o) pv[o] = __builtin_amdgcn_raw_buffer_load_b128(rsl, lvo, (o + (o >= kq)) * 8192, 16);
        unsigned mx_ = 0;
#pragma unroll
        for (int o = 0; o < 3; ++o)
#pragma unroll
          for (int e = 0; e < 4; ++e) mx_ = max(mx_, pv[o][e]);
        if (__all(mx_ != EMPTY)) break;
        __builtin_amdgcn_s_sleep(1);
        asm volatile("" ::: "memory");
      }
      float r[4];
#pragma unroll
      for (int e = 0; e < 4; ++e) r[e] = ((own[e] + __uint_as_float(pv[0][e])) + __uint_as_float(pv[1][e])) + __uint_as_float(pv[2][e]);
      char* dst = (char*)out + ((size_t)(tm * 128 + 32 * mt + l31) * DM + tn * 64 + 16 * kq + 8 * nh + 4 * h) * 2;
      bf16x4 ww = __builtin_bit_cast(bf16x4, (unsigned long long)cvtpk(r[0], r[1]) | ((unsigned long long)cvtpk(r[2], r[3]) << 32));
      *(bf16x4*)dst = ww;
    }
  }
  STAMP(25);
  STAMP_FLUSH;
}

extern "C" void kernel_launch(void* const* d_in, const int* in_sizes, int n_in, void* d_out, int out_size, void* d_ws,
                              size_t ws_size, hipStream_t stream) {
  const __hip_bfloat16* x = (const __hip_bfloat16*)d_in[0];
  const __hip_bfloat16* kc = (const __hip_bfloat16*)d_in[1];
  const __hip_bfloat16* vc = (const __hip_bfloat16*)d_in[2];
  const __hip_bfloat16* wq = (const __hip_bfloat16*)d_in[3];
  const __hip_bfloat16* wo = (const __hip_bfloat16*)d_in[4];
  __hip_bfloat16* o_ws = (__hip_bfloat16*)d_ws;
  float* slab = (float*)((char*)d_ws + (size_t)8 * 1024 * 1024);
  unsigned long long* flags = (unsigned long long*)((char*)d_ws + (size_t)24 * 1024 * 1024);
  hipFuncSetAttribute((const void*)k_fused, hipFuncAttributeMaxDynamicSharedMemorySize, K1_LDS);
  hipLaunchKernelGGL(k_fused, dim3(256), dim3(512), K1_LDS, stream, x, kc, vc, wq, wo, o_ws, slab, flags, (__hip_bfloat16*)d_out);
}
```

```cpp
#include <hip/hip_runtime.h>
#include <hip/hip_bf16.h>
#include <stdint.h>

typedef __attribute__((ext_vector_type(8))) __bf16 bf16x8;
typedef __attribute__((ext_vector_type(4))) __bf16 bf16x4;
typedef __attribute__((ext_vector_type(2))) __bf16 bf16x2;
typedef __attribute__((ext_vector_type(16))) float f32x16;
typedef __attribute__((ext_vector_type(4))) float f32x4;
typedef __attribute__((ext_vector_type(2))) float f32x2;
typedef __attribute__((ext_vector_type(4))) unsigned u32x4;
#define LDSP __attribute__((address_space(3)))
#define DI __device__ __forceinline__

constexpr int NB = 8, SQ = 112, DM = 576, HQ = 32, HKV = 16, SK = 512, DH = 128;
constexpr int MROWS = NB * SQ;
constexpr int KO = HQ * DH;

DI unsigned cvtpk(float lo, float hi) {
  f32x2 v = {lo, hi};
  bf16x2 b = __builtin_convertvector(v, bf16x2);
  return __builtin_bit_cast(unsigned, b);
}
DI void glds16(const void* sbase, unsigned voff, unsigned lds_dst) {
  unsigned keep;
  asm volatile("s_mov_b32 %0, m0\n\ts_mov_b32 m0, %3\n\ts_nop 0\n\tglobal_load_lds_dwordx4 %1, %2\n\ts_mov_b32 m0, %0"
               : "=&s"(keep) : "v"(voff), "s"(sbase), "s"(lds_dst) : "memory");
}
DI void glds16nt(const void* sbase, unsigned voff, unsigned lds_dst) {
  unsigned keep;
  asm volatile("s_mov_b32 %0, m0\n\ts_mov_b32 m0, %3\n\ts_nop 0\n\tglobal_load_lds_dwordx4 %1, %2 nt\n\ts_mov_b32 m0, %0"
               : "=&s"(keep) : "v"(voff), "s"(sbase), "s"(lds_dst) : "memory");
}
DI void glds16sc1(const void* sbase, unsigned voff, unsigned lds_dst) {
  unsigned keep;
  asm volatile("s_mov_b32 %0, m0\n\ts_mov_b32 m0, %3\n\ts_nop 0\n\tglobal_load_lds_dwordx4 %1, %2 sc1\n\ts_mov_b32 m0, %0"
               : "=&s"(keep) : "v"(voff), "s"(sbase), "s"(lds_dst) : "memory");
}
template <int J> DI void glds16_if(const void* sbase, unsigned voff, unsigned lds_dst, int sel) {
  unsigned keep;
  asm volatile("s_cmp_lg_u32 %4, %5\n\ts_cbranch_scc1 .Lgl%=\n\ts_mov_b32 %0, m0\n\ts_mov_b32 m0, %3\n\ts_nop 0\n\tglobal_load_lds_dwordx4 %1, %2\n\ts_mov_b32 m0, %0\n.Lgl%=:"
               : "=&s"(keep) : "v"(voff), "s"(sbase), "s"(lds_dst), "s"(sel), "i"(J) : "memory", "scc");
}
template <int J> DI void glds16nt_if(const void* sbase, unsigned voff, unsigned lds_dst, int sel) {
  unsigned keep;
  asm volatile("s_cmp_lg_u32 %4, %5\n\ts_cbranch_scc1 .Lgl%=\n\ts_mov_b32 %0, m0\n\ts_mov_b32 m0, %3\n\ts_nop 0\n\tglobal_load_lds_dwordx4 %1, %2 nt\n\ts_mov_b32 m0, %0\n.Lgl%=:"
               : "=&s"(keep) : "v"(voff), "s"(sbase), "s"(lds_dst), "s"(sel), "i"(J) : "memory", "scc");
}
template <bool NT, int J> DI void gl_t(const void* sbase, unsigned voff, unsigned lds_dst, int sel) {
  if constexpr (J < 0) { if constexpr (NT) glds16nt(sbase, voff, lds_dst); else glds16(sbase, voff, lds_dst); }
  else { if constexpr (NT) glds16nt_if<J>(sbase, voff, lds_dst, sel); else glds16_if<J>(sbase, voff, lds_dst, sel); }
}
template <int V> struct IC { static constexpr int value = V; };
template <int I, int N, class F> DI void sfor(F&& f) { if constexpr (I < N) { f(IC<I>{}); sfor<I + 1, N>(f); } }
DI void gl_sel(bool nt, int j, const void* sbase, unsigned voff, unsigned lds_dst, int sel) {
  if (nt) {
    if (j < 0) glds16nt(sbase, voff, lds_dst);
    else if (j == 0) glds16nt_if<0>(sbase, voff, lds_dst, sel); else if (j == 1) glds16nt_if<1>(sbase, voff, lds_dst, sel);
    else if (j == 2) glds16nt_if<2>(sbase, voff, lds_dst, sel); else glds16nt_if<3>(sbase, voff, lds_dst, sel);
  } else {
    if (j < 0) glds16(sbase, voff, lds_dst);
    else if (j == 0) glds16_if<0>(sbase, voff, lds_dst, sel); else if (j == 1) glds16_if<1>(sbase, voff, lds_dst, sel);
    else if (j == 2) glds16_if<2>(sbase, voff, lds_dst, sel); else glds16_if<3>(sbase, voff, lds_dst, sel);
  }
}
DI void set_m0(unsigned base, unsigned imm) { asm volatile("s_add_u32 m0, %0, %1\n\ts_nop 0" ::"s"(base), "i"(imm) : "scc"); }
template <int OFF, int POL> DI void gld(const void* sbase, unsigned voff) {
  if constexpr (POL == 0) asm volatile("global_load_lds_dwordx4 %0, %1 offset:%2" ::"v"(voff), "s"(sbase), "i"(OFF) : "memory");
  else if constexpr (POL == 1) asm volatile("global_load_lds_dwordx4 %0, %1 offset:%2 nt" ::"v"(voff), "s"(sbase), "i"(OFF) : "memory");
  else asm volatile("global_load_lds_dwordx4 %0, %1 offset:%2 sc1" ::"v"(voff), "s"(sbase), "i"(OFF) : "memory");
}
template <int N> DI void wait_lgkm() { __builtin_amdgcn_s_waitcnt(0xC07F | (N << 8)); }
#define GL_KV glds16nt
#define GL_XW glds16
#define GL_WO glds16
#define GL_O glds16
template <int N> DI void wait_vm() { asm volatile("s_waitcnt vmcnt(%0)" ::"i"(N) : "memory"); }
DI void bar() { asm volatile("s_waitcnt lgkmcnt(0)\n\ts_barrier" ::: "memory"); }
DI bf16x8 lds_b128(unsigned a) { return *(const LDSP bf16x8*)(uintptr_t)a; }
DI bf16x8 lds_tr2(unsigned a0, unsigned a1) {
  bf16x4 lo = __builtin_amdgcn_ds_read_tr16_b64_v4bf16((LDSP bf16x4*)(uintptr_t)a0);
  bf16x4 hi = __builtin_amdgcn_ds_read_tr16_b64_v4bf16((LDSP bf16x4*)(uintptr_t)a1);
  return __builtin_shufflevector(lo, hi, 0, 1, 2, 3, 4, 5, 6, 7);
}
DI bf16x8 pack8(const f32x16& x, int s) {
  u32x4 w = {cvtpk(x[8 * s], x[8 * s + 1]), cvtpk(x[8 * s + 2], x[8 * s + 3]), cvtpk(x[8 * s + 4], x[8 * s + 5]),
             cvtpk(x[8 * s + 6], x[8 * s + 7])};
  return __builtin_bit_cast(bf16x8, w);
}
#define STAMP(i)
#define CSTAMP(i)
#define STAMP_DECL
#define STAMP_FLUSH
#define STAMP2(i)
#define CSTAMP2(i)
#define STAMP_DECL2
#define STAMP_FLUSH2
#define MFMA(a, b, c) __builtin_amdgcn_mfma_f32_32x32x16_bf16((a), (b), (c), 0, 0, 0)

constexpr unsigned SUB = 16384u;
constexpr unsigned RGB = 65536u;
constexpr unsigned MISC = 131072u;
#define STAG1 0
#define STAG2 0
#define WO4 ((wid & 1) + 2 * (wid >> 2))
#define PIPE_MU 1
#define NT1 3
constexpr int pv_f(int g, int j) { return g < 12 ? g : 12 + 2 * (g & 1) + (j >> 1); }
constexpr int pv_d(int g, int j) { return g < 12 ? j : (g < 14 ? 2 : 0) + (j & 1); }
constexpr int pv_first(int f) { return f < 12 ? 64 + 4 * f : 112 + 2 * (f - 12); }
#define SMEND 115
struct SmSched {
  int mu[129];
  int tm[128];
  int rs[128];
  constexpr SmSched() : mu{}, tm{}, rs{} {
    int n[128] = {};
    for (int m = 0; m < 128; ++m) { tm[m] = -1; rs[m] = -1; }
    for (int j = 0; j < 8; ++j) tm[8 * j + (j == 0 ? 14 : (j == NT1 - 1 || j == 7) ? 9 : 10)] = j;
    const int sw = 64 + 8 * NT1;
    for (int d = 0; d < 4; ++d) rs[sw - 2 + d] = d;
    {
      const int a = 8 * NT1 + 3, e = 64; int av = 0, acc = 0;
      for (int m = a; m < e; ++m) if (tm[m] < 0) ++av;
      for (int m = a; m < e; ++m) if (tm[m] < 0) { acc += 8 * NT1; if (acc >= av) { acc -= av; ++n[m]; } }
    }
    {
      const int a = 67, e = SMEND; int av = 0, acc = 0;
      for (int m = a; m < e; ++m) if (rs[m] < 0) ++av;
      for (int m = a; m < e; ++m) if (rs[m] < 0) { acc += 64 - 8 * NT1; if (acc >= av) { acc -= av; ++n[m]; } }
    }
    int k = 0;
    for (int m = 0; m < 128; ++m) { mu[m] = k; k += n[m]; }
    mu[128] = k;
  }
  constexpr bool ok() const {
    if (mu[128] != 64) return false;
    for (int m = 0; m < 128; ++m)
      for (int k = mu[m]; k < mu[m + 1]; ++k) if (m + 1 >= pv_first(k >> 2)) return false;
    return true;
  }
};
constexpr SmSched SM{};
static_assert(SM.ok(), "softmax schedule misses a deadline");
constexpr int K1_LDS = 160 * 1024;
#define SCHED() __builtin_amdgcn_sched_barrier(0)
DI void rawbar() { asm volatile("s_barrier" ::: "memory"); }

typedef __attribute__((address_space(1))) unsigned long long gu64;
extern "C" __device__ unsigned long long hw_dispatch_id() __asm("llvm.amdgcn.dispatch.id");
DI constexpr unsigned wo_slot(int t) {
  constexpr unsigned pb[8] = {128u, 144u, 16u, 80u, 0u, 64u, 32u, 48u};
  return pb[t >> 1] * 1024u + (unsigned)(t & 1) * 8192u;
}
DI void wait_flags(gu64* f, bool on, unsigned long long tag) {
  for (unsigned spins = 0; spins < (1u << 20); ++spins) {
    const unsigned long long v = on ? __hip_atomic_load(f, __ATOMIC_RELAXED, __HIP_MEMORY_SCOPE_AGENT) : tag;
    if (__all(v == tag)) break;
    __builtin_amdgcn_s_sleep(1);
  }
  asm volatile("" ::: "memory");
}

__global__ __launch_bounds__(512, 2) void k_fused(const __hip_bfloat16* __restrict__ x, const __hip_bfloat16* __restrict__ kc,
                                                   const __hip_bfloat16* __restrict__ vc, const __hip_bfloat16* __restrict__ wq,
                                                   const __hip_bfloat16* __restrict__ wo, __hip_bfloat16* o_ws,
                                                   float* slab, unsigned long long* flags,
                                                   __hip_bfloat16* __restrict__ out) {
  extern __shared__ __attribute__((aligned(16))) char lds[];
  const int tid = threadIdx.x, lane = tid & 63;
  const int wid = __builtin_amdgcn_readfirstlane(tid >> 6);
  const int rg = wid & 3, kh = wid >> 2;
  const int l31 = lane & 31, h = lane >> 5;
  const int bid = blockIdx.x;
  const int hq = 4 * (bid & 7) + ((bid >> 3) & 3), b = bid >> 5;
  const int hkv = hq >> 1;
  STAMP_DECL;

  unsigned vx[2], vw[2], vkv[4];
#pragma unroll
  for (int e = 0; e < 2; ++e) {
    const int i = 2 * wid + e;
    int row = 8 * i + (lane >> 3);
    const int c = (lane & 7) ^ ((row >> 1) & 7);
    row = row < SQ ? row : SQ - 1;
    vx[e] = (unsigned)(row * (DM * 2) + c * 16 - e * 1024 + 1024);
    const int rw = 4 * i + (lane >> 4);
    const int cw = (lane & 15) ^ (((rw & 3) << 2) | ((rw >> 2) & 3));
    vw[e] = (unsigned)(rw * (KO * 2) + cw * 16 - e * 1024 + 1024);
  }
#pragma unroll
  for (int e = 0; e < 4; ++e) {
    const int r = 4 * (4 * (wid & 3) + e) + (lane >> 4);
    const int c = (lane & 15) ^ (((r & 3) << 2) | ((r >> 2) & 3));
    vkv[e] = (unsigned)((r - 4 * e) * 256 + c * 16);
  }
  const char* xb = (const char*)x + (size_t)b * SQ * DM * 2 - 1024;
  const char* wb = (const char*)wq + (size_t)hq * DH * 2 - 1024;
  const unsigned dX = (unsigned)wid * 2048u, dK = (unsigned)(kh * RGB + rg * 4096), dW = (unsigned)wid * 1024u;
  const size_t kvo = ((size_t)(b * HKV + hkv) * SK + 256 * kh) * DH * 2;
  const char* kb = (const char*)kc + kvo;
  const char* vb = (const char*)vc + kvo;

  auto issue1 = [&](auto Tc, auto Ec) {
    constexpr int t = decltype(Tc)::value, e = decltype(Ec)::value;
    constexpr unsigned sb = (unsigned)(t & 3) * SUB;
    if constexpr (t < 9) {
      if constexpr (e == 0) set_m0(dX, sb);
      if constexpr (e == 2) set_m0(dX, RGB + sb);
      if constexpr (e < 2) gld<e * 1024, 0>(xb + t * 128, vx[e]);
      else                 gld<(e - 2) * 1024, 0>(wb + (size_t)t * 64 * KO * 2, vw[e - 2]);
    } else if constexpr (t < 17) {
      if constexpr (e == 0) set_m0(dK, sb);
      if constexpr (t < 13) gld<e * 1024, 1>(kb + (t - 9) * 16384, vkv[e]);
      else                  gld<e * 1024, 1>(vb + (t - 13) * 16384, vkv[e]);
    }
  };
  STAMP(0); CSTAMP(20);
  sfor<0, 3>([&](auto Tc) { sfor<0, 4>([&](auto Ec) { issue1(Tc, Ec); }); });

  const int q4 = (lane & 15) >> 2, p4 = lane & 3, g16 = (lane >> 4) & 1;
  unsigned ax[4];
#pragma unroll
  for (int ks = 0; ks < 4; ++ks) ax[ks] = (unsigned)((32 * rg + l31) * 128 + (((2 * ks + h) ^ ((l31 >> 1) & 7)) * 16));
  unsigned aw[2][2];
#pragma unroll
  for (int dl = 0; dl < 2; ++dl)
#pragma unroll
    for (int t = 0; t < 2; ++t) {
      const int dt = 2 * kh + dl;
      const int k = 8 * h + 4 * t + q4;
      const int c = (4 * dt + 2 * g16 + (p4 & 1)) ^ ((q4 << 2) | ((2 * h + t) & 3));
      aw[dl][t] = (unsigned)(RGB + k * 256 + c * 16 + (p4 >> 1) * 8);
    }
  unsigned ak[8];
#pragma unroll
  for (int ks = 0; ks < 8; ++ks)
    ak[ks] = (unsigned)(kh * RGB + l31 * 256 + (((2 * ks + h) ^ (8 * kh) ^ (((l31 & 3) << 2) | ((l31 >> 2) & 3))) * 16));
  unsigned av[4][2];
#pragma unroll
  for (int dt = 0; dt < 4; ++dt)
#pragma unroll
    for (int t = 0; t < 2; ++t) {
      const int key = 8 * t + 4 * h + q4;
      const int c = (4 * (dt ^ (2 * kh)) + 2 * (p4 & 1) + g16) ^ ((q4 << 2) | ((2 * t + h) & 3));
      av[dt][t] = (unsigned)(kh * RGB + key * 256 + c * 16 + (p4 >> 1) * 8);
    }

  bf16x8 f1[3][3];
  auto ld1 = [&](int G) {
    const unsigned sb = (unsigned)((G >> 2) & 3) * SUB; const int ks = G & 3, r = G % 3;
    f1[r][0] = lds_b128(sb + ax[ks]);
    f1[r][1] = lds_tr2(sb + aw[0][0] + ks * 4096, sb + aw[0][1] + ks * 4096);
    f1[r][2] = lds_tr2(sb + aw[1][0] + ks * 4096, sb + aw[1][1] + ks * 4096);
  };
  bf16x8 f2[3][4];
  auto ld2 = [&](int G) {
    const int kt = G < 4 ? (G & 1) : ((G >> 1) & 1), hf = G < 4 ? (G >> 1) : (G & 1);
    const unsigned sb = (unsigned)((9 + (G >> 2)) & 3) * SUB + kt * 8192; const int r = G % 3;
#pragma unroll
    for (int j = 0; j < 4; ++j) f2[r][j] = lds_b128(sb + ak[4 * hf + j]);
  };
  auto ld3 = [&](int G) {
    const int r = (G + 1) % 3;
#pragma unroll
    for (int j = 0; j < 4; ++j) {
      const int f = pv_f(G, j), dt = pv_d(G, j);
      const unsigned sb = (unsigned)((13 + (f >> 2)) & 3) * SUB + ((f >> 1) & 1) * 8192 + (f & 1) * 4096;
      f2[r][j] = lds_tr2(sb + av[dt][0], sb + av[dt][1]);
    }
  };

  wait_vm<8>(); rawbar();
  ld1(0); ld1(1);
  SCHED();

  f32x16 qa[2] = {};
  sfor<0, 9>([&](auto Tc) {
    constexpr int t = decltype(Tc)::value;
    if constexpr (t < 8) wait_vm<4>();
    rawbar();
    STAMP(1 + t);
    sfor<0, 4>([&](auto Kc) {
      constexpr int ks = decltype(Kc)::value;
      constexpr int G = 4 * t + ks;
      if constexpr (G == 34) { wait_vm<6>(); rawbar(); }
      issue1(IC<t + 3>{}, Kc);
      wait_lgkm<5>();
      if constexpr (G + 2 < 36) ld1(G + 2); else ld2(G + 2 - 36);
      qa[0] = MFMA(f1[G % 3][1], f1[G % 3][0], qa[0]);
      qa[1] = MFMA(f1[G % 3][2], f1[G % 3][0], qa[1]);
      SCHED();
    });
  });
  bf16x8 qf[8];
  qf[0] = pack8(qa[0], 0); qf[1] = pack8(qa[0], 1); qf[2] = pack8(qa[1], 0); qf[3] = pack8(qa[1], 1);
  {
    const unsigned qw = MISC + wid * 4096 + lane * 16;
#pragma unroll
    for (int f = 0; f < 4; ++f) *(LDSP bf16x8*)(uintptr_t)(qw + f * 1024) = qf[f];
  }
  SCHED();
  CSTAMP(22);
  const float CS = 0.08838834764831845f * 1.4426950408889634f;
  const int wo4 = WO4;
  f32x16 s[8];
  unsigned pw[16][4];
  float m1 = 0.f, mx = 0.f, mb = 0.f, ls0 = 0.f, ls1 = 0.f, al = 0.f;
  float rm0 = -__builtin_inff(), rm1 = -__builtin_inff();

  const int kq = (bid & 7) >> 1;
  const int tile0 = (bid & 1) ? 32 + (bid >> 3) : (bid >> 3);
  const bool activeB = tile0 < 63;
  const int tile = activeB ? tile0 : 0;
  const int tm = tile / 9, tn = tile % 9;
  const char* wob = (const char*)wo + ((size_t)kq * 1024 * DM + tn * 64) * 2;
  unsigned vwo;
  {
    const int row = 8 * wid + (lane >> 3);
    const int c = (lane & 7) ^ (((row >> 1) & 1) << 2);
    vwo = (unsigned)(row * (DM * 2) + c * 16);
  }
  auto issueWr = [&](int t) { set_m0(dW, wo_slot(t)); gld<0, 0>(wob + (size_t)t * 64 * DM * 2, vwo); };

  f32x16 oa[4];
  float pe[2][2];
  unsigned xw = 0;
  constexpr unsigned MLB = 32768u + 3 * 8192u + 256u;
  auto vwork = [&](auto Mc) {
    constexpr int m = decltype(Mc)::value;
    if constexpr (SM.tm[m] >= 0) {
      constexpr int j = SM.tm[m];
#pragma unroll
      for (int r = 0; r < 16; r += 4) {
        rm0 = fmaxf(fmaxf(rm0, s[j][r]), s[j][r + 1]);
        rm1 = fmaxf(fmaxf(rm1, s[j][r + 2]), s[j][r + 3]);
      }
      if constexpr (j == NT1 - 1 || j == 7) {
        float m_ = fmaxf(rm0, rm1);
        auto rr = __builtin_amdgcn_permlane32_swap(__float_as_uint(m_), __float_as_uint(m_), false, false);
        m_ = fmaxf(__uint_as_float(rr[0]), __uint_as_float(rr[1]));
        if constexpr (j == 7) {
          mx = m_; mb = mx * CS;
          al = __builtin_amdgcn_exp2f((m1 - mx) * CS);
          ls0 *= al; ls1 *= al;
        } else { m1 = m_; mb = m1 * CS; }
      }
    }
    if constexpr (SM.rs[m] >= 0) {
      constexpr int d = SM.rs[m];
#pragma unroll
      for (int r = 0; r < 16; ++r) oa[d][r] *= al;
    }
    if constexpr (m > 0) {
      sfor<SM.mu[m - 1], SM.mu[m]>([&](auto Kc) {
        constexpr int k = decltype(Kc)::value, f = k >> 2, q = k & 3;
        ls0 += pe[k & 1][0]; ls1 += pe[k & 1][1];
        pw[f][q] = cvtpk(pe[k & 1][0], pe[k & 1][1]);
      });
    }
    if constexpr (m >= 120) {
      constexpr int dl = (m - 120) >> 2, g = (m - 120) & 3;
      if constexpr (m == 120) {
        int ln = lane; asm volatile("" : "+v"(ln));
        const int le = (rg == 3 && (ln & 16)) ? (ln | 48) : ln;
        xw = (unsigned)(32768 + kh * RGB + rg * 8192) + le * 16;
        float ls = ls0 + ls1;
        auto rr = __builtin_amdgcn_permlane32_swap(__float_as_uint(ls), __float_as_uint(ls), false, false);
        ls0 = __uint_as_float(rr[0]) + __uint_as_float(rr[1]);
        f32x2 ml = {mx, ls0};
        *(LDSP f32x2*)(uintptr_t)(MLB + wid * 1024 + (ln & 31) * 8) = ml;
      }
      f32x4 w = {oa[2 + dl][4 * g], oa[2 + dl][4 * g + 1], oa[2 + dl][4 * g + 2], oa[2 + dl][4 * g + 3]};
      *(LDSP f32x4*)(uintptr_t)(xw + (dl * 4 + g) * 1024) = w;
    }
    sfor<SM.mu[m], SM.mu[m + 1]>([&](auto Kc) {
      constexpr int k = decltype(Kc)::value, f = k >> 2, q = k & 3;
      pe[k & 1][0] = __builtin_amdgcn_exp2f(fmaf(s[f >> 1][8 * (f & 1) + 2 * q], CS, -mb));
      pe[k & 1][1] = __builtin_amdgcn_exp2f(fmaf(s[f >> 1][8 * (f & 1) + 2 * q + 1], CS, -mb));
    });
  };
  sfor<0, 16>([&](auto Gc) {
    constexpr int G = decltype(Gc)::value, i = G >> 2, t = 9 + i;
    constexpr int kt = G < 4 ? (G & 1) : ((G >> 1) & 1), hf = G < 4 ? (G >> 1) : (G & 1);
    if constexpr ((G & 3) == 0) {
      if constexpr (i > 0) { wait_vm<4>(); rawbar(); }
      STAMP(1 + t);
    }
    if constexpr (G == 1) {
      bar();
      const unsigned qr = MISC + (wid ^ 4) * 4096 + lane * 16;
#pragma unroll
      for (int f = 0; f < 4; ++f) qf[4 + f] = lds_b128(qr + f * 1024);
      issue1(IC<12>{}, IC<0>{});
    }
    if constexpr (G == 2) { wait_vm<6>(); rawbar(); }
    if constexpr (G > 0) issue1(IC<t + 3>{}, IC<(G & 3)>{});
    if constexpr (G == 15) wait_lgkm<8>(); else if constexpr (G == 1) wait_lgkm<8>(); else wait_lgkm<4>();
    if constexpr (G + 2 < 16) ld2(G + 2); else ld3(G + 2 - 16);
    sfor<0, 4>([&](auto Jc) {
      constexpr int j = decltype(Jc)::value;
      if constexpr (hf == 0 && j == 0) s[2 * i + kt] = MFMA(f2[G % 3][0], qf[0], f32x16{});
      else s[2 * i + kt] = MFMA(f2[G % 3][j], qf[4 * hf + j], s[2 * i + kt]);
      vwork(IC<4 * G + j>{});
      SCHED();
    });
  });
  sfor<0, 16>([&](auto Gc) {
    constexpr int G = decltype(Gc)::value, i = G >> 2, t = 13 + i;
    if constexpr ((G & 3) == 0) {
      if constexpr (i < 3) { wait_vm<4>(); rawbar(); }
      else rawbar();
      STAMP(1 + t);
    }
    issue1(IC<t + 3>{}, IC<(G & 3)>{});
    if constexpr (i == 1 || i == 2) issueWr(4 * (i - 1) + (G & 3));
    if constexpr (G < 15) wait_lgkm<8>(); else wait_lgkm<0>();
    if constexpr (G + 2 < 16) ld3(G + 2);
    sfor<0, 4>([&](auto Jc) {
      constexpr int j = decltype(Jc)::value, f = pv_f(G, j), dt = pv_d(G, j);
      const u32x4 pwv = {pw[f][0], pw[f][1], pw[f][2], pw[f][3]};
      if constexpr (G == 0) oa[dt] = MFMA(f2[(G + 1) % 3][j], __builtin_bit_cast(bf16x8, pwv), f32x16{});
      else oa[dt] = MFMA(f2[(G + 1) % 3][j], __builtin_bit_cast(bf16x8, pwv), oa[dt]);
      vwork(IC<64 + 4 * G + j>{});
      SCHED();
    });
  });
  const float ls = ls0;
  const unsigned long long tag = ((hw_dispatch_id() + 1ull) * 0x9E3779B97F4A7C15ull) ^ (unsigned long long)(uintptr_t)__builtin_amdgcn_dispatch_ptr();
  gu64* flagA = (gu64*)flags;
  gu64* flagB = (gu64*)flags + 512;
  bar();
  STAMP(18); CSTAMP(23);
  {
    const unsigned xr = (unsigned)(32768 + (kh ^ 1) * RGB + rg * 8192) + lane * 16;
    const f32x2 pml = *(const LDSP f32x2*)(uintptr_t)(MLB + (wid ^ 4) * 1024 + l31 * 8);
    const float mt = fmaxf(mx, pml[0]);
    const float f0 = __builtin_amdgcn_exp2f((mx - mt) * CS), f1_ = __builtin_amdgcn_exp2f((pml[0] - mt) * CS);
    const float inv = 1.f / (f0 * ls + f1_ * pml[1]);
    const float g0 = f0 * inv, g1 = f1_ * inv;
    const int qrow = 32 * rg + l31, R = b * SQ + qrow;
    const __amdgpu_buffer_rsrc_t rs = __builtin_amdgcn_make_buffer_rsrc((char*)o_ws + (size_t)hq * MROWS * (DH * 2), 0, MROWS * DH * 2, 0x00020000);
    const int so = (R >> 5) * 8192 + (4 * kh + h) * 1024 + (R & 31) * 16;
    auto fin = [&](const f32x16& v, int dl) {
      float r[16];
#pragma unroll
      for (int g = 0; g < 4; ++g) {
        const f32x4 w = *(const LDSP f32x4*)(uintptr_t)(xr + (dl * 4 + g) * 1024);
#pragma unroll
        for (int e = 0; e < 4; ++e) r[4 * g + e] = v[4 * g + e] * g0 + w[e] * g1;
      }
      const u32x4 w0 = {cvtpk(r[0], r[1]), cvtpk(r[2], r[3]), cvtpk(r[4], r[5]), cvtpk(r[6], r[7])};
      const u32x4 w1 = {cvtpk(r[8], r[9]), cvtpk(r[10], r[11]), cvtpk(r[12], r[13]), cvtpk(r[14], r[15])};
      if (qrow < SQ) {
        __builtin_amdgcn_raw_buffer_store_b128(w0, rs, so + dl * 2048, 0, 16);
        __builtin_amdgcn_raw_buffer_store_b128(w1, rs, so + dl * 2048 + 512, 0, 16);
      }
    };
    fin(oa[0], 0); fin(oa[1], 1);
  }
  STAMP2(3);
  issueWr(8); issueWr(9); issueWr(10); issueWr(11);
  asm volatile("s_waitcnt vmcnt(4)" ::: "memory");
  STAMP2(4);
  __syncthreads();
  STAMP2(5);
  if (tid == 0) __hip_atomic_store(flagA + (b * 4 + (hq >> 3)) * 8 + (hq & 7), tag, __ATOMIC_RELAXED, __HIP_MEMORY_SCOPE_AGENT);
  STAMP(19); CSTAMP(21);
  if (!activeB) { STAMP_FLUSH; return; }

  {
    const int mt = wid & 3, k2 = wid >> 2, nh = k2;
#pragma unroll
    for (int t = 12; t < 16; ++t) issueWr(t);
    const int q4 = (lane & 15) >> 2, p4 = lane & 3, g16 = (lane >> 4) & 1;
    unsigned aw[2][2];
#pragma unroll
    for (int n2 = 0; n2 < 2; ++n2)
#pragma unroll
      for (int t = 0; t < 2; ++t) {
        const int k = 8 * h + 4 * t + q4;
        const int c = (4 * n2 + 2 * (p4 & 1) + g16) ^ (((q4 >> 1) & 1) << 2);
        aw[n2][t] = (unsigned)(k2 * 8192 + k * 128 + c * 16 + (p4 >> 1) * 8);
      }
    const int T = 4 * tm + mt;
    const int b0 = (32 * T) / SQ, b1 = (32 * T + 31) / SQ;
    gu64* myf = flagA + (((lane & 8) ? b1 : b0) * 4 + kq) * 8 + (lane & 7);
    unsigned rdy = 0;
    auto need = [&](int sI) {
      if (!((rdy >> sI) & 1u)) {
        for (unsigned spins = 0; spins < (1u << 20); ++spins) {
          const unsigned long long v = lane < 16 ? __hip_atomic_load(myf, __ATOMIC_RELAXED, __HIP_MEMORY_SCOPE_AGENT) : tag;
          const unsigned m = (unsigned)__ballot(v == tag);
          rdy = m & (m >> 8) & 0xffu;
          if ((rdy >> sI) & 1u) break;
          __builtin_amdgcn_s_sleep(1);
        }
        asm volatile("" ::: "memory");
      }
    };
    const __amdgpu_buffer_rsrc_t ro = __builtin_amdgcn_make_buffer_rsrc((char*)o_ws + (size_t)kq * 8 * MROWS * (DH * 2), 0, 8 * MROWS * DH * 2, 0x00020000);
    u32x4 of[8][4];
    const int ovo = T * 8192 + k2 * 4096 + lane * 16;
    f32x16 acc[2] = {};
    bf16x8 fa[2][2][2];
    auto ldw = [&](int q) {
      const unsigned base = wo_slot(2 * (q >> 2)) + (q & 3) * 2048;
      fa[q & 1][0][0] = lds_tr2(base + aw[0][0], base + aw[0][1]);
      fa[q & 1][1][0] = lds_tr2(base + aw[1][0], base + aw[1][1]);
    };
    ldw(0);
#define OLAG 3
#pragma unroll
    for (int sI = 0; sI < 8 + OLAG; ++sI) {
      if (sI < 8) {
        need(sI);
        if (sI == 0) { STAMP(26); }
#pragma unroll
        for (int j = 0; j < 4; ++j) of[sI][j] = __builtin_amdgcn_raw_buffer_load_b128(ro, ovo + j * 1024, sI * MROWS * (DH * 2), 16);
        SCHED();
      }
      if (sI >= OLAG) {
#pragma unroll
        for (int j = 0; j < 4; ++j) {
          const int q = 4 * (sI - OLAG) + j;
          if (q == 15) rawbar();
          if (q == 0) { STAMP(27); }
          if (q + 1 < 32) ldw(q + 1);
          const bf16x8 ob = __builtin_bit_cast(bf16x8, of[q >> 2][q & 3]);
          acc[0] = MFMA(fa[q & 1][0][0], ob, acc[0]);
          acc[1] = MFMA(fa[q & 1][1][0], ob, acc[1]);
          SCHED();
        }
      }
    }
    STAMP(24);
    {
      const unsigned ew = 96u * 1024u + wid * 4096 + lane * 16, er = 96u * 1024u + (wid ^ 4) * 4096 + lane * 16;
      auto put = [&](const f32x16& v) {
#pragma unroll
        for (int g = 0; g < 4; ++g) {
          f32x4 w = {v[4 * g], v[4 * g + 1], v[4 * g + 2], v[4 * g + 3]};
          *(LDSP f32x4*)(uintptr_t)(ew + g * 1024) = w;
        }
      };
      if (k2 == 0) put(acc[1]); else put(acc[0]);
      bar();
      auto get = [&](const f32x16& own, bool first) {
        f32x16 o;
#pragma unroll
        for (int g = 0; g < 4; ++g) {
          const f32x4 w = *(const LDSP f32x4*)(uintptr_t)(er + g * 1024);
#pragma unroll
          for (int e = 0; e < 4; ++e) o[4 * g + e] = own[4 * g + e] + w[e];
        }
        return o;
      };
      if (k2 == 0) acc[0] = get(acc[0], true); else acc[0] = get(acc[1], false);
    }
    float* sl = slab + (size_t)tile * 4 * 4 * 2 * 1024;
    if (mt != kq) {
      const __amdgpu_buffer_rsrc_t rs = __builtin_amdgcn_make_buffer_rsrc((void*)(sl + ((mt * 4 + kq) * 2 + nh) * 1024), 0, 4096, 0x00020000);
#pragma unroll
      for (int g = 0; g < 4; ++g) {
        f32x4 w = {acc[0][4 * g], acc[0][4 * g + 1], acc[0][4 * g + 2], acc[0][4 * g + 3]};
        __builtin_amdgcn_raw_buffer_store_b128(__builtin_bit_cast(u32x4, w), rs, g * 1024 + lane * 16, 0, 16);
      }
    }
    asm volatile("s_waitcnt vmcnt(0)" ::: "memory");
    __syncthreads();
    if (wid == 0 && lane < 4 && lane != kq)
      __hip_atomic_store(flagB + (tile * 4 + lane) * 4 + kq, tag, __ATOMIC_RELAXED, __HIP_MEMORY_SCOPE_AGENT);
    if (mt == kq) {
      wait_flags(flagB + (tile * 4 + kq) * 4 + (lane & 3), lane < 4 && lane != kq, tag);
      u32x4 pv[3][4];
#pragma unroll
      for (int o = 0; o < 3; ++o) {
        const int src = o + (o >= kq);
        const __amdgpu_buffer_rsrc_t rs = __builtin_amdgcn_make_buffer_rsrc((void*)(sl + ((kq * 4 + src) * 2 + nh) * 1024), 0, 4096, 0x00020000);
#pragma unroll
        for (int g = 0; g < 4; ++g) pv[o][g] = __builtin_amdgcn_raw_buffer_load_b128(rs, g * 1024 + lane * 16, 0, 16);
      }
      float r[16];
#pragma unroll
      for (int g = 0; g < 4; ++g) {
        const f32x4 a = __builtin_bit_cast(f32x4, pv[0][g]), c = __builtin_bit_cast(f32x4, pv[1][g]), d = __builtin_bit_cast(f32x4, pv[2][g]);
#pragma unroll
        for (int e = 0; e < 4; ++e) {
          const float own = acc[0][4 * g + e];
          const float t0 = kq == 0 ? own : a[e];
          const float t1 = kq == 0 ? a[e] : (kq == 1 ? own : c[e]);
          const float t2 = kq <= 1 ? c[e] : (kq == 2 ? own : d[e]);
          const float t3 = kq == 3 ? own : d[e];
          r[4 * g + e] = ((t0 + t1) + t2) + t3;
        }
      }
      char* dst = (char*)out + ((size_t)(tm * 128 + 32 * mt + l31) * DM + tn * 64 + nh * 32 + 16 * h) * 2;
      u32x4 w0 = {cvtpk(r[0], r[1]), cvtpk(r[2], r[3]), cvtpk(r[4], r[5]), cvtpk(r[6], r[7])};
      u32x4 w1 = {cvtpk(r[8], r[9]), cvtpk(r[10], r[11]), cvtpk(r[12], r[13]), cvtpk(r[14], r[15])};
      *(u32x4*)dst = w0;
      *(u32x4*)(dst + 16) = w1;
    }
  }
  STAMP(25);
  STAMP_FLUSH;
}

extern "C" void kernel_launch(void* const* d_in, const int* in_sizes, int n_in, void* d_out, int out_size, void* d_ws,
                              size_t ws_size, hipStream_t stream) {
  const __hip_bfloat16* x = (const __hip_bfloat16*)d_in[0];
  const __hip_bfloat16* kc = (const __hip_bfloat16*)d_in[1];
  const __hip_bfloat16* vc = (const __hip_bfloat16*)d_in[2];
  const __hip_bfloat16* wq = (const __hip_bfloat16*)d_in[3];
  const __hip_bfloat16* wo = (const __hip_bfloat16*)d_in[4];
  __hip_bfloat16* o_ws = (__hip_bfloat16*)d_ws;
  float* slab = (float*)((char*)d_ws + (size_t)8 * 1024 * 1024);
  unsigned long long* flags = (unsigned long long*)((char*)d_ws + (size_t)24 * 1024 * 1024);
  hipFuncSetAttribute((const void*)k_fused, hipFuncAttributeMaxDynamicSharedMemorySize, K1_LDS);
  hipLaunchKernelGGL(k_fused, dim3(256), dim3(512), K1_LDS, stream, x, kc, vc, wq, wo, o_ws, slab, flags, (__hip_bfloat16*)d_out);
}
```

```cpp
#include <hip/hip_runtime.h>
#include <hip/hip_bf16.h>
#include <stdint.h>

typedef __attribute__((ext_vector_type(8))) __bf16 bf16x8;
typedef __attribute__((ext_vector_type(4))) __bf16 bf16x4;
typedef __attribute__((ext_vector_type(2))) __bf16 bf16x2;
typedef __attribute__((ext_vector_type(16))) float f32x16;
typedef __attribute__((ext_vector_type(4))) float f32x4;
typedef __attribute__((ext_vector_type(2))) float f32x2;
typedef __attribute__((ext_vector_type(4))) unsigned u32x4;
#define LDSP __attribute__((address_space(3)))
#define DI __device__ __forceinline__

constexpr int NB = 8, SQ = 112, DM = 576, HQ = 32, HKV = 16, SK = 512, DH = 128;
constexpr int MROWS = NB * SQ;
constexpr int KO = HQ * DH;

DI unsigned cvtpk(float lo, float hi) {
  f32x2 v = {lo, hi};
  bf16x2 b = __builtin_convertvector(v, bf16x2);
  return __builtin_bit_cast(unsigned, b);
}
DI void glds16(const void* sbase, unsigned voff, unsigned lds_dst) {
  unsigned keep;
  asm volatile("s_mov_b32 %0, m0\n\ts_mov_b32 m0, %3\n\ts_nop 0\n\tglobal_load_lds_dwordx4 %1, %2\n\ts_mov_b32 m0, %0"
               : "=&s"(keep) : "v"(voff), "s"(sbase), "s"(lds_dst) : "memory");
}
DI void glds16nt(const void* sbase, unsigned voff, unsigned lds_dst) {
  unsigned keep;
  asm volatile("s_mov_b32 %0, m0\n\ts_mov_b32 m0, %3\n\ts_nop 0\n\tglobal_load_lds_dwordx4 %1, %2 nt\n\ts_mov_b32 m0, %0"
               : "=&s"(keep) : "v"(voff), "s"(sbase), "s"(lds_dst) : "memory");
}
DI void glds16sc1(const void* sbase, unsigned voff, unsigned lds_dst) {
  unsigned keep;
  asm volatile("s_mov_b32 %0, m0\n\ts_mov_b32 m0, %3\n\ts_nop 0\n\tglobal_load_lds_dwordx4 %1, %2 sc1\n\ts_mov_b32 m0, %0"
               : "=&s"(keep) : "v"(voff), "s"(sbase), "s"(lds_dst) : "memory");
}
template <int J> DI void glds16_if(const void* sbase, unsigned voff, unsigned lds_dst, int sel) {
  unsigned keep;
  asm volatile("s_cmp_lg_u32 %4, %5\n\ts_cbranch_scc1 .Lgl%=\n\ts_mov_b32 %0, m0\n\ts_mov_b32 m0, %3\n\ts_nop 0\n\tglobal_load_lds_dwordx4 %1, %2\n\ts_mov_b32 m0, %0\n.Lgl%=:"
               : "=&s"(keep) : "v"(voff), "s"(sbase), "s"(lds_dst), "s"(sel), "i"(J) : "memory", "scc");
}
template <int J> DI void glds16nt_if(const void* sbase, unsigned voff, unsigned lds_dst, int sel) {
  unsigned keep;
  asm volatile("s_cmp_lg_u32 %4, %5\n\ts_cbranch_scc1 .Lgl%=\n\ts_mov_b32 %0, m0\n\ts_mov_b32 m0, %3\n\ts_nop 0\n\tglobal_load_lds_dwordx4 %1, %2 nt\n\ts_mov_b32 m0, %0\n.Lgl%=:"
               : "=&s"(keep) : "v"(voff), "s"(sbase), "s"(lds_dst), "s"(sel), "i"(J) : "memory", "scc");
}
template <bool NT, int J> DI void gl_t(const void* sbase, unsigned voff, unsigned lds_dst, int sel) {
  if constexpr (J < 0) { if constexpr (NT) glds16nt(sbase, voff, lds_dst); else glds16(sbase, voff, lds_dst); }
  else { if constexpr (NT) glds16nt_if<J>(sbase, voff, lds_dst, sel); else glds16_if<J>(sbase, voff, lds_dst, sel); }
}
template <int V> struct IC { static constexpr int value = V; };
template <int I, int N, class F> DI void sfor(F&& f) { if constexpr (I < N) { f(IC<I>{}); sfor<I + 1, N>(f); } }
DI void gl_sel(bool nt, int j, const void* sbase, unsigned voff, unsigned lds_dst, int sel) {
  if (nt) {
    if (j < 0) glds16nt(sbase, voff, lds_dst);
    else if (j == 0) glds16nt_if<0>(sbase, voff, lds_dst, sel); else if (j == 1) glds16nt_if<1>(sbase, voff, lds_dst, sel);
    else if (j == 2) glds16nt_if<2>(sbase, voff, lds_dst, sel); else glds16nt_if<3>(sbase, voff, lds_dst, sel);
  } else {
    if (j < 0) glds16(sbase, voff, lds_dst);
    else if (j == 0) glds16_if<0>(sbase, voff, lds_dst, sel); else if (j == 1) glds16_if<1>(sbase, voff, lds_dst, sel);
    else if (j == 2) glds16_if<2>(sbase, voff, lds_dst, sel); else glds16_if<3>(sbase, voff, lds_dst, sel);
  }
}
DI void set_m0(unsigned base, unsigned imm) { asm volatile("s_add_u32 m0, %0, %1\n\ts_nop 0" ::"s"(base), "i"(imm) : "scc"); }
template <int OFF, int POL> DI void gld(const void* sbase, unsigned voff) {
  if constexpr (POL == 0) asm volatile("global_load_lds_dwordx4 %0, %1 offset:%2" ::"v"(voff), "s"(sbase), "i"(OFF) : "memory");
  else if constexpr (POL == 1) asm volatile("global_load_lds_dwordx4 %0, %1 offset:%2 nt" ::"v"(voff), "s"(sbase), "i"(OFF) : "memory");
  else asm volatile("global_load_lds_dwordx4 %0, %1 offset:%2 sc1" ::"v"(voff), "s"(sbase), "i"(OFF) : "memory");
}
template <int N> DI void wait_lgkm() { __builtin_amdgcn_s_waitcnt(0xC07F | (N << 8)); }
#define GL_KV glds16nt
#define GL_XW glds16
#define GL_WO glds16
#define GL_O glds16
template <int N> DI void wait_vm() { asm volatile("s_waitcnt vmcnt(%0)" ::"i"(N) : "memory"); }
DI void bar() { asm volatile("s_waitcnt lgkmcnt(0)\n\ts_barrier" ::: "memory"); }
DI bf16x8 lds_b128(unsigned a) { return *(const LDSP bf16x8*)(uintptr_t)a; }
DI bf16x8 lds_tr2(unsigned a0, unsigned a1) {
  bf16x4 lo = __builtin_amdgcn_ds_read_tr16_b64_v4bf16((LDSP bf16x4*)(uintptr_t)a0);
  bf16x4 hi = __builtin_amdgcn_ds_read_tr16_b64_v4bf16((LDSP bf16x4*)(uintptr_t)a1);
  return __builtin_shufflevector(lo, hi, 0, 1, 2, 3, 4, 5, 6, 7);
}
DI bf16x8 pack8(const f32x16& x, int s) {
  u32x4 w = {cvtpk(x[8 * s], x[8 * s + 1]), cvtpk(x[8 * s + 2], x[8 * s + 3]), cvtpk(x[8 * s + 4], x[8 * s + 5]),
             cvtpk(x[8 * s + 6], x[8 * s + 7])};
  return __builtin_bit_cast(bf16x8, w);
}
#define STAMP(i)
#define CSTAMP(i)
#define STAMP_DECL
#define STAMP_FLUSH
#define STAMP2(i)
#define CSTAMP2(i)
#define STAMP_DECL2
#define STAMP_FLUSH2
#define MFMA(a, b, c) __builtin_amdgcn_mfma_f32_32x32x16_bf16((a), (b), (c), 0, 0, 0)

constexpr unsigned SUB = 16384u;
constexpr unsigned RGB = 65536u;
constexpr unsigned MISC = 131072u;
#define STAG1 0
#define STAG2 0
#define WO4 ((wid & 1) + 2 * (wid >> 2))
#define PIPE_MU 1
#define PRIO 3
#define HEADSKIP 2
#define NT1 3
struct SmSched {
  int mu[129];
  int tm[128];
  int rs[128];
  constexpr SmSched() : mu{}, tm{}, rs{} {
    int n[128] = {};
    for (int m = 0; m < 128; ++m) { tm[m] = -1; rs[m] = -1; }
    for (int j = 0; j < 8; ++j) tm[8 * j + (j == 0 ? 14 : (j == NT1 - 1 || j == 7) ? 9 : 10)] = j;
    const int sw = 64 + 8 * NT1;
    for (int d = 0; d < 4; ++d) rs[sw - 2 + d] = d;
    {
      const int a = 8 * NT1 + 3, e = 64; int av = 0, acc = 0;
      for (int m = a; m < e; ++m) if (tm[m] < 0 && (m & 15) >= HEADSKIP) ++av;
      for (int m = a; m < e; ++m) if (tm[m] < 0 && (m & 15) >= HEADSKIP) { acc += 8 * NT1; while (acc >= av) { acc -= av; ++n[m]; } }
    }
    {
      const int a = 67, e = 123; int av = 0, acc = 0;
      for (int m = a; m < e; ++m) if (rs[m] < 0 && (m & 15) >= HEADSKIP) ++av;
      for (int m = a; m < e; ++m) if (rs[m] < 0 && (m & 15) >= HEADSKIP) { acc += 64 - 8 * NT1; while (acc >= av) { acc -= av; ++n[m]; } }
    }
    int k = 0;
    for (int m = 0; m < 128; ++m) { mu[m] = k; k += n[m]; }
    mu[128] = k;
  }
  constexpr bool ok() const {
    if (mu[128] != 64) return false;
    for (int m = 0; m < 128; ++m)
      for (int k = mu[m]; k < mu[m + 1]; ++k) if (m + 1 >= 64 + 4 * (k >> 2)) return false;
    return true;
  }
};
constexpr SmSched SM{};
static_assert(SM.ok(), "softmax schedule misses a deadline");
constexpr int K1_LDS = 160 * 1024;
#define SCHED() __builtin_amdgcn_sched_barrier(0)
#define NOPK(x) asm("" : "+v"(x))
DI void rawbar() { asm volatile("s_barrier" ::: "memory"); }

typedef __attribute__((address_space(1))) unsigned long long gu64;
extern "C" __device__ unsigned long long hw_dispatch_id() __asm("llvm.amdgcn.dispatch.id");
DI constexpr unsigned wo_slot(int t) {
  constexpr unsigned pb[8] = {128u, 144u, 16u, 80u, 0u, 64u, 32u, 48u};
  return pb[t >> 1] * 1024u + (unsigned)(t & 1) * 8192u;
}
DI void wait_flags(gu64* f, bool on, unsigned long long tag) {
  for (unsigned spins = 0; spins < (1u << 20); ++spins) {
    const unsigned long long v = on ? __hip_atomic_load(f, __ATOMIC_RELAXED, __HIP_MEMORY_SCOPE_AGENT) : tag;
    if (__all(v == tag)) break;
    __builtin_amdgcn_s_sleep(1);
  }
  asm volatile("" ::: "memory");
}

__global__ __launch_bounds__(512, 2) void k_fused(const __hip_bfloat16* __restrict__ x, const __hip_bfloat16* __restrict__ kc,
                                                   const __hip_bfloat16* __restrict__ vc, const __hip_bfloat16* __restrict__ wq,
                                                   const __hip_bfloat16* __restrict__ wo, __hip_bfloat16* o_ws,
                                                   float* slab, unsigned long long* flags,
                                                   __hip_bfloat16* __restrict__ out) {
  extern __shared__ __attribute__((aligned(16))) char lds[];
  const int tid = threadIdx.x, lane = tid & 63;
  const int wid = __builtin_amdgcn_readfirstlane(tid >> 6);
  const int rg = wid & 3, kh = wid >> 2;
  const int l31 = lane & 31, h = lane >> 5;
  const int bid = blockIdx.x;
  const int hq = 4 * (bid & 7) + ((bid >> 3) & 3), b = bid >> 5;
  const int hkv = hq >> 1;
  STAMP_DECL;

  unsigned vx[2], vw[2], vkv[4];
#pragma unroll
  for (int e = 0; e < 2; ++e) {
    const int i = 2 * wid + e;
    int row = 8 * i + (lane >> 3);
    const int c = (lane & 7) ^ ((row >> 1) & 7);
    row = row < SQ ? row : SQ - 1;
    vx[e] = (unsigned)(row * (DM * 2) + c * 16 - e * 1024 + 1024);
    const int rw = 4 * i + (lane >> 4);
    const int cw = (lane & 15) ^ (((rw & 3) << 2) | ((rw >> 2) & 3));
    vw[e] = (unsigned)(rw * (KO * 2) + cw * 16 - e * 1024 + 1024);
  }
#pragma unroll
  for (int e = 0; e < 4; ++e) {
    const int r = 4 * (4 * (wid & 3) + e) + (lane >> 4);
    const int c = (lane & 15) ^ (((r & 3) << 2) | ((r >> 2) & 3));
    vkv[e] = (unsigned)((r - 4 * e) * 256 + c * 16);
  }
  const char* xb = (const char*)x + (size_t)b * SQ * DM * 2 - 1024;
  const char* wb = (const char*)wq + (size_t)hq * DH * 2 - 1024;
  const unsigned dX = (unsigned)wid * 2048u, dK = (unsigned)(kh * RGB + rg * 4096), dW = (unsigned)wid * 1024u;
  const size_t kvo = ((size_t)(b * HKV + hkv) * SK + 256 * kh) * DH * 2;
  const char* kb = (const char*)kc + kvo;
  const char* vb = (const char*)vc + kvo;

  auto issue1 = [&](auto Tc, auto Ec) {
    constexpr int t = decltype(Tc)::value, e = decltype(Ec)::value;
    constexpr unsigned sb = (unsigned)(t & 3) * SUB;
    if constexpr (t < 9) {
      if constexpr (e == 0) set_m0(dX, sb);
      if constexpr (e == 2) set_m0(dX, RGB + sb);
      if constexpr (e < 2) gld<e * 1024, 0>(xb + t * 128, vx[e]);
      else                 gld<(e - 2) * 1024, 0>(wb + (size_t)t * 64 * KO * 2, vw[e - 2]);
    } else if constexpr (t < 17) {
      if constexpr (e == 0) set_m0(dK, sb);
      if constexpr (t < 13) gld<e * 1024, 1>(kb + (t - 9) * 16384, vkv[e]);
      else                  gld<e * 1024, 1>(vb + (t - 13) * 16384, vkv[e]);
    }
  };
  if (kh) __builtin_amdgcn_s_setprio(1);
  STAMP(0); CSTAMP(20);
  sfor<0, 3>([&](auto Tc) { sfor<0, 4>([&](auto Ec) { issue1(Tc, Ec); }); });

  const int q4 = (lane & 15) >> 2, p4 = lane & 3, g16 = (lane >> 4) & 1;
  unsigned ax[4];
#pragma unroll
  for (int ks = 0; ks < 4; ++ks) ax[ks] = (unsigned)((32 * rg + l31) * 128 + (((2 * ks + h) ^ ((l31 >> 1) & 7)) * 16));
  unsigned aw[2][2];
#pragma unroll
  for (int dl = 0; dl < 2; ++dl)
#pragma unroll
    for (int t = 0; t < 2; ++t) {
      const int dt = 2 * kh + dl;
      const int k = 8 * h + 4 * t + q4;
      const int c = (4 * dt + 2 * g16 + (p4 & 1)) ^ ((q4 << 2) | ((2 * h + t) & 3));
      aw[dl][t] = (unsigned)(RGB + k * 256 + c * 16 + (p4 >> 1) * 8);
    }
  unsigned ak[8];
#pragma unroll
  for (int ks = 0; ks < 8; ++ks)
    ak[ks] = (unsigned)(kh * RGB + l31 * 256 + (((2 * ks + h) ^ (8 * kh) ^ (((l31 & 3) << 2) | ((l31 >> 2) & 3))) * 16));
  unsigned av[4][2];
#pragma unroll
  for (int dt = 0; dt < 4; ++dt)
#pragma unroll
    for (int t = 0; t < 2; ++t) {
      const int key = 8 * t + 4 * h + q4;
      const int c = (4 * (dt ^ (2 * kh)) + 2 * (p4 & 1) + g16) ^ ((q4 << 2) | ((2 * t + h) & 3));
      av[dt][t] = (unsigned)(kh * RGB + key * 256 + c * 16 + (p4 >> 1) * 8);
    }

  bf16x8 f1[3][3];
  auto ld1 = [&](int G) {
    const unsigned sb = (unsigned)((G >> 2) & 3) * SUB; const int ks = G & 3, r = G % 3;
    f1[r][0] = lds_b128(sb + ax[ks]);
    f1[r][1] = lds_tr2(sb + aw[0][0] + ks * 4096, sb + aw[0][1] + ks * 4096);
    f1[r][2] = lds_tr2(sb + aw[1][0] + ks * 4096, sb + aw[1][1] + ks * 4096);
  };
  bf16x8 f2[3][4];
  auto ld2 = [&](int G) {
    const int kt = G < 4 ? (G & 1) : ((G >> 1) & 1), hf = G < 4 ? (G >> 1) : (G & 1);
    const unsigned sb = (unsigned)((9 + (G >> 2)) & 3) * SUB + kt * 8192; const int r = G % 3;
#pragma unroll
    for (int j = 0; j < 4; ++j) f2[r][j] = lds_b128(sb + ak[4 * hf + j]);
  };
  auto ld3 = [&](int G) {
    const unsigned sb = (unsigned)((13 + (G >> 2)) & 3) * SUB + ((G >> 1) & 1) * 8192 + (G & 1) * 4096; const int r = (G + 1) % 3;
#pragma unroll
    for (int dt = 0; dt < 4; ++dt) f2[r][dt] = lds_tr2(sb + av[dt][0], sb + av[dt][1]);
  };

  wait_vm<8>(); rawbar();
  ld1(0); ld1(1);
  SCHED();

  f32x16 qa[2] = {};
  sfor<0, 9>([&](auto Tc) {
    constexpr int t = decltype(Tc)::value;
    if constexpr (t < 8) wait_vm<4>();
    rawbar();
    STAMP(1 + t);
    sfor<0, 4>([&](auto Kc) {
      constexpr int ks = decltype(Kc)::value;
      constexpr int G = 4 * t + ks;
      if constexpr (G == 34) { wait_vm<6>(); rawbar(); }
      issue1(IC<t + 3>{}, Kc);
      wait_lgkm<5>();
      if constexpr (G + 2 < 36) ld1(G + 2); else ld2(G + 2 - 36);
      qa[0] = MFMA(f1[G % 3][1], f1[G % 3][0], qa[0]);
      qa[1] = MFMA(f1[G % 3][2], f1[G % 3][0], qa[1]);
      SCHED();
    });
  });
  bf16x8 qf[8];
  qf[0] = pack8(qa[0], 0); qf[1] = pack8(qa[0], 1); qf[2] = pack8(qa[1], 0); qf[3] = pack8(qa[1], 1);
  {
    const unsigned qw = MISC + wid * 4096 + lane * 16;
#pragma unroll
    for (int f = 0; f < 4; ++f) *(LDSP bf16x8*)(uintptr_t)(qw + f * 1024) = qf[f];
  }
  SCHED();
  CSTAMP(22);
  const float CS = 0.08838834764831845f * 1.4426950408889634f;
  const int wo4 = WO4;
  f32x16 s[8];
  unsigned pw[16][4];
  float m1 = 0.f, mx = 0.f, mb = 0.f, ls0 = 0.f, ls1 = 0.f, al = 0.f;
  float rm0 = -__builtin_inff(), rm1 = -__builtin_inff();

  const int kq = (bid & 7) >> 1;
  const int tile0 = (bid & 1) ? 32 + (bid >> 3) : (bid >> 3);
  const bool activeB = tile0 < 63;
  const int tile = activeB ? tile0 : 0;
  const int tm = tile / 9, tn = tile % 9;
  const char* wob = (const char*)wo + ((size_t)kq * 1024 * DM + tn * 64) * 2;
  unsigned vwo;
  {
    const int row = 8 * wid + (lane >> 3);
    const int c = (lane & 7) ^ (((row >> 1) & 1) << 2);
    vwo = (unsigned)(row * (DM * 2) + c * 16);
  }
  auto issueWr = [&](int t) { set_m0(dW, wo_slot(t)); gld<0, 0>(wob + (size_t)t * 64 * DM * 2, vwo); };

  f32x16 oa[4];
  float pe[2][2];
  auto vwork = [&](auto Mc) {
    constexpr int m = decltype(Mc)::value;
    if constexpr (SM.tm[m] >= 0) {
      constexpr int j = SM.tm[m];
#pragma unroll
      for (int r = 0; r < 16; r += 4) {
        rm0 = fmaxf(fmaxf(rm0, s[j][r]), s[j][r + 1]);
        rm1 = fmaxf(fmaxf(rm1, s[j][r + 2]), s[j][r + 3]);
      }
      if constexpr (j == NT1 - 1 || j == 7) {
        float m_ = fmaxf(rm0, rm1);
        auto rr = __builtin_amdgcn_permlane32_swap(__float_as_uint(m_), __float_as_uint(m_), false, false);
        m_ = fmaxf(__uint_as_float(rr[0]), __uint_as_float(rr[1]));
        if constexpr (j == 7) {
          mx = m_; mb = mx * CS;
          al = __builtin_amdgcn_exp2f((m1 - mx) * CS);
          ls0 *= al; ls1 *= al;
        } else { m1 = m_; mb = m1 * CS; }
      }
    }
    if constexpr (SM.rs[m] >= 0) {
      constexpr int d = SM.rs[m];
#pragma unroll
      for (int r = 0; r < 16; ++r) { float t_ = oa[d][r] * al; NOPK(t_); oa[d][r] = t_; }
    }
    if constexpr (m > 0) {
      sfor<SM.mu[m - 1], SM.mu[m]>([&](auto Kc) {
        constexpr int k = decltype(Kc)::value, f = k >> 2, q = k & 3;
        ls0 += pe[k & 1][0]; NOPK(ls0); ls1 += pe[k & 1][1];
        pw[f][q] = cvtpk(pe[k & 1][0], pe[k & 1][1]);
      });
    }
    sfor<SM.mu[m], SM.mu[m + 1]>([&](auto Kc) {
      constexpr int k = decltype(Kc)::value, f = k >> 2, q = k & 3;
      pe[k & 1][0] = __builtin_amdgcn_exp2f(fmaf(s[f >> 1][8 * (f & 1) + 2 * q], CS, -mb));
      pe[k & 1][1] = __builtin_amdgcn_exp2f(fmaf(s[f >> 1][8 * (f & 1) + 2 * q + 1], CS, -mb));
    });
  };
  sfor<0, 16>([&](auto Gc) {
    constexpr int G = decltype(Gc)::value, i = G >> 2, t = 9 + i;
    constexpr int kt = G < 4 ? (G & 1) : ((G >> 1) & 1), hf = G < 4 ? (G >> 1) : (G & 1);
    if constexpr ((G & 3) == 0) {
      if constexpr (i > 0) { wait_vm<4>(); rawbar(); }
      STAMP(1 + t);
    }
    if constexpr (G == 1) {
      bar();
      const unsigned qr = MISC + (wid ^ 4) * 4096 + lane * 16;
#pragma unroll
      for (int f = 0; f < 4; ++f) qf[4 + f] = lds_b128(qr + f * 1024);
      issue1(IC<12>{}, IC<0>{});
    }
    if constexpr (G == 2) { wait_vm<6>(); rawbar(); }
    if constexpr (G > 0) issue1(IC<t + 3>{}, IC<(G & 3)>{});
    if constexpr (G == 15) wait_lgkm<8>(); else if constexpr (G == 1) wait_lgkm<8>(); else wait_lgkm<4>();
    if constexpr (G + 2 < 16) ld2(G + 2); else ld3(G + 2 - 16);
    sfor<0, 4>([&](auto Jc) {
      constexpr int j = decltype(Jc)::value;
      if constexpr (hf == 0 && j == 0) s[2 * i + kt] = MFMA(f2[G % 3][0], qf[0], f32x16{});
      else s[2 * i + kt] = MFMA(f2[G % 3][j], qf[4 * hf + j], s[2 * i + kt]);
      vwork(IC<4 * G + j>{});
      SCHED();
    });
  });
  sfor<0, 16>([&](auto Gc) {
    constexpr int G = decltype(Gc)::value, i = G >> 2, t = 13 + i;
    if constexpr ((G & 3) == 0) {
      if constexpr (i < 3) { wait_vm<4>(); rawbar(); }
      STAMP(1 + t);
    }
    issue1(IC<t + 3>{}, IC<(G & 3)>{});
    if constexpr (i == 1 || i == 2) issueWr(4 * (i - 1) + (G & 3));
    if constexpr (G < 15) wait_lgkm<8>(); else wait_lgkm<0>();
    if constexpr (G + 2 < 16) ld3(G + 2);
    const u32x4 pwv = {pw[G][0], pw[G][1], pw[G][2], pw[G][3]};
    sfor<0, 4>([&](auto Jc) {
      constexpr int dt = decltype(Jc)::value;
      if constexpr (G == 0) oa[dt] = MFMA(f2[(G + 1) % 3][dt], __builtin_bit_cast(bf16x8, pwv), f32x16{});
      else oa[dt] = MFMA(f2[(G + 1) % 3][dt], __builtin_bit_cast(bf16x8, pwv), oa[dt]);
      vwork(IC<64 + 4 * G + dt>{});
      SCHED();
    });
  });
  float ls = ls0 + ls1;
  {
    auto rr = __builtin_amdgcn_permlane32_swap(__float_as_uint(ls), __float_as_uint(ls), false, false);
    ls = __uint_as_float(rr[0]) + __uint_as_float(rr[1]);
  }
  const unsigned long long tag = ((hw_dispatch_id() + 1ull) * 0x9E3779B97F4A7C15ull) ^ (unsigned long long)(uintptr_t)__builtin_amdgcn_dispatch_ptr();
  gu64* flagA = (gu64*)flags;
  gu64* flagB = (gu64*)flags + 512;
  bar();
  STAMP(18); CSTAMP(23);
  issueWr(8); issueWr(9); issueWr(10);
  {
    constexpr unsigned MLB = 72u * 1024u;
    const unsigned xw = (unsigned)(32768 + kh * RGB + rg * 8192) + lane * 16, xr = (unsigned)(32768 + (kh ^ 1) * RGB + rg * 8192) + lane * 16;
    auto put = [&](const f32x16& v, int dl) {
#pragma unroll
      for (int g = 0; g < 4; ++g) {
        f32x4 w = {v[4 * g], v[4 * g + 1], v[4 * g + 2], v[4 * g + 3]};
        *(LDSP f32x4*)(uintptr_t)(xw + (dl * 4 + g) * 1024) = w;
      }
    };
    put(oa[2], 0); put(oa[3], 1);
    f32x2 ml = {mx, ls};
    *(LDSP f32x2*)(uintptr_t)(MLB + wid * 512 + lane * 8) = ml;
    bar();
    STAMP2(1);
    const f32x2 pml = *(const LDSP f32x2*)(uintptr_t)(MLB + (wid ^ 4) * 512 + lane * 8);
    const float mt = fmaxf(mx, pml[0]);
    const float f0 = __builtin_amdgcn_exp2f((mx - mt) * CS), f1_ = __builtin_amdgcn_exp2f((pml[0] - mt) * CS);
    const float inv = 1.f / (f0 * ls + f1_ * pml[1]);
    const float g0 = f0 * inv, g1 = f1_ * inv;
    const int qrow = 32 * rg + l31, R = b * SQ + qrow;
    const __amdgpu_buffer_rsrc_t rs = __builtin_amdgcn_make_buffer_rsrc((char*)o_ws + (size_t)hq * MROWS * (DH * 2), 0, MROWS * DH * 2, 0x00020000);
    const int so = (R >> 5) * 8192 + (4 * kh + h) * 1024 + (R & 31) * 16;
    auto fin = [&](const f32x16& v, int dl) {
      float r[16];
#pragma unroll
      for (int g = 0; g < 4; ++g) {
        const f32x4 w = *(const LDSP f32x4*)(uintptr_t)(xr + (dl * 4 + g) * 1024);
#pragma unroll
        for (int e = 0; e < 4; ++e) r[4 * g + e] = v[4 * g + e] * g0 + w[e] * g1;
      }
      const u32x4 w0 = {cvtpk(r[0], r[1]), cvtpk(r[2], r[3]), cvtpk(r[4], r[5]), cvtpk(r[6], r[7])};
      const u32x4 w1 = {cvtpk(r[8], r[9]), cvtpk(r[10], r[11]), cvtpk(r[12], r[13]), cvtpk(r[14], r[15])};
      if (qrow < SQ) {
        __builtin_amdgcn_raw_buffer_store_b128(w0, rs, so + dl * 2048, 0, 16);
        __builtin_amdgcn_raw_buffer_store_b128(w1, rs, so + dl * 2048 + 512, 0, 16);
      }
    };
    fin(oa[0], 0); fin(oa[1], 1);
  }
  STAMP2(3);
  asm volatile("s_waitcnt vmcnt(0)" ::: "memory");
  STAMP2(4);
  __syncthreads();
  STAMP2(5);
  if (tid == 0) __hip_atomic_store(flagA + (b * 4 + (hq >> 3)) * 8 + (hq & 7), tag, __ATOMIC_RELAXED, __HIP_MEMORY_SCOPE_AGENT);
  __builtin_amdgcn_s_setprio(0);
  STAMP(19); CSTAMP(21);
  if (!activeB) { STAMP_FLUSH; return; }

  {
    const int mt = wid & 3, k2 = wid >> 2, nh = k2;
#pragma unroll
    for (int t = 11; t < 16; ++t) issueWr(t);
    const int q4 = (lane & 15) >> 2, p4 = lane & 3, g16 = (lane >> 4) & 1;
    unsigned aw[2][2];
#pragma unroll
    for (int n2 = 0; n2 < 2; ++n2)
#pragma unroll
      for (int t = 0; t < 2; ++t) {
        const int k = 8 * h + 4 * t + q4;
        const int c = (4 * n2 + 2 * (p4 & 1) + g16) ^ (((q4 >> 1) & 1) << 2);
        aw[n2][t] = (unsigned)(k2 * 8192 + k * 128 + c * 16 + (p4 >> 1) * 8);
      }
    const int T = 4 * tm + mt;
    const int b0 = (32 * T) / SQ, b1 = (32 * T + 31) / SQ;
    gu64* myf = flagA + (((lane & 8) ? b1 : b0) * 4 + kq) * 8 + (lane & 7);
    unsigned rdy = 0;
    auto need = [&](int sI) {
      if (!((rdy >> sI) & 1u)) {
        for (unsigned spins = 0; spins < (1u << 20); ++spins) {
          const unsigned long long v = lane < 16 ? __hip_atomic_load(myf, __ATOMIC_RELAXED, __HIP_MEMORY_SCOPE_AGENT) : tag;
          const unsigned m = (unsigned)__ballot(v == tag);
          rdy = m & (m >> 8) & 0xffu;
          if ((rdy >> sI) & 1u) break;
          __builtin_amdgcn_s_sleep(1);
        }
        asm volatile("" ::: "memory");
      }
    };
    const __amdgpu_buffer_rsrc_t ro = __builtin_amdgcn_make_buffer_rsrc((char*)o_ws + (size_t)kq * 8 * MROWS * (DH * 2), 0, 8 * MROWS * DH * 2, 0x00020000);
    u32x4 of[8][4];
    const int ovo = T * 8192 + k2 * 4096 + lane * 16;
    f32x16 acc[2] = {};
    bf16x8 fa[2][2][2];
    auto ldw = [&](int q) {
      const unsigned base = wo_slot(2 * (q >> 2)) + (q & 3) * 2048;
      fa[q & 1][0][0] = lds_tr2(base + aw[0][0], base + aw[0][1]);
      fa[q & 1][1][0] = lds_tr2(base + aw[1][0], base + aw[1][1]);
    };
    ldw(0);
#define OLAG 3
#pragma unroll
    for (int sI = 0; sI < 8 + OLAG; ++sI) {
      if (sI < 8) {
        need(sI);
        if (sI == 0) { STAMP(26); }
#pragma unroll
        for (int j = 0; j < 4; ++j) of[sI][j] = __builtin_amdgcn_raw_buffer_load_b128(ro, ovo + j * 1024, sI * MROWS * (DH * 2), 16);
        SCHED();
      }
      if (sI >= OLAG) {
#pragma unroll
        for (int j = 0; j < 4; ++j) {
          const int q = 4 * (sI - OLAG) + j;
          if (q == 19) rawbar();
          if (q == 0) { STAMP(27); }
          if (q + 1 < 32) ldw(q + 1);
          const bf16x8 ob = __builtin_bit_cast(bf16x8, of[q >> 2][q & 3]);
          acc[0] = MFMA(fa[q & 1][0][0], ob, acc[0]);
          acc[1] = MFMA(fa[q & 1][1][0], ob, acc[1]);
          SCHED();
        }
      }
    }
    STAMP(24);
    {
      const unsigned ew = 96u * 1024u + wid * 4096 + lane * 16, er = 96u * 1024u + (wid ^ 4) * 4096 + lane * 16;
      auto put = [&](const f32x16& v) {
#pragma unroll
        for (int g = 0; g < 4; ++g) {
          f32x4 w = {v[4 * g], v[4 * g + 1], v[4 * g + 2], v[4 * g + 3]};
          *(LDSP f32x4*)(uintptr_t)(ew + g * 1024) = w;
        }
      };
      if (k2 == 0) put(acc[1]); else put(acc[0]);
      bar();
      auto get = [&](const f32x16& own, bool first) {
        f32x16 o;
#pragma unroll
        for (int g = 0; g < 4; ++g) {
          const f32x4 w = *(const LDSP f32x4*)(uintptr_t)(er + g * 1024);
#pragma unroll
          for (int e = 0; e < 4; ++e) o[4 * g + e] = own[4 * g + e] + w[e];
        }
        return o;
      };
      if (k2 == 0) acc[0] = get(acc[0], true); else acc[0] = get(acc[1], false);
    }
    float* sl = slab + (size_t)tile * 4 * 4 * 2 * 1024;
    if (mt != kq) {
      const __amdgpu_buffer_rsrc_t rs = __builtin_amdgcn_make_buffer_rsrc((void*)(sl + ((mt * 4 + kq) * 2 + nh) * 1024), 0, 4096, 0x00020000);
#pragma unroll
      for (int g = 0; g < 4; ++g) {
        f32x4 w = {acc[0][4 * g], acc[0][4 * g + 1], acc[0][4 * g + 2], acc[0][4 * g + 3]};
        __builtin_amdgcn_raw_buffer_store_b128(__builtin_bit_cast(u32x4, w), rs, g * 1024 + lane * 16, 0, 16);
      }
    }
    asm volatile("s_waitcnt vmcnt(0)" ::: "memory");
    __syncthreads();
    if (wid == 0 && lane < 4 && lane != kq)
      __hip_atomic_store(flagB + (tile * 4 + lane) * 4 + kq, tag, __ATOMIC_RELAXED, __HIP_MEMORY_SCOPE_AGENT);
    if (mt == kq) {
      wait_flags(flagB + (tile * 4 + kq) * 4 + (lane & 3), lane < 4 && lane != kq, tag);
      u32x4 pv[3][4];
#pragma unroll
      for (int o = 0; o < 3; ++o) {
        const int src = o + (o >= kq);
        const __amdgpu_buffer_rsrc_t rs = __builtin_amdgcn_make_buffer_rsrc((void*)(sl + ((kq * 4 + src) * 2 + nh) * 1024), 0, 4096, 0x00020000);
#pragma unroll
        for (int g = 0; g < 4; ++g) pv[o][g] = __builtin_amdgcn_raw_buffer_load_b128(rs, g * 1024 + lane * 16, 0, 16);
      }
      float r[16];
#pragma unroll
      for (int g = 0; g < 4; ++g) {
        const f32x4 a = __builtin_bit_cast(f32x4, pv[0][g]), c = __builtin_bit_cast(f32x4, pv[1][g]), d = __builtin_bit_cast(f32x4, pv[2][g]);
#pragma unroll
        for (int e = 0; e < 4; ++e) {
          const float own = acc[0][4 * g + e];
          const float t0 = kq == 0 ? own : a[e];
          const float t1 = kq == 0 ? a[e] : (kq == 1 ? own : c[e]);
          const float t2 = kq <= 1 ? c[e] : (kq == 2 ? own : d[e]);
          const float t3 = kq == 3 ? own : d[e];
          r[4 * g + e] = ((t0 + t1) + t2) + t3;
        }
      }
      char* dst = (char*)out + ((size_t)(tm * 128 + 32 * mt + l31) * DM + tn * 64 + nh * 32 + 16 * h) * 2;
      u32x4 w0 = {cvtpk(r[0], r[1]), cvtpk(r[2], r[3]), cvtpk(r[4], r[5]), cvtpk(r[6], r[7])};
      u32x4 w1 = {cvtpk(r[8], r[9]), cvtpk(r[10], r[11]), cvtpk(r[12], r[13]), cvtpk(r[14], r[15])};
      *(u32x4*)dst = w0;
      *(u32x4*)(dst + 16) = w1;
    }
  }
  STAMP(25);
  STAMP_FLUSH;
}

extern "C" void kernel_launch(void* const* d_in, const int* in_sizes, int n_in, void* d_out, int out_size, void* d_ws,
                              size_t ws_size, hipStream_t stream) {
  const __hip_bfloat16* x = (const __hip_bfloat16*)d_in[0];
  const __hip_bfloat16* kc = (const __hip_bfloat16*)d_in[1];
  const __hip_bfloat16* vc = (const __hip_bfloat16*)d_in[2];
  const __hip_bfloat16* wq = (const __hip_bfloat16*)d_in[3];
  const __hip_bfloat16* wo = (const __hip_bfloat16*)d_in[4];
  __hip_bfloat16* o_ws = (__hip_bfloat16*)d_ws;
  float* slab = (float*)((char*)d_ws + (size_t)8 * 1024 * 1024);
  unsigned long long* flags = (unsigned long long*)((char*)d_ws + (size_t)24 * 1024 * 1024);
  hipFuncSetAttribute((const void*)k_fused, hipFuncAttributeMaxDynamicSharedMemorySize, K1_LDS);
  hipLaunchKernelGGL(k_fused, dim3(256), dim3(512), K1_LDS, stream, x, kc, vc, wq, wo, o_ws, slab, flags, (__hip_bfloat16*)d_out);
}
```

```cpp
#include <hip/hip_runtime.h>
#include <hip/hip_bf16.h>
#include <stdint.h>

typedef __attribute__((ext_vector_type(8))) __bf16 bf16x8;
typedef __attribute__((ext_vector_type(4))) __bf16 bf16x4;
typedef __attribute__((ext_vector_type(2))) __bf16 bf16x2;
typedef __attribute__((ext_vector_type(16))) float f32x16;
typedef __attribute__((ext_vector_type(4))) float f32x4;
typedef __attribute__((ext_vector_type(2))) float f32x2;
typedef __attribute__((ext_vector_type(4))) unsigned u32x4;
#define LDSP __attribute__((address_space(3)))
#define DI __device__ __forceinline__

constexpr int NB = 8, SQ = 112, DM = 576, HQ = 32, HKV = 16, SK = 512, DH = 128;
constexpr int MROWS = NB * SQ;
constexpr int KO = HQ * DH;

DI unsigned cvtpk(float lo, float hi) {
  f32x2 v = {lo, hi};
  bf16x2 b = __builtin_convertvector(v, bf16x2);
  return __builtin_bit_cast(unsigned, b);
}
DI void glds16(const void* sbase, unsigned voff, unsigned lds_dst) {
  unsigned keep;
  asm volatile("s_mov_b32 %0, m0\n\ts_mov_b32 m0, %3\n\ts_nop 0\n\tglobal_load_lds_dwordx4 %1, %2\n\ts_mov_b32 m0, %0"
               : "=&s"(keep) : "v"(voff), "s"(sbase), "s"(lds_dst) : "memory");
}
DI void glds16nt(const void* sbase, unsigned voff, unsigned lds_dst) {
  unsigned keep;
  asm volatile("s_mov_b32 %0, m0\n\ts_mov_b32 m0, %3\n\ts_nop 0\n\tglobal_load_lds_dwordx4 %1, %2 nt\n\ts_mov_b32 m0, %0"
               : "=&s"(keep) : "v"(voff), "s"(sbase), "s"(lds_dst) : "memory");
}
DI void glds16sc1(const void* sbase, unsigned voff, unsigned lds_dst) {
  unsigned keep;
  asm volatile("s_mov_b32 %0, m0\n\ts_mov_b32 m0, %3\n\ts_nop 0\n\tglobal_load_lds_dwordx4 %1, %2 sc1\n\ts_mov_b32 m0, %0"
               : "=&s"(keep) : "v"(voff), "s"(sbase), "s"(lds_dst) : "memory");
}
template <int J> DI void glds16_if(const void* sbase, unsigned voff, unsigned lds_dst, int sel) {
  unsigned keep;
  asm volatile("s_cmp_lg_u32 %4, %5\n\ts_cbranch_scc1 .Lgl%=\n\ts_mov_b32 %0, m0\n\ts_mov_b32 m0, %3\n\ts_nop 0\n\tglobal_load_lds_dwordx4 %1, %2\n\ts_mov_b32 m0, %0\n.Lgl%=:"
               : "=&s"(keep) : "v"(voff), "s"(sbase), "s"(lds_dst), "s"(sel), "i"(J) : "memory", "scc");
}
template <int J> DI void glds16nt_if(const void* sbase, unsigned voff, unsigned lds_dst, int sel) {
  unsigned keep;
  asm volatile("s_cmp_lg_u32 %4, %5\n\ts_cbranch_scc1 .Lgl%=\n\ts_mov_b32 %0, m0\n\ts_mov_b32 m0, %3\n\ts_nop 0\n\tglobal_load_lds_dwordx4 %1, %2 nt\n\ts_mov_b32 m0, %0\n.Lgl%=:"
               : "=&s"(keep) : "v"(voff), "s"(sbase), "s"(lds_dst), "s"(sel), "i"(J) : "memory", "scc");
}
template <bool NT, int J> DI void gl_t(const void* sbase, unsigned voff, unsigned lds_dst, int sel) {
  if constexpr (J < 0) { if constexpr (NT) glds16nt(sbase, voff, lds_dst); else glds16(sbase, voff, lds_dst); }
  else { if constexpr (NT) glds16nt_if<J>(sbase, voff, lds_dst, sel); else glds16_if<J>(sbase, voff, lds_dst, sel); }
}
template <int V> struct IC { static constexpr int value = V; };
template <int I, int N, class F> DI void sfor(F&& f) { if constexpr (I < N) { f(IC<I>{}); sfor<I + 1, N>(f); } }
DI void gl_sel(bool nt, int j, const void* sbase, unsigned voff, unsigned lds_dst, int sel) {
  if (nt) {
    if (j < 0) glds16nt(sbase, voff, lds_dst);
    else if (j == 0) glds16nt_if<0>(sbase, voff, lds_dst, sel); else if (j == 1) glds16nt_if<1>(sbase, voff, lds_dst, sel);
    else if (j == 2) glds16nt_if<2>(sbase, voff, lds_dst, sel); else glds16nt_if<3>(sbase, voff, lds_dst, sel);
  } else {
    if (j < 0) glds16(sbase, voff, lds_dst);
    else if (j == 0) glds16_if<0>(sbase, voff, lds_dst, sel); else if (j == 1) glds16_if<1>(sbase, voff, lds_dst, sel);
    else if (j == 2) glds16_if<2>(sbase, voff, lds_dst, sel); else glds16_if<3>(sbase, voff, lds_dst, sel);
  }
}
DI void set_m0(unsigned base, unsigned imm) { asm volatile("s_add_u32 m0, %0, %1\n\ts_nop 0" ::"s"(base), "i"(imm) : "scc"); }
template <int OFF, int POL> DI void gld(const void* sbase, unsigned voff) {
  if constexpr (POL == 0) asm volatile("global_load_lds_dwordx4 %0, %1 offset:%2" ::"v"(voff), "s"(sbase), "i"(OFF) : "memory");
  else if constexpr (POL == 1) asm volatile("global_load_lds_dwordx4 %0, %1 offset:%2 nt" ::"v"(voff), "s"(sbase), "i"(OFF) : "memory");
  else asm volatile("global_load_lds_dwordx4 %0, %1 offset:%2 sc1" ::"v"(voff), "s"(sbase), "i"(OFF) : "memory");
}
template <int N> DI void wait_lgkm() { __builtin_amdgcn_s_waitcnt(0xC07F | (N << 8)); }
#define GL_KV glds16nt
#define GL_XW glds16
#define GL_WO glds16
#define GL_O glds16
template <int N> DI void wait_vm() { asm volatile("s_waitcnt vmcnt(%0)" ::"i"(N) : "memory"); }
DI void bar() { asm volatile("s_waitcnt lgkmcnt(0)\n\ts_barrier" ::: "memory"); }
DI bf16x8 lds_b128(unsigned a) { return *(const LDSP bf16x8*)(uintptr_t)a; }
DI bf16x8 lds_tr2(unsigned a0, unsigned a1) {
  bf16x4 lo = __builtin_amdgcn_ds_read_tr16_b64_v4bf16((LDSP bf16x4*)(uintptr_t)a0);
  bf16x4 hi = __builtin_amdgcn_ds_read_tr16_b64_v4bf16((LDSP bf16x4*)(uintptr_t)a1);
  return __builtin_shufflevector(lo, hi, 0, 1, 2, 3, 4, 5, 6, 7);
}
DI bf16x8 pack8(const f32x16& x, int s) {
  u32x4 w = {cvtpk(x[8 * s], x[8 * s + 1]), cvtpk(x[8 * s + 2], x[8 * s + 3]), cvtpk(x[8 * s + 4], x[8 * s + 5]),
             cvtpk(x[8 * s + 6], x[8 * s + 7])};
  return __builtin_bit_cast(bf16x8, w);
}
#define STAMP(i)
#define CSTAMP(i)
#define STAMP_DECL
#define STAMP_FLUSH
#define STAMP2(i)
#define CSTAMP2(i)
#define STAMP_DECL2
#define STAMP_FLUSH2
#define MFMA(a, b, c) __builtin_amdgcn_mfma_f32_32x32x16_bf16((a), (b), (c), 0, 0, 0)

constexpr unsigned SUB = 16384u;
constexpr unsigned RGB = 65536u;
constexpr unsigned MISC = 131072u;
#define STAG1 0
#define STAG2 0
#define WO4 ((wid & 1) + 2 * (wid >> 2))
#define PIPE_MU 1
#define PRIO 3
#define HEADSKIP 0
#define SUMDT 3
#define NT1 4
#define E1 67
#define E2 93
struct SmSched {
  int mu[129];
  int tm[128];
  int rs[128];
  constexpr SmSched() : mu{}, tm{}, rs{} {
    int n[128] = {};
    for (int m = 0; m < 128; ++m) { tm[m] = -1; rs[m] = -1; }
    for (int j = 0; j < 8; ++j) tm[8 * j + (j == 0 ? 14 : (j == NT1 - 1 || j == 7) ? 9 : 10)] = j;
    const int sw = 64 + 4 * NT1;
    for (int d = 0; d < 4; ++d) rs[sw - 1 + (d & 1) + 32 * (d >> 1)] = d;
    {
      const int a = 8 * NT1 + 3, e = E1; int av = 0, acc = 0;
      for (int m = a; m < e; ++m) if (tm[m] < 0) ++av;
      for (int m = a; m < e; ++m) if (tm[m] < 0) { acc += 8 * NT1; while (acc >= av) { acc -= av; ++n[m]; } }
    }
    {
      const int a = 67, e = E2; int av = 0, acc = 0;
      for (int m = a; m < e; ++m) if (rs[m] < 0) ++av;
      for (int m = a; m < e; ++m) if (rs[m] < 0) { acc += 64 - 8 * NT1; while (acc >= av) { acc -= av; ++n[m]; } }
    }
    int k = 0;
    for (int m = 0; m < 128; ++m) { mu[m] = k; k += n[m]; }
    mu[128] = k;
  }
  constexpr bool ok() const {
    if (mu[128] != 64) return false;
    for (int m = 0; m < 128; ++m) if (mu[m + 1] - mu[m] > 2) return false;
    for (int m = 0; m < 128; ++m)
      for (int k = mu[m]; k < mu[m + 1]; ++k) {
        if (m + 1 >= 64 + 2 * (k >> 2)) return false;
        if (k >= 8 * NT1 && m < 67) return false;
      }
    return true;
  }
};
constexpr SmSched SM{};
static_assert(SM.ok(), "softmax schedule misses a deadline");
constexpr int K1_LDS = 160 * 1024;
#define SCHED() __builtin_amdgcn_sched_barrier(0)
#define NOPK(x) asm("" : "+v"(x))
DI void rawbar() { asm volatile("s_barrier" ::: "memory"); }

typedef __attribute__((address_space(1))) unsigned long long gu64;
extern "C" __device__ unsigned long long hw_dispatch_id() __asm("llvm.amdgcn.dispatch.id");
DI constexpr unsigned wo_slot(int t) {
  constexpr unsigned pb[8] = {128u, 144u, 16u, 80u, 0u, 64u, 32u, 48u};
  return pb[t >> 1] * 1024u + (unsigned)(t & 1) * 8192u;
}
DI void wait_flags(gu64* f, bool on, unsigned long long tag) {
  for (unsigned spins = 0; spins < (1u << 20); ++spins) {
    const unsigned long long v = on ? __hip_atomic_load(f, __ATOMIC_RELAXED, __HIP_MEMORY_SCOPE_AGENT) : tag;
    if (__all(v == tag)) break;
    __builtin_amdgcn_s_sleep(1);
  }
  asm volatile("" ::: "memory");
}

__global__ __launch_bounds__(512, 2) void k_fused(const __hip_bfloat16* __restrict__ x, const __hip_bfloat16* __restrict__ kc,
                                                   const __hip_bfloat16* __restrict__ vc, const __hip_bfloat16* __restrict__ wq,
                                                   const __hip_bfloat16* __restrict__ wo, __hip_bfloat16* o_ws,
                                                   float* slab, unsigned long long* flags,
                                                   __hip_bfloat16* __restrict__ out) {
  extern __shared__ __attribute__((aligned(16))) char lds[];
  const int tid = threadIdx.x, lane = tid & 63;
  const int wid = __builtin_amdgcn_readfirstlane(tid >> 6);
  const int rg = wid & 3, kh = wid >> 2;
  const int l31 = lane & 31, h = lane >> 5;
  const int bid = blockIdx.x;
  const int hq = 4 * (bid & 7) + ((bid >> 3) & 3), b = bid >> 5;
  const int hkv = hq >> 1;
  STAMP_DECL;

  unsigned vx[2], vw[2], vkv[4];
#pragma unroll
  for (int e = 0; e < 2; ++e) {
    const int i = 2 * wid + e;
    int row = 8 * i + (lane >> 3);
    const int c = (lane & 7) ^ ((row >> 1) & 7);
    row = row < SQ ? row : SQ - 1;
    vx[e] = (unsigned)(row * (DM * 2) + c * 16 - e * 1024 + 1024);
    const int rw = 4 * i + (lane >> 4);
    const int cw = (lane & 15) ^ (((rw & 3) << 2) | ((rw >> 2) & 3));
    vw[e] = (unsigned)(rw * (KO * 2) + cw * 16 - e * 1024 + 1024);
  }
#pragma unroll
  for (int e = 0; e < 4; ++e) {
    const int r = 4 * (4 * (wid & 3) + e) + (lane >> 4);
    const int c = (lane & 15) ^ (((r & 3) << 2) | ((r >> 2) & 3));
    vkv[e] = (unsigned)((r - 4 * e) * 256 + c * 16);
  }
  const unsigned vvo = (unsigned)((32 * rg + (lane >> 3)) * 256 + (((lane & 7) ^ (((lane >> 4) & 1) << 2)) * 16));
  const char* xb = (const char*)x + (size_t)b * SQ * DM * 2 - 1024;
  const char* wb = (const char*)wq + (size_t)hq * DH * 2 - 1024;
  const unsigned dX = (unsigned)wid * 2048u, dK = (unsigned)(kh * RGB + rg * 4096), dW = (unsigned)wid * 1024u;
  const size_t kvo = ((size_t)(b * HKV + hkv) * SK + 256 * kh) * DH * 2;
  const char* kb = (const char*)kc + kvo;
  const char* vb = (const char*)vc + kvo;

  auto issue1 = [&](auto Tc, auto Ec) {
    constexpr int t = decltype(Tc)::value, e = decltype(Ec)::value;
    constexpr unsigned sb = (unsigned)(t & 3) * SUB;
    if constexpr (t < 9) {
      if constexpr (e == 0) set_m0(dX, sb);
      if constexpr (e == 2) set_m0(dX, RGB + sb);
      if constexpr (e < 2) gld<e * 1024, 0>(xb + t * 128, vx[e]);
      else                 gld<(e - 2) * 1024, 0>(wb + (size_t)t * 64 * KO * 2, vw[e - 2]);
    } else if constexpr (t < 17) {
      if constexpr (e == 0) set_m0(dK, sb);
      if constexpr (t < 13) gld<e * 1024, 1>(kb + (t - 9) * 16384, vkv[e]);
      else                  gld<e * 1024, 1>(vb + ((t - 13) & 1) * 32768 + ((t - 13) >> 1) * 128 + e * 1024, vvo);
    }
  };
  if (kh) __builtin_amdgcn_s_setprio(1);
  STAMP(0); CSTAMP(20);
  sfor<0, 3>([&](auto Tc) { sfor<0, 4>([&](auto Ec) { issue1(Tc, Ec); }); });

  const int q4 = (lane & 15) >> 2, p4 = lane & 3, g16 = (lane >> 4) & 1;
  unsigned ax[4];
#pragma unroll
  for (int ks = 0; ks < 4; ++ks) ax[ks] = (unsigned)((32 * rg + l31) * 128 + (((2 * ks + h) ^ ((l31 >> 1) & 7)) * 16));
  unsigned aw[2][2];
#pragma unroll
  for (int dl = 0; dl < 2; ++dl)
#pragma unroll
    for (int t = 0; t < 2; ++t) {
      const int dt = 2 * kh + dl;
      const int k = 8 * h + 4 * t + q4;
      const int c = (4 * dt + 2 * g16 + (p4 & 1)) ^ ((q4 << 2) | ((2 * h + t) & 3));
      aw[dl][t] = (unsigned)(RGB + k * 256 + c * 16 + (p4 >> 1) * 8);
    }
  unsigned ak[8];
#pragma unroll
  for (int ks = 0; ks < 8; ++ks)
    ak[ks] = (unsigned)(kh * RGB + l31 * 256 + (((2 * ks + h) ^ (8 * kh) ^ (((l31 & 3) << 2) | ((l31 >> 2) & 3))) * 16));
  unsigned av[2][2];
#pragma unroll
  for (int dl = 0; dl < 2; ++dl)
#pragma unroll
    for (int t = 0; t < 2; ++t) {
      const int key = 8 * t + 4 * h + q4;
      const int c = (4 * (dl ^ kh) + 2 * (p4 & 1) + g16) ^ (((q4 >> 1) & 1) << 2);
      av[dl][t] = (unsigned)(kh * RGB + key * 128 + c * 16 + (p4 >> 1) * 8);
    }

  bf16x8 f1[3][3];
  auto ld1 = [&](int G) {
    const unsigned sb = (unsigned)((G >> 2) & 3) * SUB; const int ks = G & 3, r = G % 3;
    f1[r][0] = lds_b128(sb + ax[ks]);
    f1[r][1] = lds_tr2(sb + aw[0][0] + ks * 4096, sb + aw[0][1] + ks * 4096);
    f1[r][2] = lds_tr2(sb + aw[1][0] + ks * 4096, sb + aw[1][1] + ks * 4096);
  };
  bf16x8 f2[3][4];
  auto ld2 = [&](int G) {
    const int kt = G < 4 ? (G & 1) : ((G >> 1) & 1), hf = G < 4 ? (G >> 1) : (G & 1);
    const unsigned sb = (unsigned)((9 + (G >> 2)) & 3) * SUB + kt * 8192; const int r = G % 3;
#pragma unroll
    for (int j = 0; j < 4; ++j) f2[r][j] = lds_b128(sb + ak[4 * hf + j]);
  };
  auto ld3 = [&](int G) {
    const unsigned sb = (unsigned)((13 + (G >> 2)) & 3) * SUB + (G & 3) * 4096; const int r = (G + 1) % 3;
#pragma unroll
    for (int j = 0; j < 4; ++j) f2[r][j] = lds_tr2(sb + (j >> 1) * 2048 + av[j & 1][0], sb + (j >> 1) * 2048 + av[j & 1][1]);
  };

  wait_vm<8>(); rawbar();
  ld1(0); ld1(1);
  SCHED();

  f32x16 qa[2] = {};
  sfor<0, 9>([&](auto Tc) {
    constexpr int t = decltype(Tc)::value;
    if constexpr (t < 8) wait_vm<4>();
    rawbar();
    STAMP(1 + t);
    sfor<0, 4>([&](auto Kc) {
      constexpr int ks = decltype(Kc)::value;
      constexpr int G = 4 * t + ks;
      if constexpr (G == 34) { wait_vm<6>(); rawbar(); }
      issue1(IC<t + 3>{}, Kc);
      wait_lgkm<5>();
      if constexpr (G + 2 < 36) ld1(G + 2); else ld2(G + 2 - 36);
      qa[0] = MFMA(f1[G % 3][1], f1[G % 3][0], qa[0]);
      qa[1] = MFMA(f1[G % 3][2], f1[G % 3][0], qa[1]);
      SCHED();
    });
  });
  bf16x8 qf[8];
  qf[0] = pack8(qa[0], 0); qf[1] = pack8(qa[0], 1); qf[2] = pack8(qa[1], 0); qf[3] = pack8(qa[1], 1);
  {
    const unsigned qw = MISC + wid * 4096 + lane * 16;
#pragma unroll
    for (int f = 0; f < 4; ++f) *(LDSP bf16x8*)(uintptr_t)(qw + f * 1024) = qf[f];
  }
  SCHED();
  CSTAMP(22);
  const float CS = 0.08838834764831845f * 1.4426950408889634f;
  const int wo4 = WO4;
  f32x16 s[8];
  unsigned pw[16][4];
  float m1 = 0.f, mx = 0.f, mb1 = 0.f, mb2 = 0.f, al = 0.f;
  const unsigned selw = ((lane & 15) == ((lane >> 4) & 1)) ? 0x3F803F80u : 0u;
  const u32x4 sel4 = {selw, selw, selw, selw};
  f32x4 lsum[2];
  float rm0 = -__builtin_inff(), rm1 = -__builtin_inff();

  const int kq = (bid & 7) >> 1;
  const int tile0 = (bid & 1) ? 32 + (bid >> 3) : (bid >> 3);
  const bool activeB = tile0 < 63;
  const int tile = activeB ? tile0 : 0;
  const int tm = tile / 9, tn = tile % 9;
  const char* wob = (const char*)wo + ((size_t)kq * 1024 * DM + tn * 64) * 2;
  unsigned vwo;
  {
    const int row = 8 * wid + (lane >> 3);
    const int c = (lane & 7) ^ (((row >> 1) & 1) << 2);
    vwo = (unsigned)(row * (DM * 2) + c * 16);
  }
  auto issueWr = [&](int u) { set_m0(dW, wo_slot(u)); gld<0, 0>(wob + (size_t)(2 * (u & 7) + (u >> 3)) * 64 * DM * 2, vwo); };

  f32x16 oa[4];
  float pe[2][2];
  auto vwork = [&](auto Mc) {
    constexpr int m = decltype(Mc)::value;
    if constexpr (SM.tm[m] >= 0) {
      constexpr int j = SM.tm[m];
#pragma unroll
      for (int r = 0; r < 16; r += 4) {
        rm0 = fmaxf(fmaxf(rm0, s[j][r]), s[j][r + 1]);
        rm1 = fmaxf(fmaxf(rm1, s[j][r + 2]), s[j][r + 3]);
      }
      if constexpr (j == NT1 - 1 || j == 7) {
        float m_ = fmaxf(rm0, rm1);
        auto rr = __builtin_amdgcn_permlane32_swap(__float_as_uint(m_), __float_as_uint(m_), false, false);
        m_ = fmaxf(__uint_as_float(rr[0]), __uint_as_float(rr[1]));
        if constexpr (j == 7) {
          mx = m_; mb2 = mx * CS;
          al = __builtin_amdgcn_exp2f((m1 - mx) * CS);
        } else { m1 = m_; mb1 = m1 * CS; }
      }
    }
    if constexpr (SM.rs[m] >= 0) {
      constexpr int d = SM.rs[m];
#pragma unroll
      for (int r = 0; r < 16; ++r) { float t_ = oa[d][r] * al; NOPK(t_); oa[d][r] = t_; }
    }
    if constexpr (m > 0) {
      sfor<SM.mu[m - 1], SM.mu[m]>([&](auto Kc) {
        constexpr int k = decltype(Kc)::value, f = k >> 2, q = k & 3;
        pw[f][q] = cvtpk(pe[k & 1][0], pe[k & 1][1]);
      });
    }
    sfor<SM.mu[m], SM.mu[m + 1]>([&](auto Kc) {
      constexpr int k = decltype(Kc)::value, f = k >> 2, q = k & 3;
      const float mb = k < 8 * NT1 ? mb1 : mb2;
      pe[k & 1][0] = __builtin_amdgcn_exp2f(fmaf(s[f >> 1][8 * (f & 1) + 2 * q], CS, -mb));
      pe[k & 1][1] = __builtin_amdgcn_exp2f(fmaf(s[f >> 1][8 * (f & 1) + 2 * q + 1], CS, -mb));
    });
  };
  sfor<0, 16>([&](auto Gc) {
    constexpr int G = decltype(Gc)::value, i = G >> 2, t = 9 + i;
    constexpr int kt = G < 4 ? (G & 1) : ((G >> 1) & 1), hf = G < 4 ? (G >> 1) : (G & 1);
    if constexpr ((G & 3) == 0) {
      if constexpr (i > 0) { wait_vm<4>(); rawbar(); }
      STAMP(1 + t);
    }
    if constexpr (G == 1) {
      bar();
      const unsigned qr = MISC + (wid ^ 4) * 4096 + lane * 16;
#pragma unroll
      for (int f = 0; f < 4; ++f) qf[4 + f] = lds_b128(qr + f * 1024);
      issue1(IC<12>{}, IC<0>{});
    }
    if constexpr (G == 2) { wait_vm<6>(); rawbar(); }
    if constexpr (G > 0) issue1(IC<t + 3>{}, IC<(G & 3)>{});
    if constexpr (G == 15) wait_lgkm<8>(); else if constexpr (G == 1) wait_lgkm<8>(); else wait_lgkm<4>();
    if constexpr (G + 2 < 16) ld2(G + 2); else ld3(G + 2 - 16);
    sfor<0, 4>([&](auto Jc) {
      constexpr int j = decltype(Jc)::value;
      if constexpr (hf == 0 && j == 0) s[2 * i + kt] = MFMA(f2[G % 3][0], qf[0], f32x16{});
      else s[2 * i + kt] = MFMA(f2[G % 3][j], qf[4 * hf + j], s[2 * i + kt]);
      vwork(IC<4 * G + j>{});
      SCHED();
    });
  });
  const unsigned long long tag = ((hw_dispatch_id() + 1ull) * 0x9E3779B97F4A7C15ull) ^ (unsigned long long)(uintptr_t)__builtin_amdgcn_dispatch_ptr();
  gu64* flagA = (gu64*)flags;
  gu64* flagB = (gu64*)flags + 512;
  const int qrow = 32 * rg + l31, R = b * SQ + qrow;
  const __amdgpu_buffer_rsrc_t rso = __builtin_amdgcn_make_buffer_rsrc((char*)o_ws + (size_t)hq * MROWS * (DH * 2), 0, MROWS * DH * 2, 0x00020000);
  const int so = (R >> 5) * 8192 + (2 * kh + h) * 1024 + (R & 31) * 16;
  constexpr unsigned MLB = 88u * 1024u;
  const unsigned xw = (unsigned)(32768 + kh * RGB + rg * 4096) + lane * 16, xr = (unsigned)(32768 + (kh ^ 1) * RGB + rg * 4096) + lane * 16;
  float g0 = 0.f, g1 = 0.f;
  f32x4 xv[4];
  auto put = [&](const f32x16& v, int p, int g) {
    f32x4 w = {v[4 * g], v[4 * g + 1], v[4 * g + 2], v[4 * g + 3]};
    *(LDSP f32x4*)(uintptr_t)(xw + p * SUB + g * 1024) = w;
  };
  auto get = [&](int p) {
#pragma unroll
    for (int g = 0; g < 4; ++g) xv[g] = *(const LDSP f32x4*)(uintptr_t)(xr + p * SUB + g * 1024);
  };
  u32x4 ow[2];
  auto comb = [&](const f32x16& v, int w) {
    float r[8];
#pragma unroll
    for (int e = 0; e < 8; ++e) { float t_ = v[8 * w + e] * g0; NOPK(t_); r[e] = fmaf(xv[2 * w + (e >> 2)][e & 3], g1, t_); NOPK(r[e]); }
    ow[w] = u32x4{cvtpk(r[0], r[1]), cvtpk(r[2], r[3]), cvtpk(r[4], r[5]), cvtpk(r[6], r[7])};
  };
  auto ostore = [&](int p) {
    if (qrow < SQ) {
      __builtin_amdgcn_raw_buffer_store_b128(ow[0], rso, so + p * 4096, 0, 16);
      __builtin_amdgcn_raw_buffer_store_b128(ow[1], rso, so + p * 4096 + 512, 0, 16);
    }
  };
  float lq[4];
  f32x2 pml;
#define XB 10
  auto hook = [&](auto Mc) {
    constexpr int m = decltype(Mc)::value;
    if constexpr (m == 96) {
      const int src = (lane & 15) * 4;
      lq[0] = __int_as_float(__builtin_amdgcn_ds_bpermute(src, __float_as_int(lsum[0][0])));
      lq[1] = __int_as_float(__builtin_amdgcn_ds_bpermute(src, __float_as_int(lsum[0][1])));
      lq[2] = __int_as_float(__builtin_amdgcn_ds_bpermute(src, __float_as_int(lsum[1][0])));
      lq[3] = __int_as_float(__builtin_amdgcn_ds_bpermute(src, __float_as_int(lsum[1][1])));
    }
    if constexpr (m == 97) { put(oa[1], 0, 0); put(oa[1], 0, 1); }
    if constexpr (m == 98) { put(oa[1], 0, 2); put(oa[1], 0, 3); }
    if constexpr (m == 99) {
      const float ls = (lane & 16) ? lq[1] * al + lq[3] : lq[0] * al + lq[2];
      lq[0] = ls;
      f32x2 ml = {mx, ls};
      *(LDSP f32x2*)(uintptr_t)(MLB + wid * 512 + lane * 8) = ml;
    }
    constexpr int x0 = 64 + 4 * XB;
    if constexpr (m == x0) {
      pml = *(const LDSP f32x2*)(uintptr_t)(MLB + (wid ^ 4) * 512 + lane * 8);
      get(0);
    }
    if constexpr (m == x0 + 2) {
      const float mt = fmaxf(mx, pml[0]);
      const float f0 = __builtin_amdgcn_exp2f((mx - mt) * CS), f1_ = __builtin_amdgcn_exp2f((pml[0] - mt) * CS);
      const float inv = 1.f / (f0 * lq[0] + f1_ * pml[1]);
      g0 = f0 * inv; g1 = f1_ * inv;
    }
    if constexpr (m == x0 + 3) comb(oa[0], 0);
    if constexpr (m == x0 + 4) comb(oa[0], 1);
    if constexpr (m == x0 + 5) ostore(0);
  };
  sfor<0, 16>([&](auto Gc) {
    constexpr int G = decltype(Gc)::value, i = G >> 2, t = 13 + i;
    if constexpr ((G & 3) == 0) {
      if constexpr (i < 3) { wait_vm<4>(); rawbar(); }
      STAMP(1 + t);
    }
    if constexpr (G == XB) bar();
    issue1(IC<t + 3>{}, IC<(G & 3)>{});
    if constexpr (i == 1 || (i == 2 && (G & 3) < 3)) issueWr(4 * (i - 1) + (G & 3));
    if constexpr (G < 15) wait_lgkm<8>(); else wait_lgkm<0>();
    if constexpr (G + 2 < 16) ld3(G + 2);
    sfor<0, 4>([&](auto Jc) {
      constexpr int j = decltype(Jc)::value, f = 2 * (G & 7) + (j >> 1), dt = 2 * (G >> 3) + (j & 1);
      const u32x4 pwv = {pw[f][0], pw[f][1], pw[f][2], pw[f][3]};
      if constexpr (f == 0) oa[dt] = MFMA(f2[(G + 1) % 3][j], __builtin_bit_cast(bf16x8, pwv), f32x16{});
      else oa[dt] = MFMA(f2[(G + 1) % 3][j], __builtin_bit_cast(bf16x8, pwv), oa[dt]);
      if constexpr (dt == 1) {
        constexpr int gp = f < 2 * NT1 ? 0 : 1;
        if constexpr (f == 0 || f == 2 * NT1)
          lsum[gp] = __builtin_amdgcn_mfma_f32_16x16x32_bf16(__builtin_bit_cast(bf16x8, sel4), __builtin_bit_cast(bf16x8, pwv), f32x4{}, 0, 0, 0);
        else
          lsum[gp] = __builtin_amdgcn_mfma_f32_16x16x32_bf16(__builtin_bit_cast(bf16x8, sel4), __builtin_bit_cast(bf16x8, pwv), lsum[gp], 0, 0, 0);
      }
      vwork(IC<64 + 4 * G + j>{});
      hook(IC<64 + 4 * G + j>{});
      SCHED();
    });
  });
  bar();
  STAMP(18); CSTAMP(23);
  issueWr(7); issueWr(8); issueWr(9); issueWr(10);
#pragma unroll
  for (int g = 0; g < 4; ++g) put(oa[3], 1, g);
  bar();
  STAMP2(1);
  get(1);
  comb(oa[2], 0); comb(oa[2], 1);
  ostore(1);
  STAMP2(3);
  wait_vm<2>();
  rawbar();
  if (wid == 0) {
    if (lane == 0) __hip_atomic_store(flagA + (b * 4 + (hq >> 3)) * 16 + (hq & 7), tag, __ATOMIC_RELAXED, __HIP_MEMORY_SCOPE_AGENT);
    wait_vm<1>();
  } else wait_vm<0>();
  STAMP2(4);
  __syncthreads();
  STAMP2(5);
  if (tid == 0) __hip_atomic_store(flagA + (b * 4 + (hq >> 3)) * 16 + 8 + (hq & 7), tag, __ATOMIC_RELAXED, __HIP_MEMORY_SCOPE_AGENT);
  __builtin_amdgcn_s_setprio(0);
  STAMP(19); CSTAMP(21);
  if (!activeB) { STAMP_FLUSH; return; }

  {
    const int mt = wid & 3, k2 = wid >> 2, nh = k2;
#pragma unroll
    for (int u = 11; u < 16; ++u) issueWr(u);
    const int q4 = (lane & 15) >> 2, p4 = lane & 3, g16 = (lane >> 4) & 1;
    unsigned aw[2][2];
#pragma unroll
    for (int n2 = 0; n2 < 2; ++n2)
#pragma unroll
      for (int t = 0; t < 2; ++t) {
        const int k = 8 * h + 4 * t + q4;
        const int c = (4 * n2 + 2 * (p4 & 1) + g16) ^ (((q4 >> 1) & 1) << 2);
        aw[n2][t] = (unsigned)(k2 * 4096 + k * 128 + c * 16 + (p4 >> 1) * 8);
      }
    const int T = 4 * tm + mt;
    const int b0 = (32 * T) / SQ, b1 = (32 * T + 31) / SQ;
    gu64* myf = flagA + (((lane & 16) ? b1 : b0) * 4 + kq) * 16 + (lane & 15);
    unsigned rdy = 0;
    auto need = [&](int sI) {
      if (!((rdy >> sI) & 1u)) {
        for (unsigned spins = 0; spins < (1u << 20); ++spins) {
          const unsigned long long v = lane < 32 ? __hip_atomic_load(myf, __ATOMIC_RELAXED, __HIP_MEMORY_SCOPE_AGENT) : tag;
          const unsigned m = (unsigned)__ballot(v == tag);
          rdy = m & (m >> 16) & 0xffffu;
          if ((rdy >> sI) & 1u) break;
          __builtin_amdgcn_s_sleep(1);
        }
        asm volatile("" ::: "memory");
      }
    };
    const __amdgpu_buffer_rsrc_t ro = __builtin_amdgcn_make_buffer_rsrc((char*)o_ws + (size_t)kq * 8 * MROWS * (DH * 2), 0, 8 * MROWS * DH * 2, 0x00020000);
    u32x4 of[16][2];
    const int ovo = T * 8192 + k2 * 2048 + lane * 16;
    f32x16 acc[2] = {};
    bf16x8 fa[2][2][2];
    auto ldw = [&](int q) {
      const unsigned base = wo_slot(q >> 1) + (q & 1) * 2048;
      fa[q & 1][0][0] = lds_tr2(base + aw[0][0], base + aw[0][1]);
      fa[q & 1][1][0] = lds_tr2(base + aw[1][0], base + aw[1][1]);
    };
    ldw(0);
#define OLAG 6
#pragma unroll
    for (int sI = 0; sI < 16 + OLAG; ++sI) {
      if (sI < 16) {
        need(sI);
        if (sI == 0) { STAMP(26); }
#pragma unroll
        for (int j = 0; j < 2; ++j) of[sI][j] = __builtin_amdgcn_raw_buffer_load_b128(ro, ovo + (sI >> 3) * 4096 + j * 1024, (sI & 7) * MROWS * (DH * 2), 16);
        SCHED();
      }
      if (sI >= OLAG) {
#pragma unroll
        for (int j = 0; j < 2; ++j) {
          const int q = 2 * (sI - OLAG) + j;
          if (q == 19) rawbar();
          if (q == 0) { STAMP(27); }
          if (q + 1 < 32) ldw(q + 1);
          const bf16x8 ob = __builtin_bit_cast(bf16x8, of[q >> 1][q & 1]);
          acc[0] = MFMA(fa[q & 1][0][0], ob, acc[0]);
          acc[1] = MFMA(fa[q & 1][1][0], ob, acc[1]);
          SCHED();
        }
      }
    }
    STAMP(24);
    {
      const unsigned ew = 96u * 1024u + wid * 4096 + lane * 16, er = 96u * 1024u + (wid ^ 4) * 4096 + lane * 16;
      auto put = [&](const f32x16& v) {
#pragma unroll
        for (int g = 0; g < 4; ++g) {
          f32x4 w = {v[4 * g], v[4 * g + 1], v[4 * g + 2], v[4 * g + 3]};
          *(LDSP f32x4*)(uintptr_t)(ew + g * 1024) = w;
        }
      };
      if (k2 == 0) put(acc[1]); else put(acc[0]);
      bar();
      auto get = [&](const f32x16& own, bool first) {
        f32x16 o;
#pragma unroll
        for (int g = 0; g < 4; ++g) {
          const f32x4 w = *(const LDSP f32x4*)(uintptr_t)(er + g * 1024);
#pragma unroll
          for (int e = 0; e < 4; ++e) o[4 * g + e] = own[4 * g + e] + w[e];
        }
        return o;
      };
      if (k2 == 0) acc[0] = get(acc[0], true); else acc[0] = get(acc[1], false);
    }
    float* sl = slab + (size_t)tile * 4 * 4 * 2 * 1024;
    if (mt != kq) {
      const __amdgpu_buffer_rsrc_t rs = __builtin_amdgcn_make_buffer_rsrc((void*)(sl + ((mt * 4 + kq) * 2 + nh) * 1024), 0, 4096, 0x00020000);
#pragma unroll
      for (int g = 0; g < 4; ++g) {
        f32x4 w = {acc[0][4 * g], acc[0][4 * g + 1], acc[0][4 * g + 2], acc[0][4 * g + 3]};
        __builtin_amdgcn_raw_buffer_store_b128(__builtin_bit_cast(u32x4, w), rs, g * 1024 + lane * 16, 0, 16);
      }
    }
    asm volatile("s_waitcnt vmcnt(0)" ::: "memory");
    __syncthreads();
    if (wid == 0 && lane < 4 && lane != kq)
      __hip_atomic_store(flagB + (tile * 4 + lane) * 4 + kq, tag, __ATOMIC_RELAXED, __HIP_MEMORY_SCOPE_AGENT);
    if (mt == kq) {
      wait_flags(flagB + (tile * 4 + kq) * 4 + (lane & 3), lane < 4 && lane != kq, tag);
      u32x4 pv[3][4];
#pragma unroll
      for (int o = 0; o < 3; ++o) {
        const int src = o + (o >= kq);
        const __amdgpu_buffer_rsrc_t rs = __builtin_amdgcn_make_buffer_rsrc((void*)(sl + ((kq * 4 + src) * 2 + nh) * 1024), 0, 4096, 0x00020000);
#pragma unroll
        for (int g = 0; g < 4; ++g) pv[o][g] = __builtin_amdgcn_raw_buffer_load_b128(rs, g * 1024 + lane * 16, 0, 16);
      }
      float r[16];
#pragma unroll
      for (int g = 0; g < 4; ++g) {
        const f32x4 a = __builtin_bit_cast(f32x4, pv[0][g]), c = __builtin_bit_cast(f32x4, pv[1][g]), d = __builtin_bit_cast(f32x4, pv[2][g]);
#pragma unroll
        for (int e = 0; e < 4; ++e) {
          const float own = acc[0][4 * g + e];
          const float t0 = kq == 0 ? own : a[e];
          const float t1 = kq == 0 ? a[e] : (kq == 1 ? own : c[e]);
          const float t2 = kq <= 1 ? c[e] : (kq == 2 ? own : d[e]);
          const float t3 = kq == 3 ? own : d[e];
          r[4 * g + e] = ((t0 + t1) + t2) + t3;
        }
      }
      char* dst = (char*)out + ((size_t)(tm * 128 + 32 * mt + l31) * DM + tn * 64 + nh * 32 + 16 * h) * 2;
      u32x4 w0 = {cvtpk(r[0], r[1]), cvtpk(r[2], r[3]), cvtpk(r[4], r[5]), cvtpk(r[6], r[7])};
      u32x4 w1 = {cvtpk(r[8], r[9]), cvtpk(r[10], r[11]), cvtpk(r[12], r[13]), cvtpk(r[14], r[15])};
      *(u32x4*)dst = w0;
      *(u32x4*)(dst + 16) = w1;
    }
  }
  STAMP(25);
  STAMP_FLUSH;
}

extern "C" void kernel_launch(void* const* d_in, const int* in_sizes, int n_in, void* d_out, int out_size, void* d_ws,
                              size_t ws_size, hipStream_t stream) {
  const __hip_bfloat16* x = (const __hip_bfloat16*)d_in[0];
  const __hip_bfloat16* kc = (const __hip_bfloat16*)d_in[1];
  const __hip_bfloat16* vc = (const __hip_bfloat16*)d_in[2];
  const __hip_bfloat16* wq = (const __hip_bfloat16*)d_in[3];
  const __hip_bfloat16* wo = (const __hip_bfloat16*)d_in[4];
  __hip_bfloat16* o_ws = (__hip_bfloat16*)d_ws;
  float* slab = (float*)((char*)d_ws + (size_t)8 * 1024 * 1024);
  unsigned long long* flags = (unsigned long long*)((char*)d_ws + (size_t)24 * 1024 * 1024);
  hipFuncSetAttribute((const void*)k_fused, hipFuncAttributeMaxDynamicSharedMemorySize, K1_LDS);
  hipLaunchKernelGGL(k_fused, dim3(256), dim3(512), K1_LDS, stream, x, kc, vc, wq, wo, o_ws, slab, flags, (__hip_bfloat16*)d_out);
}
```

```cpp
#include <hip/hip_runtime.h>
#include <hip/hip_bf16.h>
#include <stdint.h>

typedef __attribute__((ext_vector_type(8))) __bf16 bf16x8;
typedef __attribute__((ext_vector_type(4))) __bf16 bf16x4;
typedef __attribute__((ext_vector_type(2))) __bf16 bf16x2;
typedef __attribute__((ext_vector_type(16))) float f32x16;
typedef __attribute__((ext_vector_type(4))) float f32x4;
typedef __attribute__((ext_vector_type(2))) float f32x2;
typedef __attribute__((ext_vector_type(4))) unsigned u32x4;
#define LDSP __attribute__((address_space(3)))
#define DI __device__ __forceinline__

constexpr int NB = 8, SQ = 112, DM = 576, HQ = 32, HKV = 16, SK = 512, DH = 128;
constexpr int MROWS = NB * SQ;
constexpr int KO = HQ * DH;

DI unsigned cvtpk(float lo, float hi) {
  f32x2 v = {lo, hi};
  bf16x2 b = __builtin_convertvector(v, bf16x2);
  return __builtin_bit_cast(unsigned, b);
}
DI void glds16(const void* sbase, unsigned voff, unsigned lds_dst) {
  unsigned keep;
  asm volatile("s_mov_b32 %0, m0\n\ts_mov_b32 m0, %3\n\ts_nop 0\n\tglobal_load_lds_dwordx4 %1, %2\n\ts_mov_b32 m0, %0"
               : "=&s"(keep) : "v"(voff), "s"(sbase), "s"(lds_dst) : "memory");
}
DI void glds16nt(const void* sbase, unsigned voff, unsigned lds_dst) {
  unsigned keep;
  asm volatile("s_mov_b32 %0, m0\n\ts_mov_b32 m0, %3\n\ts_nop 0\n\tglobal_load_lds_dwordx4 %1, %2 nt\n\ts_mov_b32 m0, %0"
               : "=&s"(keep) : "v"(voff), "s"(sbase), "s"(lds_dst) : "memory");
}
DI void glds16sc1(const void* sbase, unsigned voff, unsigned lds_dst) {
  unsigned keep;
  asm volatile("s_mov_b32 %0, m0\n\ts_mov_b32 m0, %3\n\ts_nop 0\n\tglobal_load_lds_dwordx4 %1, %2 sc1\n\ts_mov_b32 m0, %0"
               : "=&s"(keep) : "v"(voff), "s"(sbase), "s"(lds_dst) : "memory");
}
template <int J> DI void glds16_if(const void* sbase, unsigned voff, unsigned lds_dst, int sel) {
  unsigned keep;
  asm volatile("s_cmp_lg_u32 %4, %5\n\ts_cbranch_scc1 .Lgl%=\n\ts_mov_b32 %0, m0\n\ts_mov_b32 m0, %3\n\ts_nop 0\n\tglobal_load_lds_dwordx4 %1, %2\n\ts_mov_b32 m0, %0\n.Lgl%=:"
               : "=&s"(keep) : "v"(voff), "s"(sbase), "s"(lds_dst), "s"(sel), "i"(J) : "memory", "scc");
}
template <int J> DI void glds16nt_if(const void* sbase, unsigned voff, unsigned lds_dst, int sel) {
  unsigned keep;
  asm volatile("s_cmp_lg_u32 %4, %5\n\ts_cbranch_scc1 .Lgl%=\n\ts_mov_b32 %0, m0\n\ts_mov_b32 m0, %3\n\ts_nop 0\n\tglobal_load_lds_dwordx4 %1, %2 nt\n\ts_mov_b32 m0, %0\n.Lgl%=:"
               : "=&s"(keep) : "v"(voff), "s"(sbase), "s"(lds_dst), "s"(sel), "i"(J) : "memory", "scc");
}
template <bool NT, int J> DI void gl_t(const void* sbase, unsigned voff, unsigned lds_dst, int sel) {
  if constexpr (J < 0) { if constexpr (NT) glds16nt(sbase, voff, lds_dst); else glds16(sbase, voff, lds_dst); }
  else { if constexpr (NT) glds16nt_if<J>(sbase, voff, lds_dst, sel); else glds16_if<J>(sbase, voff, lds_dst, sel); }
}
template <int V> struct IC { static constexpr int value = V; };
template <int I, int N, class F> DI void sfor(F&& f) { if constexpr (I < N) { f(IC<I>{}); sfor<I + 1, N>(f); } }
DI void gl_sel(bool nt, int j, const void* sbase, unsigned voff, unsigned lds_dst, int sel) {
  if (nt) {
    if (j < 0) glds16nt(sbase, voff, lds_dst);
    else if (j == 0) glds16nt_if<0>(sbase, voff, lds_dst, sel); else if (j == 1) glds16nt_if<1>(sbase, voff, lds_dst, sel);
    else if (j == 2) glds16nt_if<2>(sbase, voff, lds_dst, sel); else glds16nt_if<3>(sbase, voff, lds_dst, sel);
  } else {
    if (j < 0) glds16(sbase, voff, lds_dst);
    else if (j == 0) glds16_if<0>(sbase, voff, lds_dst, sel); else if (j == 1) glds16_if<1>(sbase, voff, lds_dst, sel);
    else if (j == 2) glds16_if<2>(sbase, voff, lds_dst, sel); else glds16_if<3>(sbase, voff, lds_dst, sel);
  }
}
DI void set_m0(unsigned base, unsigned imm) { asm volatile("s_add_u32 m0, %0, %1\n\ts_nop 0" ::"s"(base), "i"(imm) : "scc"); }
template <int OFF, int POL> DI void gld(const void* sbase, unsigned voff) {
  if constexpr (POL == 0) asm volatile("global_load_lds_dwordx4 %0, %1 offset:%2" ::"v"(voff), "s"(sbase), "i"(OFF) : "memory");
  else if constexpr (POL == 1) asm volatile("global_load_lds_dwordx4 %0, %1 offset:%2 nt" ::"v"(voff), "s"(sbase), "i"(OFF) : "memory");
  else asm volatile("global_load_lds_dwordx4 %0, %1 offset:%2 sc1" ::"v"(voff), "s"(sbase), "i"(OFF) : "memory");
}
template <int N> DI void wait_lgkm() { __builtin_amdgcn_s_waitcnt(0xC07F | (N << 8)); }
#define GL_KV glds16nt
#define GL_XW glds16
#define GL_WO glds16
#define GL_O glds16
template <int N> DI void wait_vm() { asm volatile("s_waitcnt vmcnt(%0)" ::"i"(N) : "memory"); }
DI void bar() { asm volatile("s_waitcnt lgkmcnt(0)\n\ts_barrier" ::: "memory"); }
DI bf16x8 lds_b128(unsigned a) { return *(const LDSP bf16x8*)(uintptr_t)a; }
DI bf16x8 lds_tr2(unsigned a0, unsigned a1) {
  bf16x4 lo = __builtin_amdgcn_ds_read_tr16_b64_v4bf16((LDSP bf16x4*)(uintptr_t)a0);
  bf16x4 hi = __builtin_amdgcn_ds_read_tr16_b64_v4bf16((LDSP bf16x4*)(uintptr_t)a1);
  return __builtin_shufflevector(lo, hi, 0, 1, 2, 3, 4, 5, 6, 7);
}
DI bf16x8 pack8(const f32x16& x, int s) {
  u32x4 w = {cvtpk(x[8 * s], x[8 * s + 1]), cvtpk(x[8 * s + 2], x[8 * s + 3]), cvtpk(x[8 * s + 4], x[8 * s + 5]),
             cvtpk(x[8 * s + 6], x[8 * s + 7])};
  return __builtin_bit_cast(bf16x8, w);
}
#define STAMP(i)
#define CSTAMP(i)
#define STAMP_DECL
#define STAMP_FLUSH
#define STAMP2(i)
#define CSTAMP2(i)
#define STAMP_DECL2
#define STAMP_FLUSH2
#define MFMA(a, b, c) __builtin_amdgcn_mfma_f32_32x32x16_bf16((a), (b), (c), 0, 0, 0)

constexpr unsigned SUB = 16384u;
constexpr unsigned RGB = 65536u;
constexpr unsigned MISC = 131072u;
#define STAG1 0
#define STAG2 0
#define WO4 ((wid & 1) + 2 * (wid >> 2))
#define PIPE_MU 1
#define PRIO 3
#define HEADSKIP 0
#define SUMDT 3
#define NT1 4
#define E1 67
#define E2 93
struct SmSched {
  int mu[129];
  int tm[128];
  int rs[128];
  constexpr SmSched() : mu{}, tm{}, rs{} {
    int n[128] = {};
    for (int m = 0; m < 128; ++m) { tm[m] = -1; rs[m] = -1; }
    for (int j = 0; j < 8; ++j) tm[8 * j + (j == 0 ? 14 : (j == NT1 - 1 || j == 7) ? 9 : 10)] = j;
    const int sw = 64 + 4 * NT1;
    for (int d = 0; d < 2; ++d) rs[sw - 1 + d] = d;
    {
      const int a = 8 * NT1 + 3, e = E1; int av = 0, acc = 0;
      for (int m = a; m < e; ++m) if (tm[m] < 0) ++av;
      for (int m = a; m < e; ++m) if (tm[m] < 0) { acc += 8 * NT1; while (acc >= av) { acc -= av; ++n[m]; } }
    }
    {
      const int a = 67, e = E2; int av = 0, acc = 0;
      for (int m = a; m < e; ++m) if (rs[m] < 0) ++av;
      for (int m = a; m < e; ++m) if (rs[m] < 0) { acc += 64 - 8 * NT1; while (acc >= av) { acc -= av; ++n[m]; } }
    }
    int k = 0;
    for (int m = 0; m < 128; ++m) { mu[m] = k; k += n[m]; }
    mu[128] = k;
  }
  constexpr bool ok() const {
    if (mu[128] != 64) return false;
    for (int m = 0; m < 128; ++m) if (mu[m + 1] - mu[m] > 2) return false;
    for (int m = 0; m < 128; ++m)
      for (int k = mu[m]; k < mu[m + 1]; ++k) {
        if (m + 1 >= 64 + 2 * (k >> 2)) return false;
        if (k >= 8 * NT1 && m < 67) return false;
      }
    return true;
  }
};
constexpr SmSched SM{};
static_assert(SM.ok(), "softmax schedule misses a deadline");
constexpr int K1_LDS = 160 * 1024;
#define SCHED() __builtin_amdgcn_sched_barrier(0)
#define NOPK(x) asm("" : "+v"(x))
DI void rawbar() { asm volatile("s_barrier" ::: "memory"); }

typedef __attribute__((address_space(1))) unsigned long long gu64;
extern "C" __device__ unsigned long long hw_dispatch_id() __asm("llvm.amdgcn.dispatch.id");
DI constexpr unsigned wo_slot(int t) {
  constexpr unsigned pb[8] = {128u, 144u, 16u, 80u, 0u, 64u, 32u, 96u};
  return pb[t >> 1] * 1024u + (unsigned)(t & 1) * 8192u;
}
DI void wait_flags(gu64* f, bool on, unsigned long long tag) {
  for (unsigned spins = 0; spins < (1u << 20); ++spins) {
    const unsigned long long v = on ? __hip_atomic_load(f, __ATOMIC_RELAXED, __HIP_MEMORY_SCOPE_AGENT) : tag;
    if (__all(v == tag)) break;
    __builtin_amdgcn_s_sleep(1);
  }
  asm volatile("" ::: "memory");
}

__global__ __launch_bounds__(512, 2) void k_fused(const __hip_bfloat16* __restrict__ x, const __hip_bfloat16* __restrict__ kc,
                                                   const __hip_bfloat16* __restrict__ vc, const __hip_bfloat16* __restrict__ wq,
                                                   const __hip_bfloat16* __restrict__ wo, __hip_bfloat16* o_ws,
                                                   float* slab, unsigned long long* flags,
                                                   __hip_bfloat16* __restrict__ out) {
  extern __shared__ __attribute__((aligned(16))) char lds[];
  const int tid = threadIdx.x, lane = tid & 63;
  const int wid = __builtin_amdgcn_readfirstlane(tid >> 6);
  const int rg = wid & 3, kh = wid >> 2;
  const int l31 = lane & 31, h = lane >> 5;
  const int bid = blockIdx.x;
  const int hq = 4 * (bid & 7) + ((bid >> 3) & 3), b = bid >> 5;
  const int hkv = hq >> 1;
  STAMP_DECL;

  unsigned vx[2], vw[2], vkv[4];
#pragma unroll
  for (int e = 0; e < 2; ++e) {
    const int i = 2 * wid + e;
    int row = 8 * i + (lane >> 3);
    const int c = (lane & 7) ^ ((row >> 1) & 7);
    row = row < SQ ? row : SQ - 1;
    vx[e] = (unsigned)(row * (DM * 2) + c * 16 - e * 1024 + 1024);
    const int rw = 4 * i + (lane >> 4);
    const int cw = (lane & 15) ^ (((rw & 3) << 2) | ((rw >> 2) & 3));
    vw[e] = (unsigned)(rw * (KO * 2) + cw * 16 - e * 1024 + 1024);
  }
#pragma unroll
  for (int e = 0; e < 4; ++e) {
    const int r = 4 * (4 * (wid & 3) + e) + (lane >> 4);
    const int c = (lane & 15) ^ (((r & 3) << 2) | ((r >> 2) & 3));
    vkv[e] = (unsigned)((r - 4 * e) * 256 + c * 16);
  }
  const unsigned vvo = (unsigned)((32 * rg + (lane >> 3)) * 256 + (((lane & 7) ^ (((lane >> 4) & 1) << 2)) * 16));
  const char* xb = (const char*)x + (size_t)b * SQ * DM * 2 - 1024;
  const char* wb = (const char*)wq + (size_t)hq * DH * 2 - 1024;
  const unsigned dX = (unsigned)wid * 2048u, dK = (unsigned)(kh * RGB + rg * 4096), dW = (unsigned)wid * 1024u;
  const size_t kvo = ((size_t)(b * HKV + hkv) * SK + 256 * kh) * DH * 2;
  const char* kb = (const char*)kc + kvo;
  const char* vb = (const char*)vc + kvo;

  auto issue1 = [&](auto Tc, auto Ec) {
    constexpr int t = decltype(Tc)::value, e = decltype(Ec)::value;
    constexpr unsigned sb = (unsigned)(t & 3) * SUB;
    if constexpr (t < 9) {
      if constexpr (e == 0) set_m0(dX, sb);
      if constexpr (e == 2) set_m0(dX, RGB + sb);
      if constexpr (e < 2) gld<e * 1024, 0>(xb + t * 128, vx[e]);
      else                 gld<(e - 2) * 1024, 0>(wb + (size_t)t * 64 * KO * 2, vw[e - 2]);
    } else if constexpr (t < 17) {
      if constexpr (e == 0) set_m0(dK, sb);
      if constexpr (t < 13) gld<e * 1024, 1>(kb + (t - 9) * 16384, vkv[e]);
      else                  gld<e * 1024, 1>(vb + ((t - 13) & 1) * 32768 + ((t - 13) >> 1) * 128 + e * 1024, vvo);
    }
  };
  if (kh) __builtin_amdgcn_s_setprio(1);
  STAMP(0); CSTAMP(20);
  sfor<0, 3>([&](auto Tc) { sfor<0, 4>([&](auto Ec) { issue1(Tc, Ec); }); });

  const int q4 = (lane & 15) >> 2, p4 = lane & 3, g16 = (lane >> 4) & 1;
  unsigned ax[4];
#pragma unroll
  for (int ks = 0; ks < 4; ++ks) ax[ks] = (unsigned)((32 * rg + l31) * 128 + (((2 * ks + h) ^ ((l31 >> 1) & 7)) * 16));
  unsigned aw[2][2];
#pragma unroll
  for (int dl = 0; dl < 2; ++dl)
#pragma unroll
    for (int t = 0; t < 2; ++t) {
      const int dt = 2 * kh + dl;
      const int k = 8 * h + 4 * t + q4;
      const int c = (4 * dt + 2 * g16 + (p4 & 1)) ^ ((q4 << 2) | ((2 * h + t) & 3));
      aw[dl][t] = (unsigned)(RGB + k * 256 + c * 16 + (p4 >> 1) * 8);
    }
  unsigned ak[8];
#pragma unroll
  for (int ks = 0; ks < 8; ++ks)
    ak[ks] = (unsigned)(kh * RGB + l31 * 256 + (((2 * ks + h) ^ (8 * kh) ^ (((l31 & 3) << 2) | ((l31 >> 2) & 3))) * 16));
  unsigned av[2][2];
#pragma unroll
  for (int dl = 0; dl < 2; ++dl)
#pragma unroll
    for (int t = 0; t < 2; ++t) {
      const int key = 8 * t + 4 * h + q4;
      const int c = (4 * (dl ^ kh) + 2 * (p4 & 1) + g16) ^ (((q4 >> 1) & 1) << 2);
      av[dl][t] = (unsigned)(kh * RGB + key * 128 + c * 16 + (p4 >> 1) * 8);
    }

  bf16x8 f1[3][3];
  auto ld1 = [&](int G) {
    const unsigned sb = (unsigned)((G >> 2) & 3) * SUB; const int ks = G & 3, r = G % 3;
    f1[r][0] = lds_b128(sb + ax[ks]);
    f1[r][1] = lds_tr2(sb + aw[0][0] + ks * 4096, sb + aw[0][1] + ks * 4096);
    f1[r][2] = lds_tr2(sb + aw[1][0] + ks * 4096, sb + aw[1][1] + ks * 4096);
  };
  bf16x8 f2[3][4];
  auto ld2 = [&](int G) {
    const int kt = G < 4 ? (G & 1) : ((G >> 1) & 1), hf = G < 4 ? (G >> 1) : (G & 1);
    const unsigned sb = (unsigned)((9 + (G >> 2)) & 3) * SUB + kt * 8192; const int r = G % 3;
#pragma unroll
    for (int j = 0; j < 4; ++j) f2[r][j] = lds_b128(sb + ak[4 * hf + j]);
  };
  auto ld3 = [&](int G) {
    const int r = (G + 1) % 3;
    if (G < 8) {
      const unsigned sb = (unsigned)((13 + (G >> 2)) & 3) * SUB + (G & 3) * 4096;
#pragma unroll
      for (int j = 0; j < 4; ++j) f2[r][j] = lds_tr2(sb + (j >> 1) * 2048 + av[j & 1][0], sb + (j >> 1) * 2048 + av[j & 1][1]);
    } else {
      const int dl = G < 12 ? 1 : 0;
#pragma unroll
      for (int j = 0; j < 4; ++j) {
        const int f = 4 * (G & 3) + j;
        const unsigned sb = (unsigned)((15 + (f >> 3)) & 3) * SUB + (f & 7) * 2048;
        f2[r][j] = lds_tr2(sb + av[dl][0], sb + av[dl][1]);
      }
    }
  };

  wait_vm<8>(); rawbar();
  ld1(0); ld1(1);
  SCHED();

  f32x16 qa[2] = {};
  sfor<0, 9>([&](auto Tc) {
    constexpr int t = decltype(Tc)::value;
    if constexpr (t < 8) wait_vm<4>();
    rawbar();
    STAMP(1 + t);
    sfor<0, 4>([&](auto Kc) {
      constexpr int ks = decltype(Kc)::value;
      constexpr int G = 4 * t + ks;
      if constexpr (G == 34) { wait_vm<6>(); rawbar(); }
      issue1(IC<t + 3>{}, Kc);
      wait_lgkm<5>();
      if constexpr (G + 2 < 36) ld1(G + 2); else ld2(G + 2 - 36);
      qa[0] = MFMA(f1[G % 3][1], f1[G % 3][0], qa[0]);
      qa[1] = MFMA(f1[G % 3][2], f1[G % 3][0], qa[1]);
      SCHED();
    });
  });
  bf16x8 qf[8];
  qf[0] = pack8(qa[0], 0); qf[1] = pack8(qa[0], 1); qf[2] = pack8(qa[1], 0); qf[3] = pack8(qa[1], 1);
  {
    const unsigned qw = MISC + wid * 4096 + lane * 16;
#pragma unroll
    for (int f = 0; f < 4; ++f) *(LDSP bf16x8*)(uintptr_t)(qw + f * 1024) = qf[f];
  }
  SCHED();
  CSTAMP(22);
  const float CS = 0.08838834764831845f * 1.4426950408889634f;
  const int wo4 = WO4;
  f32x16 s[8];
  unsigned pw[16][4];
  float m1 = 0.f, mx = 0.f, mb1 = 0.f, mb2 = 0.f, al = 0.f;
  const unsigned selw = ((lane & 15) == ((lane >> 4) & 1)) ? 0x3F803F80u : 0u;
  const u32x4 sel4 = {selw, selw, selw, selw};
  f32x4 lsum[2];
  float rm0 = -__builtin_inff(), rm1 = -__builtin_inff();

  const int kq = (bid & 7) >> 1;
  const int tile0 = (bid & 1) ? 32 + (bid >> 3) : (bid >> 3);
  const bool activeB = tile0 < 63;
  const int tile = activeB ? tile0 : 0;
  const int tm = tile / 9, tn = tile % 9;
  const char* wob = (const char*)wo + ((size_t)kq * 1024 * DM + tn * 64) * 2;
  unsigned vwo;
  {
    const int row = 8 * wid + (lane >> 3);
    const int c = (lane & 7) ^ (((row >> 1) & 1) << 2);
    vwo = (unsigned)(row * (DM * 2) + c * 16);
  }
  auto issueWr = [&](int u) { set_m0(dW, wo_slot(u)); gld<0, 0>(wob + (size_t)(2 * (u & 7) + (u >> 3)) * 64 * DM * 2, vwo); };

  f32x16 oa[4];
  float pe[2][2];
  auto vwork = [&](auto Mc) {
    constexpr int m = decltype(Mc)::value;
    if constexpr (SM.tm[m] >= 0) {
      constexpr int j = SM.tm[m];
#pragma unroll
      for (int r = 0; r < 16; r += 4) {
        rm0 = fmaxf(fmaxf(rm0, s[j][r]), s[j][r + 1]);
        rm1 = fmaxf(fmaxf(rm1, s[j][r + 2]), s[j][r + 3]);
      }
      if constexpr (j == NT1 - 1 || j == 7) {
        float m_ = fmaxf(rm0, rm1);
        auto rr = __builtin_amdgcn_permlane32_swap(__float_as_uint(m_), __float_as_uint(m_), false, false);
        m_ = fmaxf(__uint_as_float(rr[0]), __uint_as_float(rr[1]));
        if constexpr (j == 7) {
          mx = m_; mb2 = mx * CS;
          al = __builtin_amdgcn_exp2f((m1 - mx) * CS);
        } else { m1 = m_; mb1 = m1 * CS; }
      }
    }
    if constexpr (SM.rs[m] >= 0) {
      constexpr int d = SM.rs[m];
#pragma unroll
      for (int r = 0; r < 16; ++r) { float t_ = oa[d][r] * al; NOPK(t_); oa[d][r] = t_; }
    }
    if constexpr (m > 0) {
      sfor<SM.mu[m - 1], SM.mu[m]>([&](auto Kc) {
        constexpr int k = decltype(Kc)::value, f = k >> 2, q = k & 3;
        pw[f][q] = cvtpk(pe[k & 1][0], pe[k & 1][1]);
      });
    }
    sfor<SM.mu[m], SM.mu[m + 1]>([&](auto Kc) {
      constexpr int k = decltype(Kc)::value, f = k >> 2, q = k & 3;
      const float mb = k < 8 * NT1 ? mb1 : mb2;
      pe[k & 1][0] = __builtin_amdgcn_exp2f(fmaf(s[f >> 1][8 * (f & 1) + 2 * q], CS, -mb));
      pe[k & 1][1] = __builtin_amdgcn_exp2f(fmaf(s[f >> 1][8 * (f & 1) + 2 * q + 1], CS, -mb));
    });
  };
  sfor<0, 16>([&](auto Gc) {
    constexpr int G = decltype(Gc)::value, i = G >> 2, t = 9 + i;
    constexpr int kt = G < 4 ? (G & 1) : ((G >> 1) & 1), hf = G < 4 ? (G >> 1) : (G & 1);
    if constexpr ((G & 3) == 0) {
      if constexpr (i > 0) { wait_vm<4>(); rawbar(); }
      STAMP(1 + t);
    }
    if constexpr (G == 1) {
      bar();
      const unsigned qr = MISC + (wid ^ 4) * 4096 + lane * 16;
#pragma unroll
      for (int f = 0; f < 4; ++f) qf[4 + f] = lds_b128(qr + f * 1024);
      issue1(IC<12>{}, IC<0>{});
    }
    if constexpr (G == 2) { wait_vm<6>(); rawbar(); }
    if constexpr (G > 0) issue1(IC<t + 3>{}, IC<(G & 3)>{});
    if constexpr (G == 15) wait_lgkm<8>(); else if constexpr (G == 1) wait_lgkm<8>(); else wait_lgkm<4>();
    if constexpr (G + 2 < 16) ld2(G + 2); else ld3(G + 2 - 16);
    sfor<0, 4>([&](auto Jc) {
      constexpr int j = decltype(Jc)::value;
      if constexpr (hf == 0 && j == 0) s[2 * i + kt] = MFMA(f2[G % 3][0], qf[0], f32x16{});
      else s[2 * i + kt] = MFMA(f2[G % 3][j], qf[4 * hf + j], s[2 * i + kt]);
      vwork(IC<4 * G + j>{});
      SCHED();
    });
  });
  const unsigned long long tag = ((hw_dispatch_id() + 1ull) * 0x9E3779B97F4A7C15ull) ^ (unsigned long long)(uintptr_t)__builtin_amdgcn_dispatch_ptr();
  gu64* flagA = (gu64*)flags;
  gu64* flagB = (gu64*)flags + 512;
  const int qrow = 32 * rg + l31, R = b * SQ + qrow;
  const __amdgpu_buffer_rsrc_t rso = __builtin_amdgcn_make_buffer_rsrc((char*)o_ws + (size_t)hq * MROWS * (DH * 2), 0, MROWS * DH * 2, 0x00020000);
  const int so = (R >> 5) * 8192 + (2 * kh + h) * 1024 + (R & 31) * 16;
  constexpr unsigned MLB = 88u * 1024u;
  const unsigned xw = (unsigned)(32768 + kh * RGB + rg * 4096) + lane * 16, xr = (unsigned)(32768 + (kh ^ 1) * RGB + rg * 4096) + lane * 16;
  float g0 = 0.f, g1 = 0.f;
  f32x4 xv[4];
  f32x16 ob[2];
  auto put1 = [&](int g) {
    f32x4 w = {oa[1][4 * g], oa[1][4 * g + 1], oa[1][4 * g + 2], oa[1][4 * g + 3]};
    *(LDSP f32x4*)(uintptr_t)(xw + g * 1024) = w;
  };
  auto put2 = [&](int g) {
    f32x4 w;
#pragma unroll
    for (int e = 0; e < 4; ++e) { float t_ = fmaf(oa[3][4 * g + e], al, ob[1][4 * g + e]); NOPK(t_); w[e] = t_; }
    *(LDSP f32x4*)(uintptr_t)(xr + g * 1024) = w;
  };
  auto get = [&](unsigned a) {
#pragma unroll
    for (int g = 0; g < 4; ++g) xv[g] = *(const LDSP f32x4*)(uintptr_t)(a + g * 1024);
  };
  u32x4 ow[2];
  auto comb1 = [&](int w) {
    float r[8];
#pragma unroll
    for (int e = 0; e < 8; ++e) { float t_ = oa[0][8 * w + e] * g0; NOPK(t_); r[e] = fmaf(xv[2 * w + (e >> 2)][e & 3], g1, t_); NOPK(r[e]); }
    ow[w] = u32x4{cvtpk(r[0], r[1]), cvtpk(r[2], r[3]), cvtpk(r[4], r[5]), cvtpk(r[6], r[7])};
  };
  auto comb2 = [&](int w, float ga) {
    float r[8];
#pragma unroll
    for (int e = 0; e < 8; ++e) r[e] = fmaf(oa[2][8 * w + e], ga, fmaf(ob[0][8 * w + e], g0, xv[2 * w + (e >> 2)][e & 3]));
    ow[w] = u32x4{cvtpk(r[0], r[1]), cvtpk(r[2], r[3]), cvtpk(r[4], r[5]), cvtpk(r[6], r[7])};
  };
  auto ostore = [&](int p) {
    if (qrow < SQ) {
      __builtin_amdgcn_raw_buffer_store_b128(ow[0], rso, so + p * 4096, 0, 16);
      __builtin_amdgcn_raw_buffer_store_b128(ow[1], rso, so + p * 4096 + 512, 0, 16);
    }
  };
  float lq[4];
  f32x2 pml;
#define FLAGT 448
#define XB 9
#define XB2 13
  auto hook = [&](auto Mc) {
    constexpr int m = decltype(Mc)::value;
    if constexpr (m == 96) {
      const int src = (lane & 15) * 4;
      lq[0] = __int_as_float(__builtin_amdgcn_ds_bpermute(src, __float_as_int(lsum[0][0])));
      lq[1] = __int_as_float(__builtin_amdgcn_ds_bpermute(src, __float_as_int(lsum[0][1])));
      lq[2] = __int_as_float(__builtin_amdgcn_ds_bpermute(src, __float_as_int(lsum[1][0])));
      lq[3] = __int_as_float(__builtin_amdgcn_ds_bpermute(src, __float_as_int(lsum[1][1])));
    }
    if constexpr (m == 97) { put1(0); put1(1); }
    if constexpr (m == 98) { put1(2); put1(3); }
    if constexpr (m == 99) {
      const float ls = (lane & 16) ? lq[1] * al + lq[3] : lq[0] * al + lq[2];
      lq[0] = ls;
      f32x2 ml = {mx, ls};
      *(LDSP f32x2*)(uintptr_t)(MLB + wid * 512 + lane * 8) = ml;
    }
    constexpr int x0 = 64 + 4 * XB;
    if constexpr (m == x0) {
      pml = *(const LDSP f32x2*)(uintptr_t)(MLB + (wid ^ 4) * 512 + lane * 8);
      get(xr);
    }
    if constexpr (m == x0 + 2) {
      const float mt = fmaxf(mx, pml[0]);
      const float f0 = __builtin_amdgcn_exp2f((mx - mt) * CS), f1_ = __builtin_amdgcn_exp2f((pml[0] - mt) * CS);
      const float inv = 1.f / (f0 * lq[0] + f1_ * pml[1]);
      g0 = f0 * inv; g1 = f1_ * inv;
    }
    if constexpr (m == x0 + 3) comb1(0);
    if constexpr (m == x0 + 4) comb1(1);
    if constexpr (m == x0 + 5) ostore(0);
    if constexpr (m == 113) { put2(0); put2(1); }
    if constexpr (m == 114) { put2(2); put2(3); }
    constexpr int x1 = 64 + 4 * XB2;
    if constexpr (m == x1) get(xw);
    if constexpr (m == x1 + 2 || m == x1 + 3) {
#pragma unroll
      for (int g = 2 * (m - x1 - 2); g < 2 * (m - x1 - 2) + 2; ++g)
#pragma unroll
        for (int e = 0; e < 4; ++e) { float t_ = xv[g][e] * g1; NOPK(t_); xv[g][e] = t_; }
    }
  };
  sfor<0, 16>([&](auto Gc) {
    constexpr int G = decltype(Gc)::value, i = G >> 2, t = 13 + i;
    if constexpr ((G & 3) == 0) {
      if constexpr (i < 3) { wait_vm<4>(); rawbar(); }
      STAMP(1 + t);
    }
    if constexpr (G == XB || G == XB2) bar();
    issue1(IC<t + 3>{}, IC<(G & 3)>{});
    if constexpr (i == 1 || (i == 2 && (G & 3) < 3)) issueWr(4 * (i - 1) + (G & 3));
    if constexpr (G < 15) wait_lgkm<8>(); else wait_lgkm<0>();
    if constexpr (G + 2 < 16) ld3(G + 2);
    sfor<0, 4>([&](auto Jc) {
      constexpr int j = decltype(Jc)::value;
      constexpr int f = G < 8 ? 2 * G + (j >> 1) : 4 * (G & 3) + j;
      constexpr int dt = G < 8 ? (j & 1) : G < 12 ? 3 : 2;
      const u32x4 pwv = {pw[f][0], pw[f][1], pw[f][2], pw[f][3]};
      const bf16x8 pb = __builtin_bit_cast(bf16x8, pwv);
      if constexpr (G < 8) {
        if constexpr (f == 0) oa[dt] = MFMA(f2[(G + 1) % 3][j], pb, f32x16{});
        else oa[dt] = MFMA(f2[(G + 1) % 3][j], pb, oa[dt]);
      } else if constexpr (f < 2 * NT1) {
        if constexpr (f == 0) oa[dt] = MFMA(f2[(G + 1) % 3][j], pb, f32x16{});
        else oa[dt] = MFMA(f2[(G + 1) % 3][j], pb, oa[dt]);
      } else {
        if constexpr (f == 2 * NT1) ob[dt & 1] = MFMA(f2[(G + 1) % 3][j], pb, f32x16{});
        else ob[dt & 1] = MFMA(f2[(G + 1) % 3][j], pb, ob[dt & 1]);
      }
      if constexpr (G < 8 && dt == 1) {
        constexpr int gp = f < 2 * NT1 ? 0 : 1;
        if constexpr (f == 0 || f == 2 * NT1)
          lsum[gp] = __builtin_amdgcn_mfma_f32_16x16x32_bf16(__builtin_bit_cast(bf16x8, sel4), pb, f32x4{}, 0, 0, 0);
        else
          lsum[gp] = __builtin_amdgcn_mfma_f32_16x16x32_bf16(__builtin_bit_cast(bf16x8, sel4), pb, lsum[gp], 0, 0, 0);
      }
      vwork(IC<64 + 4 * G + j>{});
      hook(IC<64 + 4 * G + j>{});
      SCHED();
    });
  });
  STAMP(18); CSTAMP(23);
  { const float ga = al * g0; comb2(0, ga); comb2(1, ga); }
  ostore(1);
  wait_vm<2>();
  bar();
  STAMP2(1);
  const bool fl = tid == FLAGT;
  if (fl) __hip_atomic_store(flagA + (b * 4 + (hq >> 3)) * 16 + (hq & 7), tag, __ATOMIC_RELAXED, __HIP_MEMORY_SCOPE_AGENT);
#pragma unroll
  for (int u = 7; u < 16; ++u) issueWr(u);
  STAMP2(3);
  const int mt = wid & 3, k2 = wid >> 2, nh = k2;
  const int T = 4 * tm + mt;
  const int b0 = (32 * T) / SQ, b1 = (32 * T + 31) / SQ;
  gu64* myf = flagA + (((lane & 16) ? b1 : b0) * 4 + kq) * 16 + (lane & 15);
  if (wid == (FLAGT >> 6)) wait_vm<10>(); else wait_vm<9>();
  STAMP2(4);
  unsigned long long pv0;
  asm volatile("global_load_dwordx2 %0, %1, off sc1" : "=v"(pv0) : "v"(myf) : "memory");
  rawbar();
  STAMP2(5);
  if (fl) __hip_atomic_store(flagA + (b * 4 + (hq >> 3)) * 16 + 8 + (hq & 7), tag, __ATOMIC_RELAXED, __HIP_MEMORY_SCOPE_AGENT);
  __builtin_amdgcn_s_setprio(0);
  if (wid == (FLAGT >> 6)) wait_vm<1>(); else wait_vm<0>();
  asm volatile("" : "+v"(pv0));
  STAMP(19); CSTAMP(21);
  if (!activeB) { STAMP_FLUSH; return; }

  {
    const int q4 = (lane & 15) >> 2, p4 = lane & 3, g16 = (lane >> 4) & 1;
    unsigned aw[2][2];
#pragma unroll
    for (int n2 = 0; n2 < 2; ++n2)
#pragma unroll
      for (int t = 0; t < 2; ++t) {
        const int k = 8 * h + 4 * t + q4;
        const int c = (4 * n2 + 2 * (p4 & 1) + g16) ^ (((q4 >> 1) & 1) << 2);
        aw[n2][t] = (unsigned)(k2 * 4096 + k * 128 + c * 16 + (p4 >> 1) * 8);
      }
    unsigned rdy;
    { const unsigned m = (unsigned)__ballot(lane >= 32 || pv0 == tag); rdy = m & (m >> 16) & 0xffffu; }
    auto need = [&](int sI) {
      if (!((rdy >> sI) & 1u)) {
        for (unsigned spins = 0; spins < (1u << 20); ++spins) {
          const unsigned long long v = lane < 32 ? __hip_atomic_load(myf, __ATOMIC_RELAXED, __HIP_MEMORY_SCOPE_AGENT) : tag;
          const unsigned m = (unsigned)__ballot(v == tag);
          rdy = m & (m >> 16) & 0xffffu;
          if ((rdy >> sI) & 1u) break;
          __builtin_amdgcn_s_sleep(1);
        }
        asm volatile("" ::: "memory");
      }
    };
    const __amdgpu_buffer_rsrc_t ro = __builtin_amdgcn_make_buffer_rsrc((char*)o_ws + (size_t)kq * 8 * MROWS * (DH * 2), 0, 8 * MROWS * DH * 2, 0x00020000);
    u32x4 of[16][2];
    const int ovo = T * 8192 + k2 * 2048 + lane * 16;
    f32x16 acc[2] = {};
    bf16x8 fa[2][2][2];
    auto ldw = [&](int q) {
      const unsigned base = wo_slot(q >> 1) + (q & 1) * 2048;
      fa[q & 1][0][0] = lds_tr2(base + aw[0][0], base + aw[0][1]);
      fa[q & 1][1][0] = lds_tr2(base + aw[1][0], base + aw[1][1]);
    };
    ldw(0);
#define OLAG 6
#pragma unroll
    for (int sI = 0; sI < 16 + OLAG; ++sI) {
      if (sI < 16) {
        need(sI);
        if (sI == 0) { STAMP(26); }
#pragma unroll
        for (int j = 0; j < 2; ++j) of[sI][j] = __builtin_amdgcn_raw_buffer_load_b128(ro, ovo + (sI >> 3) * 4096 + j * 1024, (sI & 7) * MROWS * (DH * 2), 16);
        SCHED();
      }
      if (sI >= OLAG) {
#pragma unroll
        for (int j = 0; j < 2; ++j) {
          const int q = 2 * (sI - OLAG) + j;
          if (q == 19) rawbar();
          if (q == 0) { STAMP(27); }
          if (q + 1 < 32) ldw(q + 1);
          const bf16x8 ob = __builtin_bit_cast(bf16x8, of[q >> 1][q & 1]);
          acc[0] = MFMA(fa[q & 1][0][0], ob, acc[0]);
          acc[1] = MFMA(fa[q & 1][1][0], ob, acc[1]);
          SCHED();
        }
      }
    }
    STAMP(24);
    {
      const unsigned ew = MISC + wid * 4096 + lane * 16, er = MISC + (wid ^ 4) * 4096 + lane * 16;
      auto put = [&](const f32x16& v) {
#pragma unroll
        for (int g = 0; g < 4; ++g) {
          f32x4 w = {v[4 * g], v[4 * g + 1], v[4 * g + 2], v[4 * g + 3]};
          *(LDSP f32x4*)(uintptr_t)(ew + g * 1024) = w;
        }
      };
      if (k2 == 0) put(acc[1]); else put(acc[0]);
      bar();
      auto get = [&](const f32x16& own, bool first) {
        f32x16 o;
#pragma unroll
        for (int g = 0; g < 4; ++g) {
          const f32x4 w = *(const LDSP f32x4*)(uintptr_t)(er + g * 1024);
#pragma unroll
          for (int e = 0; e < 4; ++e) o[4 * g + e] = own[4 * g + e] + w[e];
        }
        return o;
      };
      if (k2 == 0) acc[0] = get(acc[0], true); else acc[0] = get(acc[1], false);
    }
    float* sl = slab + (size_t)tile * 4 * 4 * 2 * 1024;
    if (mt != kq) {
      const __amdgpu_buffer_rsrc_t rs = __builtin_amdgcn_make_buffer_rsrc((void*)(sl + ((mt * 4 + kq) * 2 + nh) * 1024), 0, 4096, 0x00020000);
#pragma unroll
      for (int g = 0; g < 4; ++g) {
        f32x4 w = {acc[0][4 * g], acc[0][4 * g + 1], acc[0][4 * g + 2], acc[0][4 * g + 3]};
        __builtin_amdgcn_raw_buffer_store_b128(__builtin_bit_cast(u32x4, w), rs, g * 1024 + lane * 16, 0, 16);
      }
    }
    asm volatile("s_waitcnt vmcnt(0)" ::: "memory");
    __syncthreads();
    if (wid == 0 && lane < 4 && lane != kq)
      __hip_atomic_store(flagB + (tile * 4 + lane) * 4 + kq, tag, __ATOMIC_RELAXED, __HIP_MEMORY_SCOPE_AGENT);
    if (mt == kq) {
      wait_flags(flagB + (tile * 4 + kq) * 4 + (lane & 3), lane < 4 && lane != kq, tag);
      u32x4 pv[3][4];
#pragma unroll
      for (int o = 0; o < 3; ++o) {
        const int src = o + (o >= kq);
        const __amdgpu_buffer_rsrc_t rs = __builtin_amdgcn_make_buffer_rsrc((void*)(sl + ((kq * 4 + src) * 2 + nh) * 1024), 0, 4096, 0x00020000);
#pragma unroll
        for (int g = 0; g < 4; ++g) pv[o][g] = __builtin_amdgcn_raw_buffer_load_b128(rs, g * 1024 + lane * 16, 0, 16);
      }
      float r[16];
#pragma unroll
      for (int g = 0; g < 4; ++g) {
        const f32x4 a = __builtin_bit_cast(f32x4, pv[0][g]), c = __builtin_bit_cast(f32x4, pv[1][g]), d = __builtin_bit_cast(f32x4, pv[2][g]);
#pragma unroll
        for (int e = 0; e < 4; ++e) {
          const float own = acc[0][4 * g + e];
          const float t0 = kq == 0 ? own : a[e];
          const float t1 = kq == 0 ? a[e] : (kq == 1 ? own : c[e]);
          const float t2 = kq <= 1 ? c[e] : (kq == 2 ? own : d[e]);
          const float t3 = kq == 3 ? own : d[e];
          r[4 * g + e] = ((t0 + t1) + t2) + t3;
        }
      }
      char* dst = (char*)out + ((size_t)(tm * 128 + 32 * mt + l31) * DM + tn * 64 + nh * 32 + 16 * h) * 2;
      u32x4 w0 = {cvtpk(r[0], r[1]), cvtpk(r[2], r[3]), cvtpk(r[4], r[5]), cvtpk(r[6], r[7])};
      u32x4 w1 = {cvtpk(r[8], r[9]), cvtpk(r[10], r[11]), cvtpk(r[12], r[13]), cvtpk(r[14], r[15])};
      *(u32x4*)dst = w0;
      *(u32x4*)(dst + 16) = w1;
    }
  }
  STAMP(25);
  STAMP_FLUSH;
}

extern "C" void kernel_launch(void* const* d_in, const int* in_sizes, int n_in, void* d_out, int out_size, void* d_ws,
                              size_t ws_size, hipStream_t stream) {
  const __hip_bfloat16* x = (const __hip_bfloat16*)d_in[0];
  const __hip_bfloat16* kc = (const __hip_bfloat16*)d_in[1];
  const __hip_bfloat16* vc = (const __hip_bfloat16*)d_in[2];
  const __hip_bfloat16* wq = (const __hip_bfloat16*)d_in[3];
  const __hip_bfloat16* wo = (const __hip_bfloat16*)d_in[4];
  __hip_bfloat16* o_ws = (__hip_bfloat16*)d_ws;
  float* slab = (float*)((char*)d_ws + (size_t)8 * 1024 * 1024);
  unsigned long long* flags = (unsigned long long*)((char*)d_ws + (size_t)24 * 1024 * 1024);
  hipFuncSetAttribute((const void*)k_fused, hipFuncAttributeMaxDynamicSharedMemorySize, K1_LDS);
  hipLaunchKernelGGL(k_fused, dim3(256), dim3(512), K1_LDS, stream, x, kc, vc, wq, wo, o_ws, slab, flags, (__hip_bfloat16*)d_out);
}
```

```cpp
#include <hip/hip_runtime.h>
#include <hip/hip_bf16.h>
#include <stdint.h>

typedef __attribute__((ext_vector_type(8))) __bf16 bf16x8;
typedef __attribute__((ext_vector_type(4))) __bf16 bf16x4;
typedef __attribute__((ext_vector_type(2))) __bf16 bf16x2;
typedef __attribute__((ext_vector_type(16))) float f32x16;
typedef __attribute__((ext_vector_type(4))) float f32x4;
typedef __attribute__((ext_vector_type(2))) float f32x2;
typedef __attribute__((ext_vector_type(4))) unsigned u32x4;
#define LDSP __attribute__((address_space(3)))
#define DI __device__ __forceinline__

constexpr int NB = 8, SQ = 112, DM = 576, HQ = 32, HKV = 16, SK = 512, DH = 128;
constexpr int MROWS = NB * SQ;
constexpr int KO = HQ * DH;

DI unsigned cvtpk(float lo, float hi) {
  f32x2 v = {lo, hi};
  bf16x2 b = __builtin_convertvector(v, bf16x2);
  return __builtin_bit_cast(unsigned, b);
}
DI void glds16(const void* sbase, unsigned voff, unsigned lds_dst) {
  unsigned keep;
  asm volatile("s_mov_b32 %0, m0\n\ts_mov_b32 m0, %3\n\ts_nop 0\n\tglobal_load_lds_dwordx4 %1, %2\n\ts_mov_b32 m0, %0"
               : "=&s"(keep) : "v"(voff), "s"(sbase), "s"(lds_dst) : "memory");
}
DI void glds16nt(const void* sbase, unsigned voff, unsigned lds_dst) {
  unsigned keep;
  asm volatile("s_mov_b32 %0, m0\n\ts_mov_b32 m0, %3\n\ts_nop 0\n\tglobal_load_lds_dwordx4 %1, %2 nt\n\ts_mov_b32 m0, %0"
               : "=&s"(keep) : "v"(voff), "s"(sbase), "s"(lds_dst) : "memory");
}
DI void glds16sc1(const void* sbase, unsigned voff, unsigned lds_dst) {
  unsigned keep;
  asm volatile("s_mov_b32 %0, m0\n\ts_mov_b32 m0, %3\n\ts_nop 0\n\tglobal_load_lds_dwordx4 %1, %2 sc1\n\ts_mov_b32 m0, %0"
               : "=&s"(keep) : "v"(voff), "s"(sbase), "s"(lds_dst) : "memory");
}
template <int J> DI void glds16_if(const void* sbase, unsigned voff, unsigned lds_dst, int sel) {
  unsigned keep;
  asm volatile("s_cmp_lg_u32 %4, %5\n\ts_cbranch_scc1 .Lgl%=\n\ts_mov_b32 %0, m0\n\ts_mov_b32 m0, %3\n\ts_nop 0\n\tglobal_load_lds_dwordx4 %1, %2\n\ts_mov_b32 m0, %0\n.Lgl%=:"
               : "=&s"(keep) : "v"(voff), "s"(sbase), "s"(lds_dst), "s"(sel), "i"(J) : "memory", "scc");
}
template <int J> DI void glds16nt_if(const void* sbase, unsigned voff, unsigned lds_dst, int sel) {
  unsigned keep;
  asm volatile("s_cmp_lg_u32 %4, %5\n\ts_cbranch_scc1 .Lgl%=\n\ts_mov_b32 %0, m0\n\ts_mov_b32 m0, %3\n\ts_nop 0\n\tglobal_load_lds_dwordx4 %1, %2 nt\n\ts_mov_b32 m0, %0\n.Lgl%=:"
               : "=&s"(keep) : "v"(voff), "s"(sbase), "s"(lds_dst), "s"(sel), "i"(J) : "memory", "scc");
}
template <bool NT, int J> DI void gl_t(const void* sbase, unsigned voff, unsigned lds_dst, int sel) {
  if constexpr (J < 0) { if constexpr (NT) glds16nt(sbase, voff, lds_dst); else glds16(sbase, voff, lds_dst); }
  else { if constexpr (NT) glds16nt_if<J>(sbase, voff, lds_dst, sel); else glds16_if<J>(sbase, voff, lds_dst, sel); }
}
template <int V> struct IC { static constexpr int value = V; };
template <int I, int N, class F> DI void sfor(F&& f) { if constexpr (I < N) { f(IC<I>{}); sfor<I + 1, N>(f); } }
DI void gl_sel(bool nt, int j, const void* sbase, unsigned voff, unsigned lds_dst, int sel) {
  if (nt) {
    if (j < 0) glds16nt(sbase, voff, lds_dst);
    else if (j == 0) glds16nt_if<0>(sbase, voff, lds_dst, sel); else if (j == 1) glds16nt_if<1>(sbase, voff, lds_dst, sel);
    else if (j == 2) glds16nt_if<2>(sbase, voff, lds_dst, sel); else glds16nt_if<3>(sbase, voff, lds_dst, sel);
  } else {
    if (j < 0) glds16(sbase, voff, lds_dst);
    else if (j == 0) glds16_if<0>(sbase, voff, lds_dst, sel); else if (j == 1) glds16_if<1>(sbase, voff, lds_dst, sel);
    else if (j == 2) glds16_if<2>(sbase, voff, lds_dst, sel); else glds16_if<3>(sbase, voff, lds_dst, sel);
  }
}
DI void set_m0(unsigned base, unsigned imm) { asm volatile("s_add_u32 m0, %0, %1\n\ts_nop 0" ::"s"(base), "i"(imm) : "scc"); }
template <int OFF, int POL> DI void gld(const void* sbase, unsigned voff) {
  if constexpr (POL == 0) asm volatile("global_load_lds_dwordx4 %0, %1 offset:%2" ::"v"(voff), "s"(sbase), "i"(OFF) : "memory");
  else if constexpr (POL == 1) asm volatile("global_load_lds_dwordx4 %0, %1 offset:%2 nt" ::"v"(voff), "s"(sbase), "i"(OFF) : "memory");
  else asm volatile("global_load_lds_dwordx4 %0, %1 offset:%2 sc1" ::"v"(voff), "s"(sbase), "i"(OFF) : "memory");
}
template <int N> DI void wait_lgkm() { __builtin_amdgcn_s_waitcnt(0xC07F | (N << 8)); }
#define GL_KV glds16nt
#define GL_XW glds16
#define GL_WO glds16
#define GL_O glds16
template <int N> DI void wait_vm() { asm volatile("s_waitcnt vmcnt(%0)" ::"i"(N) : "memory"); }
DI void bar() { asm volatile("s_waitcnt lgkmcnt(0)\n\ts_barrier" ::: "memory"); }
DI bf16x8 lds_b128(unsigned a) { return *(const LDSP bf16x8*)(uintptr_t)a; }
DI bf16x8 lds_tr2(unsigned a0, unsigned a1) {
  bf16x4 lo = __builtin_amdgcn_ds_read_tr16_b64_v4bf16((LDSP bf16x4*)(uintptr_t)a0);
  bf16x4 hi = __builtin_amdgcn_ds_read_tr16_b64_v4bf16((LDSP bf16x4*)(uintptr_t)a1);
  return __builtin_shufflevector(lo, hi, 0, 1, 2, 3, 4, 5, 6, 7);
}
DI bf16x8 pack8(const f32x16& x, int s) {
  u32x4 w = {cvtpk(x[8 * s], x[8 * s + 1]), cvtpk(x[8 * s + 2], x[8 * s + 3]), cvtpk(x[8 * s + 4], x[8 * s + 5]),
             cvtpk(x[8 * s + 6], x[8 * s + 7])};
  return __builtin_bit_cast(bf16x8, w);
}
#define STAMP(i)
#define CSTAMP(i)
#define STAMP_DECL
#define STAMP_FLUSH
#define STAMP2(i)
#define CSTAMP2(i)
#define STAMP_DECL2
#define STAMP_FLUSH2
#define MFMA(a, b, c) __builtin_amdgcn_mfma_f32_32x32x16_bf16((a), (b), (c), 0, 0, 0)

constexpr unsigned SUB = 16384u;
constexpr unsigned RGB = 65536u;
constexpr unsigned MISC = 131072u;
#define STAG1 0
#define STAG2 0
#define WO4 ((wid & 1) + 2 * (wid >> 2))
#define PIPE_MU 1
#define PRIO 3
#define HEADSKIP 0
#define SUMDT 3
#define NT1 3
struct SmSched {
  int mu[129];
  int tm[128];
  int rs[128];
  constexpr SmSched() : mu{}, tm{}, rs{} {
    int n[128] = {};
    for (int m = 0; m < 128; ++m) { tm[m] = -1; rs[m] = -1; }
    for (int j = 0; j < 8; ++j) tm[8 * j + (j == 0 ? 14 : (j == NT1 - 1 || j == 7) ? 9 : 10)] = j;
    const int sw = 64 + 8 * NT1;
    for (int d = 0; d < 4; ++d) rs[sw - 2 + d] = d;
    {
      const int a = 8 * NT1 + 3, e = 64; int av = 0, acc = 0;
      for (int m = a; m < e; ++m) if (tm[m] < 0 && (m & 15) >= HEADSKIP) ++av;
      for (int m = a; m < e; ++m) if (tm[m] < 0 && (m & 15) >= HEADSKIP) { acc += 8 * NT1; while (acc >= av) { acc -= av; ++n[m]; } }
    }
    {
      const int a = 67, e = 123; int av = 0, acc = 0;
      for (int m = a; m < e; ++m) if (rs[m] < 0 && (m & 15) >= HEADSKIP) ++av;
      for (int m = a; m < e; ++m) if (rs[m] < 0 && (m & 15) >= HEADSKIP) { acc += 64 - 8 * NT1; while (acc >= av) { acc -= av; ++n[m]; } }
    }
    int k = 0;
    for (int m = 0; m < 128; ++m) { mu[m] = k; k += n[m]; }
    mu[128] = k;
  }
  constexpr bool ok() const {
    if (mu[128] != 64) return false;
    for (int m = 0; m < 128; ++m)
      for (int k = mu[m]; k < mu[m + 1]; ++k) if (m + 1 >= 64 + 4 * (k >> 2)) return false;
    return true;
  }
};
constexpr SmSched SM{};
static_assert(SM.ok(), "softmax schedule misses a deadline");
constexpr int K1_LDS = 160 * 1024;
#define SCHED() __builtin_amdgcn_sched_barrier(0)
#define NOPK(x) asm("" : "+v"(x))
DI void rawbar() { asm volatile("s_barrier" ::: "memory"); }

typedef __attribute__((address_space(1))) unsigned long long gu64;
extern "C" __device__ unsigned long long hw_dispatch_id() __asm("llvm.amdgcn.dispatch.id");
DI constexpr unsigned wo_slot(int t) {
  constexpr unsigned pb[8] = {128u, 144u, 16u, 80u, 0u, 64u, 32u, 48u};
  return pb[t >> 1] * 1024u + (unsigned)(t & 1) * 8192u;
}
DI void wait_flags(gu64* f, bool on, unsigned long long tag) {
  for (unsigned spins = 0; spins < (1u << 20); ++spins) {
    const unsigned long long v = on ? __hip_atomic_load(f, __ATOMIC_RELAXED, __HIP_MEMORY_SCOPE_AGENT) : tag;
    if (__all(v == tag)) break;
    __builtin_amdgcn_s_sleep(1);
  }
  asm volatile("" ::: "memory");
}

__global__ __launch_bounds__(512, 2) void k_fused(const __hip_bfloat16* __restrict__ x, const __hip_bfloat16* __restrict__ kc,
                                                   const __hip_bfloat16* __restrict__ vc, const __hip_bfloat16* __restrict__ wq,
                                                   const __hip_bfloat16* __restrict__ wo, __hip_bfloat16* o_ws,
                                                   float* slab, unsigned long long* flags,
                                                   __hip_bfloat16* __restrict__ out) {
  extern __shared__ __attribute__((aligned(16))) char lds[];
  const int tid = threadIdx.x, lane = tid & 63;
  const int wid = __builtin_amdgcn_readfirstlane(tid >> 6);
  const int rg = wid & 3, kh = wid >> 2;
  const int l31 = lane & 31, h = lane >> 5;
  const int bid = blockIdx.x;
  const int hq = 4 * (bid & 7) + ((bid >> 3) & 3), b = bid >> 5;
  const int hkv = hq >> 1;
  STAMP_DECL;

  unsigned vx[2], vw[2], vkv[4];
#pragma unroll
  for (int e = 0; e < 2; ++e) {
    const int i = 2 * wid + e;
    int row = 8 * i + (lane >> 3);
    const int c = (lane & 7) ^ ((row >> 1) & 7);
    row = row < SQ ? row : SQ - 1;
    vx[e] = (unsigned)(row * (DM * 2) + c * 16 - e * 1024 + 1024);
    const int rw = 4 * i + (lane >> 4);
    const int cw = (lane & 15) ^ (((rw & 3) << 2) | ((rw >> 2) & 3));
    vw[e] = (unsigned)(rw * (KO * 2) + cw * 16 - e * 1024 + 1024);
  }
#pragma unroll
  for (int e = 0; e < 4; ++e) {
    const int r = 4 * (4 * (wid & 3) + e) + (lane >> 4);
    const int c = (lane & 15) ^ (((r & 3) << 2) | ((r >> 2) & 3));
    vkv[e] = (unsigned)((r - 4 * e) * 256 + c * 16);
  }
  const char* xb = (const char*)x + (size_t)b * SQ * DM * 2 - 1024;
  const char* wb = (const char*)wq + (size_t)hq * DH * 2 - 1024;
  const unsigned dX = (unsigned)wid * 2048u, dK = (unsigned)(kh * RGB + rg * 4096), dW = (unsigned)wid * 1024u;
  const size_t kvo = ((size_t)(b * HKV + hkv) * SK + 256 * kh) * DH * 2;
  const char* kb = (const char*)kc + kvo;
  const char* vb = (const char*)vc + kvo;

  auto issue1 = [&](auto Tc, auto Ec) {
    constexpr int t = decltype(Tc)::value, e = decltype(Ec)::value;
    constexpr unsigned sb = (unsigned)(t & 3) * SUB;
    if constexpr (t < 9) {
      if constexpr (e == 0) set_m0(dX, sb);
      if constexpr (e == 2) set_m0(dX, RGB + sb);
      if constexpr (e < 2) gld<e * 1024, 0>(xb + t * 128, vx[e]);
      else                 gld<(e - 2) * 1024, 0>(wb + (size_t)t * 64 * KO * 2, vw[e - 2]);
    } else if constexpr (t < 17) {
      if constexpr (e == 0) set_m0(dK, sb);
      if constexpr (t < 13) gld<e * 1024, 1>(kb + (t - 9) * 16384, vkv[e]);
      else                  gld<e * 1024, 1>(vb + (t - 13) * 16384, vkv[e]);
    }
  };
  if (kh) __builtin_amdgcn_s_setprio(1);
  STAMP(0); CSTAMP(20);
  sfor<0, 3>([&](auto Tc) { sfor<0, 4>([&](auto Ec) { issue1(Tc, Ec); }); });

  const int q4 = (lane & 15) >> 2, p4 = lane & 3, g16 = (lane >> 4) & 1;
  unsigned ax[4];
#pragma unroll
  for (int ks = 0; ks < 4; ++ks) ax[ks] = (unsigned)((32 * rg + l31) * 128 + (((2 * ks + h) ^ ((l31 >> 1) & 7)) * 16));
  unsigned aw[2][2];
#pragma unroll
  for (int dl = 0; dl < 2; ++dl)
#pragma unroll
    for (int t = 0; t < 2; ++t) {
      const int dt = 2 * kh + dl;
      const int k = 8 * h + 4 * t + q4;
      const int c = (4 * dt + 2 * g16 + (p4 & 1)) ^ ((q4 << 2) | ((2 * h + t) & 3));
      aw[dl][t] = (unsigned)(RGB + k * 256 + c * 16 + (p4 >> 1) * 8);
    }
  unsigned ak[8];
#pragma unroll
  for (int ks = 0; ks < 8; ++ks)
    ak[ks] = (unsigned)(kh * RGB + l31 * 256 + (((2 * ks + h) ^ (8 * kh) ^ (((l31 & 3) << 2) | ((l31 >> 2) & 3))) * 16));
  unsigned av[4][2];
#pragma unroll
  for (int dt = 0; dt < 4; ++dt)
#pragma unroll
    for (int t = 0; t < 2; ++t) {
      const int key = 8 * t + 4 * h + q4;
      const int c = (4 * (dt ^ (2 * kh)) + 2 * (p4 & 1) + g16) ^ ((q4 << 2) | ((2 * t + h) & 3));
      av[dt][t] = (unsigned)(kh * RGB + key * 256 + c * 16 + (p4 >> 1) * 8);
    }

  bf16x8 f1[3][3];
  auto ld1 = [&](int G) {
    const unsigned sb = (unsigned)((G >> 2) & 3) * SUB; const int ks = G & 3, r = G % 3;
    f1[r][0] = lds_b128(sb + ax[ks]);
    f1[r][1] = lds_tr2(sb + aw[0][0] + ks * 4096, sb + aw[0][1] + ks * 4096);
    f1[r][2] = lds_tr2(sb + aw[1][0] + ks * 4096, sb + aw[1][1] + ks * 4096);
  };
  bf16x8 f2[3][4];
  auto ld2 = [&](int G) {
    const int kt = G < 4 ? (G & 1) : ((G >> 1) & 1), hf = G < 4 ? (G >> 1) : (G & 1);
    const unsigned sb = (unsigned)((9 + (G >> 2)) & 3) * SUB + kt * 8192; const int r = G % 3;
#pragma unroll
    for (int j = 0; j < 4; ++j) f2[r][j] = lds_b128(sb + ak[4 * hf + j]);
  };
  auto ld3 = [&](int G) {
    const unsigned sb = (unsigned)((13 + (G >> 2)) & 3) * SUB + ((G >> 1) & 1) * 8192 + (G & 1) * 4096; const int r = (G + 1) % 3;
#pragma unroll
    for (int dt = 0; dt < 4; ++dt) f2[r][dt] = lds_tr2(sb + av[dt][0], sb + av[dt][1]);
  };

  wait_vm<8>(); rawbar();
  ld1(0); ld1(1);
  SCHED();

  f32x16 qa[2] = {};
  sfor<0, 9>([&](auto Tc) {
    constexpr int t = decltype(Tc)::value;
    if constexpr (t < 8) wait_vm<4>();
    rawbar();
    STAMP(1 + t);
    sfor<0, 4>([&](auto Kc) {
      constexpr int ks = decltype(Kc)::value;
      constexpr int G = 4 * t + ks;
      if constexpr (G == 34) { wait_vm<6>(); rawbar(); }
      issue1(IC<t + 3>{}, Kc);
      wait_lgkm<5>();
      if constexpr (G + 2 < 36) ld1(G + 2); else ld2(G + 2 - 36);
      qa[0] = MFMA(f1[G % 3][1], f1[G % 3][0], qa[0]);
      qa[1] = MFMA(f1[G % 3][2], f1[G % 3][0], qa[1]);
      SCHED();
    });
  });
  bf16x8 qf[8];
  qf[0] = pack8(qa[0], 0); qf[1] = pack8(qa[0], 1); qf[2] = pack8(qa[1], 0); qf[3] = pack8(qa[1], 1);
  {
    const unsigned qw = MISC + wid * 4096 + lane * 16;
#pragma unroll
    for (int f = 0; f < 4; ++f) *(LDSP bf16x8*)(uintptr_t)(qw + f * 1024) = qf[f];
  }
  SCHED();
  CSTAMP(22);
  const float CS = 0.08838834764831845f * 1.4426950408889634f;
  const int wo4 = WO4;
  f32x16 s[8];
  unsigned pw[16][4];
  float m1 = 0.f, mx = 0.f, mb = 0.f, al = 0.f;
  const unsigned selw = ((lane & 15) == ((lane >> 4) & 1)) ? 0x3F803F80u : 0u;
  const u32x4 sel4 = {selw, selw, selw, selw};
  f32x4 lsum[2];
  float rm0 = -__builtin_inff(), rm1 = -__builtin_inff();

  const int kq = (bid & 7) >> 1;
  const int tile0 = (bid & 1) ? 32 + (bid >> 3) : (bid >> 3);
  const bool activeB = tile0 < 63;
  const int tile = activeB ? tile0 : 0;
  const int tm = tile / 9, tn = tile % 9;
  const char* wob = (const char*)wo + ((size_t)kq * 1024 * DM + tn * 64) * 2;
  unsigned vwo;
  {
    const int row = 8 * wid + (lane >> 3);
    const int c = (lane & 7) ^ (((row >> 1) & 1) << 2);
    vwo = (unsigned)(row * (DM * 2) + c * 16);
  }
  auto issueWr = [&](int t) { set_m0(dW, wo_slot(t)); gld<0, 0>(wob + (size_t)t * 64 * DM * 2, vwo); };

  f32x16 oa[4];
  float pe[2][2];
  auto vwork = [&](auto Mc) {
    constexpr int m = decltype(Mc)::value;
    if constexpr (SM.tm[m] >= 0) {
      constexpr int j = SM.tm[m];
#pragma unroll
      for (int r = 0; r < 16; r += 4) {
        rm0 = fmaxf(fmaxf(rm0, s[j][r]), s[j][r + 1]);
        rm1 = fmaxf(fmaxf(rm1, s[j][r + 2]), s[j][r + 3]);
      }
      if constexpr (j == NT1 - 1 || j == 7) {
        float m_ = fmaxf(rm0, rm1);
        auto rr = __builtin_amdgcn_permlane32_swap(__float_as_uint(m_), __float_as_uint(m_), false, false);
        m_ = fmaxf(__uint_as_float(rr[0]), __uint_as_float(rr[1]));
        if constexpr (j == 7) {
          mx = m_; mb = mx * CS;
          al = __builtin_amdgcn_exp2f((m1 - mx) * CS);
        } else { m1 = m_; mb = m1 * CS; }
      }
    }
    if constexpr (SM.rs[m] >= 0) {
      constexpr int d = SM.rs[m];
#pragma unroll
      for (int r = 0; r < 16; ++r) { float t_ = oa[d][r] * al; NOPK(t_); oa[d][r] = t_; }
    }
    if constexpr (m > 0) {
      sfor<SM.mu[m - 1], SM.mu[m]>([&](auto Kc) {
        constexpr int k = decltype(Kc)::value, f = k >> 2, q = k & 3;
        pw[f][q] = cvtpk(pe[k & 1][0], pe[k & 1][1]);
      });
    }
    sfor<SM.mu[m], SM.mu[m + 1]>([&](auto Kc) {
      constexpr int k = decltype(Kc)::value, f = k >> 2, q = k & 3;
      pe[k & 1][0] = __builtin_amdgcn_exp2f(fmaf(s[f >> 1][8 * (f & 1) + 2 * q], CS, -mb));
      pe[k & 1][1] = __builtin_amdgcn_exp2f(fmaf(s[f >> 1][8 * (f & 1) + 2 * q + 1], CS, -mb));
    });
  };
  sfor<0, 16>([&](auto Gc) {
    constexpr int G = decltype(Gc)::value, i = G >> 2, t = 9 + i;
    constexpr int kt = G < 4 ? (G & 1) : ((G >> 1) & 1), hf = G < 4 ? (G >> 1) : (G & 1);
    if constexpr ((G & 3) == 0) {
      if constexpr (i > 0) { wait_vm<4>(); rawbar(); }
      STAMP(1 + t);
    }
    if constexpr (G == 1) {
      bar();
      const unsigned qr = MISC + (wid ^ 4) * 4096 + lane * 16;
#pragma unroll
      for (int f = 0; f < 4; ++f) qf[4 + f] = lds_b128(qr + f * 1024);
      issue1(IC<12>{}, IC<0>{});
    }
    if constexpr (G == 2) { wait_vm<6>(); rawbar(); }
    if constexpr (G > 0) issue1(IC<t + 3>{}, IC<(G & 3)>{});
    if constexpr (G == 15) wait_lgkm<8>(); else if constexpr (G == 1) wait_lgkm<8>(); else wait_lgkm<4>();
    if constexpr (G + 2 < 16) ld2(G + 2); else ld3(G + 2 - 16);
    sfor<0, 4>([&](auto Jc) {
      constexpr int j = decltype(Jc)::value;
      if constexpr (hf == 0 && j == 0) s[2 * i + kt] = MFMA(f2[G % 3][0], qf[0], f32x16{});
      else s[2 * i + kt] = MFMA(f2[G % 3][j], qf[4 * hf + j], s[2 * i + kt]);
      vwork(IC<4 * G + j>{});
      SCHED();
    });
  });
  sfor<0, 16>([&](auto Gc) {
    constexpr int G = decltype(Gc)::value, i = G >> 2, t = 13 + i;
    if constexpr ((G & 3) == 0) {
      if constexpr (i < 3) { wait_vm<4>(); rawbar(); }
      STAMP(1 + t);
    }
    issue1(IC<t + 3>{}, IC<(G & 3)>{});
    if constexpr (i == 1 || i == 2) issueWr(4 * (i - 1) + (G & 3));
    if constexpr (G < 15) wait_lgkm<8>(); else wait_lgkm<0>();
    if constexpr (G + 2 < 16) ld3(G + 2);
    const u32x4 pwv = {pw[G][0], pw[G][1], pw[G][2], pw[G][3]};
    sfor<0, 4>([&](auto Jc) {
      constexpr int dt = decltype(Jc)::value;
      if constexpr (G == 0) oa[dt] = MFMA(f2[(G + 1) % 3][dt], __builtin_bit_cast(bf16x8, pwv), f32x16{});
      else oa[dt] = MFMA(f2[(G + 1) % 3][dt], __builtin_bit_cast(bf16x8, pwv), oa[dt]);
      if constexpr (dt == SUMDT) {
        constexpr int gp = G < 2 * NT1 ? 0 : 1;
        if constexpr (G == 0 || G == 2 * NT1)
          lsum[gp] = __builtin_amdgcn_mfma_f32_16x16x32_bf16(__builtin_bit_cast(bf16x8, sel4), __builtin_bit_cast(bf16x8, pwv), f32x4{}, 0, 0, 0);
        else
          lsum[gp] = __builtin_amdgcn_mfma_f32_16x16x32_bf16(__builtin_bit_cast(bf16x8, sel4), __builtin_bit_cast(bf16x8, pwv), lsum[gp], 0, 0, 0);
      }
      vwork(IC<64 + 4 * G + dt>{});
      SCHED();
    });
  });
  float ls;
  {
    const int src = (lane & 15) * 4;
    const float a0 = __int_as_float(__builtin_amdgcn_ds_bpermute(src, __float_as_int(lsum[0][0])));
    const float a1 = __int_as_float(__builtin_amdgcn_ds_bpermute(src, __float_as_int(lsum[0][1])));
    const float b0 = __int_as_float(__builtin_amdgcn_ds_bpermute(src, __float_as_int(lsum[1][0])));
    const float b1 = __int_as_float(__builtin_amdgcn_ds_bpermute(src, __float_as_int(lsum[1][1])));
    ls = (lane & 16) ? a1 * al + b1 : a0 * al + b0;
  }
  const unsigned long long tag = ((hw_dispatch_id() + 1ull) * 0x9E3779B97F4A7C15ull) ^ (unsigned long long)(uintptr_t)__builtin_amdgcn_dispatch_ptr();
  gu64* flagA = (gu64*)flags;
  gu64* flagB = (gu64*)flags + 512;
  bar();
  STAMP(18); CSTAMP(23);
  issueWr(8); issueWr(9); issueWr(10);
  {
    constexpr unsigned MLB = 72u * 1024u;
    const unsigned xw = (unsigned)(32768 + kh * RGB + rg * 8192) + lane * 16, xr = (unsigned)(32768 + (kh ^ 1) * RGB + rg * 8192) + lane * 16;
    auto put = [&](const f32x16& v, int dl) {
#pragma unroll
      for (int g = 0; g < 4; ++g) {
        f32x4 w = {v[4 * g], v[4 * g + 1], v[4 * g + 2], v[4 * g + 3]};
        *(LDSP f32x4*)(uintptr_t)(xw + (dl * 4 + g) * 1024) = w;
      }
    };
    put(oa[2], 0); put(oa[3], 1);
    f32x2 ml = {mx, ls};
    *(LDSP f32x2*)(uintptr_t)(MLB + wid * 512 + lane * 8) = ml;
    bar();
    STAMP2(1);
    const f32x2 pml = *(const LDSP f32x2*)(uintptr_t)(MLB + (wid ^ 4) * 512 + lane * 8);
    const float mt = fmaxf(mx, pml[0]);
    const float f0 = __builtin_amdgcn_exp2f((mx - mt) * CS), f1_ = __builtin_amdgcn_exp2f((pml[0] - mt) * CS);
    const float inv = 1.f / (f0 * ls + f1_ * pml[1]);
    const float g0 = f0 * inv, g1 = f1_ * inv;
    const int qrow = 32 * rg + l31, R = b * SQ + qrow;
    const __amdgpu_buffer_rsrc_t rs = __builtin_amdgcn_make_buffer_rsrc((char*)o_ws + (size_t)hq * MROWS * (DH * 2), 0, MROWS * DH * 2, 0x00020000);
    const int so = (R >> 5) * 8192 + (4 * kh + h) * 1024 + (R & 31) * 16;
    auto fin = [&](const f32x16& v, int dl) {
      float r[16];
#pragma unroll
      for (int g = 0; g < 4; ++g) {
        const f32x4 w = *(const LDSP f32x4*)(uintptr_t)(xr + (dl * 4 + g) * 1024);
#pragma unroll
        for (int e = 0; e < 4; ++e) r[4 * g + e] = v[4 * g + e] * g0 + w[e] * g1;
      }
      const u32x4 w0 = {cvtpk(r[0], r[1]), cvtpk(r[2], r[3]), cvtpk(r[4], r[5]), cvtpk(r[6], r[7])};
      const u32x4 w1 = {cvtpk(r[8], r[9]), cvtpk(r[10], r[11]), cvtpk(r[12], r[13]), cvtpk(r[14], r[15])};
      if (qrow < SQ) {
        __builtin_amdgcn_raw_buffer_store_b128(w0, rs, so + dl * 2048, 0, 16);
        __builtin_amdgcn_raw_buffer_store_b128(w1, rs, so + dl * 2048 + 512, 0, 16);
      }
    };
    fin(oa[0], 0); fin(oa[1], 1);
  }
  STAMP2(3);
  asm volatile("s_waitcnt vmcnt(0)" ::: "memory");
  STAMP2(4);
  __syncthreads();
  STAMP2(5);
  if (tid == 0) __hip_atomic_store(flagA + (b * 4 + (hq >> 3)) * 8 + (hq & 7), tag, __ATOMIC_RELAXED, __HIP_MEMORY_SCOPE_AGENT);
  __builtin_amdgcn_s_setprio(0);
  STAMP(19); CSTAMP(21);
  if (!activeB) { STAMP_FLUSH; return; }

  {
    const int mt = wid & 3, k2 = wid >> 2, nh = k2;
#pragma unroll
    for (int t = 11; t < 16; ++t) issueWr(t);
    const int q4 = (lane & 15) >> 2, p4 = lane & 3, g16 = (lane >> 4) & 1;
    unsigned aw[2][2];
#pragma unroll
    for (int n2 = 0; n2 < 2; ++n2)
#pragma unroll
      for (int t = 0; t < 2; ++t) {
        const int k = 8 * h + 4 * t + q4;
        const int c = (4 * n2 + 2 * (p4 & 1) + g16) ^ (((q4 >> 1) & 1) << 2);
        aw[n2][t] = (unsigned)(k2 * 8192 + k * 128 + c * 16 + (p4 >> 1) * 8);
      }
    const int T = 4 * tm + mt;
    const int b0 = (32 * T) / SQ, b1 = (32 * T + 31) / SQ;
    gu64* myf = flagA + (((lane & 8) ? b1 : b0) * 4 + kq) * 8 + (lane & 7);
    unsigned rdy = 0;
    auto need = [&](int sI) {
      if (!((rdy >> sI) & 1u)) {
        for (unsigned spins = 0; spins < (1u << 20); ++spins) {
          const unsigned long long v = lane < 16 ? __hip_atomic_load(myf, __ATOMIC_RELAXED, __HIP_MEMORY_SCOPE_AGENT) : tag;
          const unsigned m = (unsigned)__ballot(v == tag);
          rdy = m & (m >> 8) & 0xffu;
          if ((rdy >> sI) & 1u) break;
          __builtin_amdgcn_s_sleep(1);
        }
        asm volatile("" ::: "memory");
      }
    };
    const __amdgpu_buffer_rsrc_t ro = __builtin_amdgcn_make_buffer_rsrc((char*)o_ws + (size_t)kq * 8 * MROWS * (DH * 2), 0, 8 * MROWS * DH * 2, 0x00020000);
    u32x4 of[8][4];
    const int ovo = T * 8192 + k2 * 4096 + lane * 16;
    f32x16 acc[2] = {};
    bf16x8 fa[2][2][2];
    auto ldw = [&](int q) {
      const unsigned base = wo_slot(2 * (q >> 2)) + (q & 3) * 2048;
      fa[q & 1][0][0] = lds_tr2(base + aw[0][0], base + aw[0][1]);
      fa[q & 1][1][0] = lds_tr2(base + aw[1][0], base + aw[1][1]);
    };
    ldw(0);
#define OLAG 3
#pragma unroll
    for (int sI = 0; sI < 8 + OLAG; ++sI) {
      if (sI < 8) {
        need(sI);
        if (sI == 0) { STAMP(26); }
#pragma unroll
        for (int j = 0; j < 4; ++j) of[sI][j] = __builtin_amdgcn_raw_buffer_load_b128(ro, ovo + j * 1024, sI * MROWS * (DH * 2), 16);
        SCHED();
      }
      if (sI >= OLAG) {
#pragma unroll
        for (int j = 0; j < 4; ++j) {
          const int q = 4 * (sI - OLAG) + j;
          if (q == 19) rawbar();
          if (q == 0) { STAMP(27); }
          if (q + 1 < 32) ldw(q + 1);
          const bf16x8 ob = __builtin_bit_cast(bf16x8, of[q >> 2][q & 3]);
          acc[0] = MFMA(fa[q & 1][0][0], ob, acc[0]);
          acc[1] = MFMA(fa[q & 1][1][0], ob, acc[1]);
          SCHED();
        }
      }
    }
    STAMP(24);
    {
      const unsigned ew = 96u * 1024u + wid * 4096 + lane * 16, er = 96u * 1024u + (wid ^ 4) * 4096 + lane * 16;
      auto put = [&](const f32x16& v) {
#pragma unroll
        for (int g = 0; g < 4; ++g) {
          f32x4 w = {v[4 * g], v[4 * g + 1], v[4 * g + 2], v[4 * g + 3]};
          *(LDSP f32x4*)(uintptr_t)(ew + g * 1024) = w;
        }
      };
      if (k2 == 0) put(acc[1]); else put(acc[0]);
      bar();
      auto get = [&](const f32x16& own, bool first) {
        f32x16 o;
#pragma unroll
        for (int g = 0; g < 4; ++g) {
          const f32x4 w = *(const LDSP f32x4*)(uintptr_t)(er + g * 1024);
#pragma unroll
          for (int e = 0; e < 4; ++e) o[4 * g + e] = own[4 * g + e] + w[e];
        }
        return o;
      };
      if (k2 == 0) acc[0] = get(acc[0], true); else acc[0] = get(acc[1], false);
    }
    float* sl = slab + (size_t)tile * 4 * 4 * 2 * 1024;
    if (mt != kq) {
      const __amdgpu_buffer_rsrc_t rs = __builtin_amdgcn_make_buffer_rsrc((void*)(sl + ((mt * 4 + kq) * 2 + nh) * 1024), 0, 4096, 0x00020000);
#pragma unroll
      for (int g = 0; g < 4; ++g) {
        f32x4 w = {acc[0][4 * g], acc[0][4 * g + 1], acc[0][4 * g + 2], acc[0][4 * g + 3]};
        __builtin_amdgcn_raw_buffer_store_b128(__builtin_bit_cast(u32x4, w), rs, g * 1024 + lane * 16, 0, 16);
      }
    }
    asm volatile("s_waitcnt vmcnt(0)" ::: "memory");
    __syncthreads();
    if (wid == 0 && lane < 4 && lane != kq)
      __hip_atomic_store(flagB + (tile * 4 + lane) * 4 + kq, tag, __ATOMIC_RELAXED, __HIP_MEMORY_SCOPE_AGENT);
    if (mt == kq) {
      wait_flags(flagB + (tile * 4 + kq) * 4 + (lane & 3), lane < 4 && lane != kq, tag);
      u32x4 pv[3][4];
#pragma unroll
      for (int o = 0; o < 3; ++o) {
        const int src = o + (o >= kq);
        const __amdgpu_buffer_rsrc_t rs = __builtin_amdgcn_make_buffer_rsrc((void*)(sl + ((kq * 4 + src) * 2 + nh) * 1024), 0, 4096, 0x00020000);
#pragma unroll
        for (int g = 0; g < 4; ++g) pv[o][g] = __builtin_amdgcn_raw_buffer_load_b128(rs, g * 1024 + lane * 16, 0, 16);
      }
      float r[16];
#pragma unroll
      for (int g = 0; g < 4; ++g) {
        const f32x4 a = __builtin_bit_cast(f32x4, pv[0][g]), c = __builtin_bit_cast(f32x4, pv[1][g]), d = __builtin_bit_cast(f32x4, pv[2][g]);
#pragma unroll
        for (int e = 0; e < 4; ++e) {
          const float own = acc[0][4 * g + e];
          const float t0 = kq == 0 ? own : a[e];
          const float t1 = kq == 0 ? a[e] : (kq == 1 ? own : c[e]);
          const float t2 = kq <= 1 ? c[e] : (kq == 2 ? own : d[e]);
          const float t3 = kq == 3 ? own : d[e];
          r[4 * g + e] = ((t0 + t1) + t2) + t3;
        }
      }
      char* dst = (char*)out + ((size_t)(tm * 128 + 32 * mt + l31) * DM + tn * 64 + nh * 32 + 16 * h) * 2;
      u32x4 w0 = {cvtpk(r[0], r[1]), cvtpk(r[2], r[3]), cvtpk(r[4], r[5]), cvtpk(r[6], r[7])};
      u32x4 w1 = {cvtpk(r[8], r[9]), cvtpk(r[10], r[11]), cvtpk(r[12], r[13]), cvtpk(r[14], r[15])};
      *(u32x4*)dst = w0;
      *(u32x4*)(dst + 16) = w1;
    }
  }
  STAMP(25);
  STAMP_FLUSH;
}

extern "C" void kernel_launch(void* const* d_in, const int* in_sizes, int n_in, void* d_out, int out_size, void* d_ws,
                              size_t ws_size, hipStream_t stream) {
  const __hip_bfloat16* x = (const __hip_bfloat16*)d_in[0];
  const __hip_bfloat16* kc = (const __hip_bfloat16*)d_in[1];
  const __hip_bfloat16* vc = (const __hip_bfloat16*)d_in[2];
  const __hip_bfloat16* wq = (const __hip_bfloat16*)d_in[3];
  const __hip_bfloat16* wo = (const __hip_bfloat16*)d_in[4];
  __hip_bfloat16* o_ws = (__hip_bfloat16*)d_ws;
  float* slab = (float*)((char*)d_ws + (size_t)8 * 1024 * 1024);
  unsigned long long* flags = (unsigned long long*)((char*)d_ws + (size_t)24 * 1024 * 1024);
  hipFuncSetAttribute((const void*)k_fused, hipFuncAttributeMaxDynamicSharedMemorySize, K1_LDS);
  hipLaunchKernelGGL(k_fused, dim3(256), dim3(512), K1_LDS, stream, x, kc, vc, wq, wo, o_ws, slab, flags, (__hip_bfloat16*)d_out);
}
```
